```python
import torch

def _softmax(x, axis=-1):
    return torch.softmax(x, dim=axis)
def _gelu(x):
    return torch.nn.functional.gelu(x, approximate="tanh")
def _silu(x):
    return torch.nn.functional.silu(x)
def _sigmoid(x):
    return torch.sigmoid(x)
def _layernorm(x, axis=-1, eps=1e-5):
    m = x.mean(dim=axis, keepdim=True)
    v = x.var(dim=axis, keepdim=True, unbiased=False)
    return (x - m) / torch.sqrt(v + eps)
def _scatter_add(tgt, idx, src, axis=0):
    return tgt.clone().index_add_(axis, idx, src)
def _segment_sum(src, seg, n):
    out = torch.zeros((n,) + src.shape[1:], dtype=src.dtype)
    return out.index_add_(0, seg, src)
def _pad(x, pw):
    flat = tuple(v for pair in reversed(pw) for v in pair)
    return torch.nn.functional.pad(x, flat)

kernel_name = 'standalone__modern_conv_block_convnext_v2__vision'

def setup_inputs():

    _w = (lambda a: (lambda t: t.to(torch.bfloat16) if t.is_floating_point() else t)(torch.as_tensor(a)))
    H = W = 512
    C = 96
    K = 3
    return {
        "x": _w((lambda *s: torch.randn(*s, dtype=torch.bfloat16) * 0.05)(1, H, W, C)),
        "dw": _w((lambda *s: torch.randn(*s, dtype=torch.bfloat16) * 0.05)(K, K, C)),
        "w1": _w((lambda *s: torch.randn(*s, dtype=torch.bfloat16) * 0.05)(C, 4 * C)),
        "w2": _w((lambda *s: torch.randn(*s, dtype=torch.bfloat16) * 0.05)(4 * C, C)),
        "grn_gamma": _w((lambda *s: torch.randn(*s, dtype=torch.bfloat16) * 0.05)(4 * C)),
        "grn_beta": _w((lambda *s: torch.randn(*s, dtype=torch.bfloat16) * 0.05)(4 * C)),
    }

def reference(x, dw, w1, w2, grn_gamma, grn_beta):
    B, H, W, C = x.shape
    K = dw.shape[0]
    pad = K // 2
    xp = _pad(x, ((0,0),(pad,pad),(pad,pad),(0,0)))
    out = torch.zeros_like(x)
    for i in range(K):
        for j in range(K):
            out = out + xp[:, i:i+H, j:j+W, :] * dw[i, j][None, None, None, :]
    h = _layernorm(out, axis=-1)
    h = torch.einsum('bhwc,ce->bhwe', h, w1)
    h = _gelu(h)
    gx = torch.sqrt((h * h).sum(axis=(1, 2), keepdims=True) + 1e-6)
    nx = gx / (gx.mean(axis=-1, keepdims=True) + 1e-6)
    h = grn_gamma * (h * nx) + grn_beta + h
    h = torch.einsum('bhwe,ec->bhwc', h, w2)
    return x + h
```

```cpp
#include <hip/hip_runtime.h>
#include <hip/hip_bf16.h>
#include <stdint.h>

typedef __bf16 bf16x8 __attribute__((ext_vector_type(8)));
typedef __bf16 bf16x16 __attribute__((ext_vector_type(16)));
typedef _Float16 f16x8 __attribute__((ext_vector_type(8)));
typedef _Float16 f16x2 __attribute__((ext_vector_type(2)));
typedef float f32x16 __attribute__((ext_vector_type(16)));
typedef float f32x4 __attribute__((ext_vector_type(4)));
typedef float f32x2 __attribute__((ext_vector_type(2)));
typedef unsigned u32x4 __attribute__((ext_vector_type(4)));
typedef unsigned u32x2 __attribute__((ext_vector_type(2)));
typedef unsigned u32x8 __attribute__((ext_vector_type(8)));
#define DI __device__ __forceinline__
#define LDSP __attribute__((address_space(3)))

#define NT 10
constexpr int H = 512, W = 512, C = 96, E = 384;
constexpr int NBIN = 26;
constexpr int TABB = NBIN * 256;
constexpr float GA = -0.46866869f;
constexpr float GB = 4.91222954f;
constexpr int NSHARD = 16;

constexpr size_t WS_W1 = 0;
constexpr size_t WS_DW = 73728;
constexpr size_t WS_TAB = 82944;
constexpr size_t WS_PAR = 96256;
constexpr size_t WS_ACC = 96512;
constexpr size_t WS_W2 = 121088;
constexpr size_t WS_BIAS = 194816;
constexpr size_t WS_LN = 1 << 20;

constexpr int L_TABP = 0, L_SSQ = TABB, L_STG = 8192, L_TABN = 65536, L_MISC = 65536 + TABB, L_W1 = 73728, L_DW = 147456;
constexpr int STG_PL = 3360, STG_SZ = 4 * STG_PL;
constexpr int L_W2A = 8192, L_W2B = 147456;
constexpr int LDS_BYTES = 163840;

DI int krow(int s, int h, int j) { return 16 * s + 8 * (j >> 2) + 4 * h + (j & 3); }
DI int cmap(int T, int r) { return 32 * T + (r & 16) + ((r & 4) << 1) + ((r & 8) >> 1) + (r & 3); }
DI float bf2f(unsigned short u) { return __uint_as_float((unsigned)u << 16); }
DI unsigned short f2bf(float f) { __bf16 b = (__bf16)f; return __builtin_bit_cast(unsigned short, b); }
DI unsigned pkbf(float a, float b) { typedef __bf16 b2 __attribute__((ext_vector_type(2))); f32x2 v = {a, b}; return __builtin_bit_cast(unsigned, __builtin_convertvector(v, b2)); }
DI unsigned pkh(float a, float b) { f32x2 v = {a, b}; return __builtin_bit_cast(unsigned, __builtin_convertvector(v, f16x2)); }

__global__ __launch_bounds__(256) void k_prep(const unsigned short* __restrict__ dw, const unsigned short* __restrict__ w1,
                                              const unsigned short* __restrict__ w2, const unsigned short* __restrict__ beta, char* ws) {
  __shared__ unsigned red[8];
  const int tid = threadIdx.x, gt = blockIdx.x * 256 + tid, nthr = gridDim.x * 256;
  unsigned m = 0;
  for (int i = tid; i < C * E / 8; i += 256) {
    u32x4 v = ((const u32x4*)w1)[i];
    for (int j = 0; j < 4; ++j) { m = max(m, v[j] & 0x7fffu); m = max(m, (v[j] >> 16) & 0x7fffu); }
  }
  for (int o = 32; o; o >>= 1) m = max(m, (unsigned)__shfl_xor((int)m, o));
  if ((tid & 63) == 0) red[tid >> 6] = m;
  __syncthreads();
  m = max(max(red[0], red[1]), max(red[2], red[3]));
  int ex = (int)(m >> 7) - 127; if (ex < -100) ex = -100; if (ex > 20) ex = 20;
  const int lb = ex + 7;
  const int sh = 127 - NBIN + lb;
  const int lsig = 7 - lb;
  if (gt == 0) { ((int*)(ws + WS_PAR))[0] = lsig; }
  for (int i = gt; i < 72 * 64; i += nthr) {
    int f = i >> 6, l = i & 63, t = f / 6, s = f % 6, r = l & 31, h = l >> 5;
    bool tab = ((r & 3) + 4 * (r >> 3)) < NT;
    unsigned short v[8];
    for (int j = 0; j < 8; ++j) { float w = bf2f(w1[krow(s, h, j) * E + 32 * t + r]); if (tab) w = ldexpf(w, -sh); v[j] = f2bf(w); }
    u32x4 o; for (int j = 0; j < 4; ++j) o[j] = v[2 * j] | ((unsigned)v[2 * j + 1] << 16);
    ((u32x4*)(ws + WS_W1))[i] = o;
  }
  for (int i = gt; i < 72 * 64; i += nthr) {
    int f = i >> 6, l = i & 63, t = f / 6, u = (f % 6) / 3, T = f % 3, r = l & 31, h = l >> 5;
    u32x4 o;
    for (int j = 0; j < 4; ++j) {
      float a = bf2f(w2[(32 * t + krow(u, h, 2 * j)) * C + cmap(T, r)]), b = bf2f(w2[(32 * t + krow(u, h, 2 * j + 1)) * C + cmap(T, r)]);
      o[j] = pkh(ldexpf(a, -lsig), ldexpf(b, -lsig));
    }
    ((u32x4*)(ws + WS_W2))[i] = o;
  }
  for (int i = gt; i < 4 * 3 * 96; i += nthr) {
    int ch = i % 96, tap = i / 96;
    unsigned v = tap < 9 ? dw[tap * 96 + ch] : 0;
    u32x2 o; o[0] = (ch & 4) ? 0 : v; o[1] = (ch & 4) ? v : 0;
    ((u32x2*)(ws + WS_DW))[i] = o;
  }
  for (int i = gt; i < 2 * NBIN * 128; i += nthr) {
    int sg = i / (NBIN * 128), k = i % (NBIN * 128);
    float z = __uint_as_float(((unsigned)k << 16) | 0x8000u);
    float y = ldexpf(z, sh); if (sg) y = -y;
    float t = y * (y * y + GB);
    float g = y / (1.0f + exp2f(t));
    _Float16 gh = (_Float16)ldexpf(g, lsig);
    ((unsigned short*)(ws + WS_TAB))[i] = __builtin_bit_cast(unsigned short, gh);
  }
  for (int i = gt; i < NSHARD * E; i += nthr) ((float*)(ws + WS_ACC))[i] = 0.f;
  if (blockIdx.x == gridDim.x - 1 && tid < C) {
    float b = 0.f;
    for (int e = 0; e < E; ++e) b += bf2f(beta[e]) * bf2f(w2[e * C + tid]);
    ((float*)(ws + WS_BIAS))[tid] = b;
  }
}

DI void dma16(const void* g, LDSP char* l) { __builtin_amdgcn_global_load_lds((const __attribute__((address_space(1))) void*)g, (LDSP void*)l, 16, 0, 0); }
DI u32x4 ldsr128(LDSP char* p) { return *(LDSP u32x4*)p; }
DI u32x2 ldsr64(LDSP char* p) { return *(LDSP u32x2*)p; }
DI f32x16 mfb(u32x4 a, u32x4 b, f32x16 c) { return __builtin_amdgcn_mfma_f32_32x32x16_bf16(__builtin_bit_cast(bf16x8, a), __builtin_bit_cast(bf16x8, b), c, 0, 0, 0); }
DI f32x16 mfh(u32x4 a, u32x4 b, f32x16 c) { return __builtin_amdgcn_mfma_f32_32x32x16_f16(__builtin_bit_cast(f16x8, a), __builtin_bit_cast(f16x8, b), c, 0, 0, 0); }

DI void gelu_tile(const f32x16& hh, unsigned (&g)[8], LDSP char* lds, unsigned gb2, unsigned isig2) {
#pragma unroll
  for (int i = 0; i < NT / 2; ++i) {
    unsigned a0 = (__float_as_uint(hh[2 * i]) >> 16) << 1, a1 = (__float_as_uint(hh[2 * i + 1]) >> 16) << 1;
    unsigned lo = *(LDSP unsigned short*)(lds + L_TABP + a0), hi = *(LDSP unsigned short*)(lds + L_TABP + a1);
    g[i] = lo | (hi << 16);
  }
#pragma unroll
  for (int i = NT / 2; i < 8; ++i) {
    f16x2 y = __builtin_bit_cast(f16x2, pkh(hh[2 * i], hh[2 * i + 1]));
    f16x2 p = __builtin_elementwise_fma(y, y, __builtin_bit_cast(f16x2, gb2));
    f16x2 t = y * p;
    unsigned e, r;
    asm("v_exp_f16_e32 %0, %1\n\ts_nop 0\n\tv_exp_f16_sdwa %0, %1 dst_sel:WORD_1 dst_unused:UNUSED_PRESERVE src0_sel:WORD_1" : "=&v"(e) : "v"(t));
    f16x2 d = __builtin_elementwise_fma(__builtin_bit_cast(f16x2, e), __builtin_bit_cast(f16x2, isig2), __builtin_bit_cast(f16x2, isig2));
    asm("v_rcp_f16_e32 %0, %1\n\ts_nop 0\n\tv_rcp_f16_sdwa %0, %1 dst_sel:WORD_1 dst_unused:UNUSED_PRESERVE src0_sel:WORD_1" : "=&v"(r) : "v"(d));
    g[i] = __builtin_bit_cast(unsigned, y * __builtin_bit_cast(f16x2, r));
  }
}

DI void tile_of_block(int b, int& ty, int& tx) {
  int x = b & 7, i = b >> 3;
  ty = (x >> 1) * 4 + (i >> 3); tx = (x & 1) * 8 + (i & 7);
}

__global__ __launch_bounds__(512, 2) void k_pass1(const unsigned short* __restrict__ x, char* __restrict__ ws) {
  extern __shared__ __attribute__((aligned(16))) char smem_[];
  LDSP char* lds = (LDSP char*)smem_;
  const int tid = threadIdx.x, lane = tid & 63, wv = __builtin_amdgcn_readfirstlane(tid >> 6), p = lane & 31, h = lane >> 5;
  int ty, tx; tile_of_block(blockIdx.x, ty, tx);
  const int y0 = ty * 32 + wv * 4, x0 = tx * 32;
  __amdgpu_buffer_rsrc_t rx = __builtin_amdgcn_make_buffer_rsrc((void*)x, 0, H * W * C * 2, 0x00020000);
  u32x4 xr[3][13];
  {
    unsigned off[13];
#pragma unroll
    for (int i = 0; i < 13; ++i) {
      int q = i * 16 + (lane >> 2), row = q / 34, col = q - row * 34, gy = y0 - 1 + row, gx = x0 - 1 + col;
      bool ok = q < 204 && gy >= 0 && gy < H && gx >= 0 && gx < W;
      off[i] = ok ? (unsigned)((gy * W + gx) * 192 + (lane & 3) * 16) : 0x80000000u;
    }
#pragma unroll
    for (int s = 0; s < 3; ++s)
#pragma unroll
      for (int i = 0; i < 13; ++i) xr[s][i] = __builtin_amdgcn_raw_buffer_load_b128(rx, off[i] + s * 64, 0, 2);
  }
  for (int i = tid; i < 4608; i += 512) *(LDSP u32x4*)(lds + L_W1 + i * 16) = ((const u32x4*)(ws + WS_W1))[i];
  for (int i = tid; i < 576; i += 512) *(LDSP u32x4*)(lds + L_DW + i * 16) = ((const u32x4*)(ws + WS_DW))[i];
  for (int i = tid; i < 2 * TABB / 16; i += 512) { int o = i * 16; *(LDSP u32x4*)(lds + (o < TABB ? L_TABP + o : L_TABN + o - TABB)) = ((const u32x4*)(ws + WS_TAB))[i]; }
  if (tid < E) *(LDSP float*)(lds + L_SSQ + tid * 4) = 0.f;
  const int lsig = ((const int*)(ws + WS_PAR))[0];
  __syncthreads();
  if (wv >= 4) __builtin_amdgcn_s_barrier();

  f32x16 acc[12];
  {
    LDSP char* stg = lds + L_STG + (wv & 3) * STG_SZ;
    LDSP char* wr = stg + (lane & 3) * STG_PL + (lane >> 2) * 16;
    LDSP char* rd = stg + p * 16 + h * (3 * 34 * 16);
    const int orow = p >> 3, c = p & 7;
    LDSP char* wa[5][2];
#pragma unroll
    for (int n = 0; n < 5; ++n) {
      int m = n < 4 ? 2 * n + h : 8;
      int k0 = m / 3 - orow, k1 = k0 + 3, kx = m % 3;
      bool dead = (n == 4 && h == 0);
      k0 = (k0 >= 0 && !dead) ? k0 : 3; k1 = (k1 < 3 && !dead) ? k1 : 3;
      wa[n][0] = lds + L_DW + ((k0 * 3 + kx) * 96 + c) * 8; wa[n][1] = lds + L_DW + ((k1 * 3 + kx) * 96 + c) * 8;
    }
    const int idx = (c & 3) * 0x1111;
#pragma unroll
    for (int s = 0; s < 3; ++s) {
#pragma unroll
      for (int i = 0; i < 13; ++i) *(LDSP u32x4*)(wr + i * 256) = xr[s][i];
#pragma unroll
      for (int o4 = 0; o4 < 4; ++o4) {
        u32x4 b[9];
#pragma unroll
        for (int m = 0; m < 9; ++m) b[m] = ldsr128(rd + o4 * STG_PL + ((m / 3) * 34 + m % 3) * 16);
        f32x16 a = {};
#pragma unroll
        for (int n = 0; n < 5; ++n) {
          u32x2 a0 = ldsr64(wa[n][0] + (s * 4 + o4) * 64), a1 = ldsr64(wa[n][1] + (s * 4 + o4) * 64);
          u32x4 av = {a0[0], a0[1], a1[0], a1[1]};
          int m0 = n < 4 ? 2 * n : 7;
          u32x8 bv = __builtin_shufflevector(b[m0], b[m0 + 1], 0, 1, 2, 3, 4, 5, 6, 7);
          a = __builtin_amdgcn_smfmac_f32_32x32x32_bf16(__builtin_bit_cast(bf16x8, av), __builtin_bit_cast(bf16x16, bv), a, idx, 0, 0);
        }
        acc[s * 4 + o4] = a;
        __builtin_amdgcn_sched_barrier(0);
      }
    }
  }
  if (wv < 4) { asm volatile("s_waitcnt lgkmcnt(0)" ::: "memory"); __builtin_amdgcn_s_barrier(); }

  u32x4 ln[4][6];
#pragma unroll
  for (int g = 0; g < 4; ++g) {
    float s1 = 0.f, s2 = 0.f;
#pragma unroll
    for (int o = 0; o < 12; ++o)
#pragma unroll
      for (int i = 0; i < 4; ++i) { float v = acc[o][4 * g + i]; s1 += v; s2 = fmaf(v, v, s2); }
    s1 += __shfl_xor(s1, 32); s2 += __shfl_xor(s2, 32);
    float mean = s1 * (1.f / 96.f), var = fmaxf(s2 * (1.f / 96.f) - mean * mean, 0.f);
    float sc = GA * __builtin_amdgcn_rsqf(var + 1e-5f), of = -mean * sc;
#pragma unroll
    for (int s = 0; s < 6; ++s)
#pragma unroll
      for (int q = 0; q < 4; ++q) {
        const f32x16& a = acc[2 * s + (q >> 1)];
        ln[g][s][q] = pkbf(fmaf(a[4 * g + 2 * (q & 1)], sc, of), fmaf(a[4 * g + 2 * (q & 1) + 1], sc, of));
      }
    u32x4* dst = (u32x4*)(ws + WS_LN) + ((size_t)((blockIdx.x * 8 + wv) * 4 + g) * 6) * 64 + lane;
#pragma unroll
    for (int s = 0; s < 6; ++s) dst[s * 64] = ln[g][s];
  }

  const unsigned gb2 = pkh(GB, GB), isig2 = pkh(ldexpf(1.f, -lsig), ldexpf(1.f, -lsig));
  u32x4 sel;
  { int col = lane & 15, q = lane >> 4;
    for (int j2 = 0; j2 < 4; ++j2) { unsigned v = 0;
      for (int b = 0; b < 2; ++b) { int j = 2 * j2 + b; if (col == 8 * (j >> 2) + 4 * (q >> 1) + (j & 3)) v |= 0x3c00u << (16 * b); }
      sel[j2] = v; } }
  LDSP char* w1p = lds + L_W1 + lane * 16;
#pragma unroll 1
  for (int t = 0; t < 12; ++t) {
    u32x4 wf[6];
#pragma unroll
    for (int s = 0; s < 6; ++s) wf[s] = ldsr128(w1p + (t * 6 + s) * 1024);
    f16x2 S[8];
#pragma unroll
    for (int g = 0; g < 4; ++g) {
      f32x16 hh = {};
#pragma unroll
      for (int s = 0; s < 6; ++s) hh = mfb(wf[s], ln[g][s], hh);
      unsigned gg[8];
      gelu_tile(hh, gg, lds, gb2, isig2);
#pragma unroll
      for (int i = 0; i < 8; ++i) { f16x2 v = __builtin_bit_cast(f16x2, gg[i]); S[i] = g == 0 ? v * v : __builtin_elementwise_fma(v, v, S[i]); }
    }
#pragma unroll
    for (int u = 0; u < 2; ++u) {
      u32x4 sa; for (int i = 0; i < 4; ++i) sa[i] = __builtin_bit_cast(unsigned, S[4 * u + i]);
      f32x4 d = {};
      d = __builtin_amdgcn_mfma_f32_16x16x32_f16(__builtin_bit_cast(f16x8, sa), __builtin_bit_cast(f16x8, sel), d, 0, 0, 0);
      float v = (d[0] + d[1]) + (d[2] + d[3]);
      __hip_atomic_fetch_add((LDSP float*)(lds + L_SSQ + (32 * t + 16 * u + (lane & 15)) * 4), v, __ATOMIC_RELAXED, __HIP_MEMORY_SCOPE_WORKGROUP);
    }
  }
  __syncthreads();
  if (tid < E) atomicAdd((float*)(ws + WS_ACC) + (blockIdx.x % NSHARD) * E + tid, *(LDSP float*)(lds + L_SSQ + tid * 4));
}

__global__ __launch_bounds__(512, 2) void k_pass2(const unsigned short* __restrict__ x, const unsigned short* __restrict__ gamma,
                                                  const char* __restrict__ ws, unsigned short* __restrict__ out) {
  extern __shared__ __attribute__((aligned(16))) char smem_[];
  LDSP char* lds = (LDSP char*)smem_;
  const int tid = threadIdx.x, lane = tid & 63, wv = __builtin_amdgcn_readfirstlane(tid >> 6), p = lane & 31, h = lane >> 5;
  int ty, tx; tile_of_block(blockIdx.x, ty, tx);
  const int lsig = ((const int*)(ws + WS_PAR))[0];
  float gx = 0.f;
  if (tid < E) {
    float s = 0.f;
#pragma unroll
    for (int k = 0; k < NSHARD; ++k) s += ((const float*)(ws + WS_ACC))[k * E + tid];
    s = ldexpf(s, -2 * lsig) * (1.f / (GA * GA));
    gx = sqrtf(s + 1e-6f);
  }
  u32x4 w2r[9];
#pragma unroll
  for (int k = 0; k < 9; ++k) w2r[k] = ((const u32x4*)(ws + WS_W2))[(wv * 9 + k) * 64 + lane];
  for (int i = tid; i < 4608; i += 512) *(LDSP u32x4*)(lds + L_W1 + i * 16) = ((const u32x4*)(ws + WS_W1))[i];
  for (int i = tid; i < 2 * TABB / 16; i += 512) { int o = i * 16; *(LDSP u32x4*)(lds + (o < TABB ? L_TABP + o : L_TABN + o - TABB)) = ((const u32x4*)(ws + WS_TAB))[i]; }
  if (tid < C) { int T = tid >> 5, hh = (tid >> 4) & 1, r = tid & 15; *(LDSP float*)(lds + L_MISC + 768 + tid * 4) = ((const float*)(ws + WS_BIAS))[32 * T + 16 * (r >> 3) + 8 * hh + (r & 7)]; }
  { float v = gx; for (int o = 32; o; o >>= 1) v += __shfl_xor(v, o);
    if (lane == 0) *(LDSP float*)(lds + L_SSQ + wv * 4) = v; }
  __syncthreads();
  if (tid < E) {
    float tot = 0.f; for (int k = 0; k < 6; ++k) tot += *(LDSP float*)(lds + L_SSQ + k * 4);
    float nx = gx / (tot * (1.f / E) + 1e-6f);
    float s = fmaf(bf2f(gamma[tid]), nx, 1.f) * (1.f / GA);
    int tu = tid >> 4, r = tid & 15, hh = (r >> 2) & 1, j = ((r >> 3) << 2) | (r & 3);
    *(LDSP _Float16*)(lds + L_MISC + (tu * 16 + hh * 8 + j) * 2) = (_Float16)s;
  }
  __syncthreads();
#pragma unroll
  for (int k = 0; k < 9; ++k) {
    int f = wv * 9 + k;
    u32x4 sv = ldsr128(lds + L_MISC + (f / 3) * 32 + h * 16);
    u32x4 o = __builtin_bit_cast(u32x4, __builtin_bit_cast(f16x8, w2r[k]) * __builtin_bit_cast(f16x8, sv));
    *(LDSP u32x4*)(lds + (f < 56 ? L_W2A + f * 1024 : L_W2B + (f - 56) * 1024) + lane * 16) = o;
  }
  __syncthreads();

  const unsigned gb2 = pkh(GB, GB), isig2 = pkh(ldexpf(1.f, -lsig), ldexpf(1.f, -lsig));
  u32x4 idn[2];
#pragma unroll
  for (int gq = 0; gq < 2; ++gq) {
    int j = ((p >> 3) & 1) * 4 + (p & 3);
    bool on = (p >> 4) == gq && ((p >> 2) & 1) == h;
    for (int i = 0; i < 4; ++i) idn[gq][i] = (on && (j >> 1) == i) ? (0x3f80u << (16 * (j & 1))) : 0u;
  }
  LDSP char* w1p = lds + L_W1 + lane * 16;
#pragma unroll 1
  for (int pr = 0; pr < 2; ++pr) {
    u32x4 ln[2][6];
    f32x16 o[2][3];
    const size_t pix0 = (size_t)(ty * 32 + wv * 4 + pr * 2) * W + tx * 32 + p;
#pragma unroll
    for (int g = 0; g < 2; ++g) {
      const u32x4* src = (const u32x4*)(ws + WS_LN) + ((size_t)((blockIdx.x * 8 + wv) * 4 + pr * 2 + g) * 6) * 64 + lane;
#pragma unroll
      for (int s = 0; s < 6; ++s) ln[g][s] = src[s * 64];
      const u32x4* xs = (const u32x4*)(x + (pix0 + g * W) * C) + h;
#pragma unroll
      for (int T = 0; T < 3; ++T) {
        f32x16 b;
#pragma unroll
        for (int q = 0; q < 4; ++q) { f32x4 v = *(LDSP f32x4*)(lds + L_MISC + 768 + (T * 2 + h) * 64 + q * 16); for (int i = 0; i < 4; ++i) b[4 * q + i] = v[i]; }
        b = mfb(idn[0], xs[4 * T], b);
        o[g][T] = mfb(idn[1], xs[4 * T + 2], b);
      }
    }
#pragma unroll 1
    for (int t = 0; t < 12; ++t) {
      f32x16 hh[2] = {};
#pragma unroll
      for (int s = 0; s < 6; ++s) { u32x4 wf = ldsr128(w1p + (t * 6 + s) * 1024); hh[0] = mfb(wf, ln[0][s], hh[0]); hh[1] = mfb(wf, ln[1][s], hh[1]); }
      unsigned gg[2][8];
      gelu_tile(hh[0], gg[0], lds, gb2, isig2);
      gelu_tile(hh[1], gg[1], lds, gb2, isig2);
#pragma unroll
      for (int k = 0; k < 6; ++k) {
        int f = t * 6 + k, u = k / 3, T = k % 3;
        u32x4 wf = ldsr128(lds + (f < 56 ? L_W2A + f * 1024 : L_W2B + (f - 56) * 1024) + lane * 16);
#pragma unroll
        for (int g = 0; g < 2; ++g) { u32x4 gv = {gg[g][4 * u], gg[g][4 * u + 1], gg[g][4 * u + 2], gg[g][4 * u + 3]}; o[g][T] = mfh(wf, gv, o[g][T]); }
      }
    }
#pragma unroll
    for (int g = 0; g < 2; ++g) {
      u32x4* os = (u32x4*)(out + (pix0 + g * W) * C) + h;
#pragma unroll
      for (int T = 0; T < 3; ++T)
#pragma unroll
        for (int gq = 0; gq < 2; ++gq) {
          u32x4 v; for (int i = 0; i < 4; ++i) v[i] = pkbf(o[g][T][8 * gq + 2 * i], o[g][T][8 * gq + 2 * i + 1]);
          os[4 * T + 2 * gq] = v;
        }
    }
  }
}

extern "C" void kernel_launch(void* const* d_in, const int* in_sizes, int n_in, void* d_out, int out_size, void* d_ws, size_t ws_size, hipStream_t stream) {
  const unsigned short* x = (const unsigned short*)d_in[0];
  const unsigned short* dw = (const unsigned short*)d_in[1];
  const unsigned short* w1 = (const unsigned short*)d_in[2];
  const unsigned short* w2 = (const unsigned short*)d_in[3];
  const unsigned short* gamma = (const unsigned short*)d_in[4];
  const unsigned short* beta = (const unsigned short*)d_in[5];
  (void)hipFuncSetAttribute((const void*)k_pass1, hipFuncAttributeMaxDynamicSharedMemorySize, LDS_BYTES);
  (void)hipFuncSetAttribute((const void*)k_pass2, hipFuncAttributeMaxDynamicSharedMemorySize, LDS_BYTES);
  k_prep<<<32, 256, 0, stream>>>(dw, w1, w2, beta, (char*)d_ws);
  k_pass1<<<256, 512, LDS_BYTES, stream>>>(x, (char*)d_ws);
  k_pass2<<<256, 512, LDS_BYTES, stream>>>(x, gamma, (const char*)d_ws, (unsigned short*)d_out);
}
```

```cpp
#include <hip/hip_runtime.h>

__global__ __launch_bounds__(256, 1) void k_main(const void* x, void* out, void* ws, const void* w2, const void* gamma,
                                                 const void* w1, const void* dw, const void* beta) {
  unsigned t0 = threadIdx.x;
  asm volatile(
".macro GA r\n"
"v_lshlrev_b32_sdwa \\r, 1, \\r dst_sel:DWORD dst_unused:UNUSED_PAD src0_sel:DWORD src1_sel:WORD_1\n"
".endm\n"
".macro GL r\n"
"ds_read_u16 \\r, \\r\n"
".endm\n"
".macro GH r\n"
"ds_read_u16_d16_hi \\r, \\r\n"
".endm\n"
".macro EX r, w\n"
"v_exp_f16_sdwa \\r, \\r dst_sel:WORD_\\w\\() dst_unused:UNUSED_PRESERVE src0_sel:WORD_\\w\n"
".endm\n"
".macro RC r, w\n"
"v_rcp_f16_sdwa \\r, \\r dst_sel:WORD_\\w\\() dst_unused:UNUSED_PRESERVE src0_sel:WORD_\\w\n"
".endm\n"
".macro MB d, a, b\n"
"v_mfma_f32_32x32x16_bf16 \\d, \\a, \\b, \\d\n"
".endm\n"
".macro MZ d, a, b\n"
"v_mfma_f32_32x32x16_bf16 \\d, \\a, \\b, 0\n"
".endm\n"
".macro SB d, a, b\n"
"v_mfma_f32_16x16x32_bf16 \\d, \\a, \\b, \\d\n"
".endm\n"
".macro SZ d, a, b\n"
"v_mfma_f32_16x16x32_bf16 \\d, \\a, \\b, 0\n"
".endm\n"
".macro SH d, a, b\n"
"v_mfma_f32_16x16x32_f16 \\d, \\a, \\b, \\d\n"
".endm\n"
".macro NR d, p\n"
"v_pk_fma_f32 \\d, \\d, \\p, \\p op_sel:[0,0,1] op_sel_hi:[1,0,1] neg_lo:[0,0,1] neg_hi:[0,0,1]\n"
".endm\n"
".macro LD d, a, o\n"
"ds_read_b128 \\d, \\a offset:\\o\n"
".endm\n"
".macro WL n\n"
"s_waitcnt lgkmcnt(\\n)\n"
".endm\n"
".macro AW d, s\n"
"v_accvgpr_write_b32 \\d, \\s\n"
".endm\n"
".macro AR d, s\n"
"v_accvgpr_read_b32 \\d, \\s\n"
".endm\n"
".macro PF d, a, b, c\n"
"v_pk_fma_f16 \\d, \\a, \\b, \\c\n"
".endm\n"
".macro PM d, a, b\n"
"v_pk_mul_f16 \\d, \\a, \\b\n"
".endm\n"
".macro CB d, a, b\n"
"v_cvt_pk_bf16_f32 \\d, \\a, \\b\n"
".endm\n"
".macro CH d, a, b\n"
"v_cvt_pk_f16_f32 \\d, \\a, \\b\n"
".endm\n"
"s_load_dwordx16 s[4:19], %1, 0\n"
"s_getpc_b64 s[24:25]\n"
"Lcnx_beg:\n"
"v_lshlrev_b32 v1, 4, v0\n"
"s_lshr_b32 s28, %2, 3\n"
"s_lshl_b32 s28, s28, 12\n"
"s_mov_b32 s26, Lcnx_end-Lcnx_beg\n"
"s_mov_b32 s27, 0x20000\n"
"buffer_load_dword a255, v1, s[24:27], s28 offen\n"
"s_mov_b32 s20, %2\n"
".p2align 3\n"
"v_lshrrev_b32 v1, 6, v0\n"
"v_and_b32 v29, 63, v0\n"
"s_mov_b32 s54, 73728\n"
"v_readfirstlane_b32 s58, v1\n"
"s_mov_b32 s55, 0x20000\n"
"v_lshrrev_b32 v30, 2, v29\n"
"v_and_b32_e64 v31, 3, v29\n"
"v_mul_u32_u24 v7, 768, v30\n"
"v_lshl_add_u32 v7, v31, 4, v7\n"
"s_mul_i32 s70, s58, 192\n"
"s_mul_i32 s73, s58, 18432\n"
"s_add_u32 s73, s73, 73728\n"
"s_lshr_b32 s60, s20, 3\n"
"s_mul_i32 s87, s60, 43\n"
"s_lshr_b32 s87, s87, 8\n"
"s_mul_i32 s87, s87, 6\n"
"s_sub_u32 s60, s60, s87\n"
"s_movk_i32 s42, 1728\n"
"s_mov_b32 s43, 0x20000\n"
"v_lshlrev_b32 v5, 1, v29\n"
"s_mul_i32 s71, s58, 576\n"
"s_mov_b32 s38, 0x3000000\n"
"s_mov_b32 s39, 0x20000\n"
"s_mov_b32 s59, s20\n"
"s_bfe_u32 s64, s59, 0x20001\n"
"s_lshr_b32 s66, s59, 6\n"
"s_lshl2_add_u32 s64, s64, s66\n"
"s_and_b32 s65, s59, 1\n"
"s_bfe_u32 s66, s59, 0x30003\n"
"s_lshl3_add_u32 s65, s65, s66\n"
"s_bitcmp0_b32 s64, 0\n"
"s_cselect_b32 s66, 1572864, 0\n"
"s_lshl_b32 s23, s66, 1\n"
"s_sub_u32 s23, 1572864, s23\n"
"s_mul_i32 s64, s64, 3145728\n"
"s_add_u32 s64, s64, s66\n"
"s_mul_i32 s66, s65, 6144\n"
"s_add_u32 s64, s64, s66\n"
"s_sub_u32 s64, s64, 98304\n"
"v_mul_u32_u24 v22, 192, v30\n"
"v_lshl_add_u32 v22, v31, 4, v22\n"
"s_mul_i32 s67, s58, 9\n"
"s_lshr_b32 s74, s67, 1\n"
"s_and_b32 s75, s67, 1\n"
"s_mul_i32 s66, s74, 98304\n"
"s_add_u32 s66, s66, s64\n"
"s_mul_i32 s68, s75, 3072\n"
"s_add_u32 s62, s66, s68\n"
"s_mul_i32 s68, s75, 95232\n"
"s_add_u32 s63, s66, s68\n"
"s_add_u32 s63, s63, 3072\n"
"s_min_u32 s88, s62, s38\n"
"s_min_u32 s89, s63, s38\n"
"s_add_u32 s90, s62, 98304\n"
"s_add_u32 s91, s63, 98304\n"
"s_add_u32 s92, s62, 196608\n"
"s_add_u32 s93, s63, 196608\n"
"s_add_u32 s94, s62, 294912\n"
"s_add_u32 s95, s63, 294912\n"
"s_add_u32 s81, s62, 393216\n"
"WL 0\n"
"s_mov_b64 s[52:53], s[14:15]\n"
"s_mov_b64 s[40:41], s[16:17]\n"
"s_mov_b64 s[36:37], s[4:5]\n"
"s_lshl_b32 s86, s60, 10\n"
"s_add_u32 s86, s86, s73\n"
"s_mul_i32 s87, s60, 12288\n"
"s_add_u32 s87, s87, s70\n"
"s_add_u32 m0, s86, 0\n"
"s_nop 0\n"
"buffer_load_dwordx4 v7, s[52:55], s87 offen offset:0 lds\n"
"s_add_u32 m0, s86, 6080\n"
"s_nop 0\n"
"buffer_load_dwordx4 v7, s[52:55], s87 offen offset:64 lds\n"
"s_add_u32 m0, s86, 12160\n"
"s_nop 0\n"
"buffer_load_dwordx4 v7, s[52:55], s87 offen offset:128 lds\n"
"buffer_load_ushort v52, v5, s[40:43], s71 offen offset:0\n"
"buffer_load_ushort v53, v5, s[40:43], s71 offen offset:128\n"
"buffer_load_ushort v54, v5, s[40:43], s71 offen offset:256\n"
"buffer_load_ushort v55, v5, s[40:43], s71 offen offset:384\n"
"buffer_load_ushort v56, v5, s[40:43], s71 offen offset:512\n"
"s_add_u32 s60, s60, 1\n"
"s_cmp_eq_u32 s60, 6\n"
"s_cselect_b32 s60, 0, s60\n"
"s_lshl_b32 s86, s60, 10\n"
"s_add_u32 s86, s86, s73\n"
"s_mul_i32 s87, s60, 12288\n"
"s_add_u32 s87, s87, s70\n"
"s_add_u32 m0, s86, 0\n"
"s_nop 0\n"
"buffer_load_dwordx4 v7, s[52:55], s87 offen offset:0 lds\n"
"s_add_u32 m0, s86, 6080\n"
"s_nop 0\n"
"buffer_load_dwordx4 v7, s[52:55], s87 offen offset:64 lds\n"
"s_add_u32 m0, s86, 12160\n"
"s_nop 0\n"
"buffer_load_dwordx4 v7, s[52:55], s87 offen offset:128 lds\n"
"s_add_u32 s60, s60, 1\n"
"s_cmp_eq_u32 s60, 6\n"
"s_cselect_b32 s60, 0, s60\n"
"s_lshl_b32 s86, s60, 10\n"
"s_add_u32 s86, s86, s73\n"
"s_mul_i32 s87, s60, 12288\n"
"s_add_u32 s87, s87, s70\n"
"s_add_u32 m0, s86, 0\n"
"s_nop 0\n"
"buffer_load_dwordx4 v7, s[52:55], s87 offen offset:0 lds\n"
"v_add_u32 v33, s67, v30\n"
"v_lshrrev_b32 v32, 1, v33\n"
"v_and_b32 v33, 1, v33\n"
"s_mov_b32 s68, 98304\n"
"v_mul_u32_u24 v23, 6336, v33\n"
"v_mad_u32_u24 v23, v32, s68, v23\n"
"v_lshl_add_u32 v23, v31, 4, v23\n"
"s_sub_u32 s68, s64, 192\n"
"v_add_u32 v23, s68, v23\n"
"v_mul_u32_u24 v34, 15, v33\n"
"v_cmp_ne_u32 vcc, s65, v34\n"
"v_bfrev_b32 v35, 1\n"
"s_and_b32 vcc_hi, vcc_hi, 0xf\n"
"s_add_u32 m0, s86, 6080\n"
"s_nop 0\n"
"buffer_load_dwordx4 v7, s[52:55], s87 offen offset:64 lds\n"
"v_mul_u32_u24 v21, 34, v32\n"
"v_cndmask_b32 v23, v35, v23, vcc\n"
"v_mad_u32_u24 v21, v33, 33, v21\n"
"v_mov_b32 v34, 612\n"
"v_cmp_gt_u32 vcc, 36, v29\n"
"v_lshrrev_b32 v36, 1, v31\n"
"v_mul_u32_u24 v36, 19776, v36\n"
"v_cndmask_b32_e64 v21, v34, v21, vcc\n"
"v_add_u32 v36, 6144, v36\n"
"v_and_b32_e64 v37, 1, v31\n"
"v_lshl_add_u32 v21, v21, 5, v36\n"
"v_lshl_add_u32 v21, v37, 4, v21\n"
"s_add_u32 m0, s86, 12160\n"
"s_nop 0\n"
"buffer_load_dwordx4 v7, s[52:55], s87 offen offset:128 lds\n"
"v_add_u32 v38, 1, v30\n"
"v_lshl_add_u32 v36, v38, 5, v36\n"
"v_bfe_u32 v38, v38, 3, 1\n"
"v_xor_b32 v37, v37, v38\n"
"v_lshl_add_u32 v36, v37, 4, v36\n"
"s_mul_i32 s66, s74, 1088\n"
"s_mul_i32 s68, s75, 576\n"
"s_add_u32 s68, s68, s66\n"
"s_add_u32 s68, s68, 512\n"
"v_add_u32_e64 v20, s68, v36\n"
"s_lshl_b32 s68, s75, 9\n"
"s_add_u32 s68, s68, s66\n"
"s_add_u32 s60, s60, 1\n"
"s_cmp_eq_u32 s60, 6\n"
"s_cselect_b32 s60, 0, s60\n"
"s_lshl_b32 s86, s60, 10\n"
"s_add_u32 s86, s86, s73\n"
"s_mul_i32 s87, s60, 12288\n"
"s_add_u32 s87, s87, s70\n"
"s_add_u32 m0, s86, 0\n"
"s_nop 0\n"
"buffer_load_dwordx4 v7, s[52:55], s87 offen offset:0 lds\n"
"v_add_u32 v19, s68, v36\n"
"s_mul_i32 s61, s58, 393216\n"
"s_add_u32 s61, s61, s64\n"
"s_add_u32 s61, s61, 98304\n"
"s_add_u32 s31, s61, s23\n"
"s_mov_b32 s30, s59\n"
"s_mov_b64 s[48:49], s[8:9]\n"
"s_mov_b32 s50, 0x100000\n"
"s_mov_b32 s51, 0x20000\n"
"s_mov_b64 s[40:41], s[6:7]\n"
"s_mov_b32 s42, 0x3000000\n"
"s_lshl2_add_u32 s62, s59, s58\n"
"s_add_u32 m0, s86, 6080\n"
"s_nop 0\n"
"buffer_load_dwordx4 v7, s[52:55], s87 offen offset:64 lds\n"
"s_mul_i32 s63, s62, 51456\n"
"s_add_u32 s63, s63, 1048576\n"
"s_add_u32 s44, s48, s63\n"
"s_addc_u32 s45, s49, 0\n"
"s_mov_b32 s46, 49152\n"
"s_mov_b32 s47, 0x20000\n"
"v_lshlrev_b32_e64 v0, 4, v29\n"
"v_and_b32 v1, 0x270, v0\n"
"v_and_b32 v2, 0x80, v0\n"
"v_lshl_or_b32 v1, v2, 1, v1\n"
"v_and_b32 v2, 0x100, v0\n"
"v_lshrrev_b32 v2, 1, v2\n"
"s_add_u32 m0, s86, 12160\n"
"s_nop 0\n"
"buffer_load_dwordx4 v7, s[52:55], s87 offen offset:128 lds\n"
"v_add_u32 v1, 73728, v1\n"
"v_add_u32_e64 v1, v1, v2\n"
"v_add_u32 v2, 0x10000, v1\n"
"v_and_b32 v44, 31, v29\n"
"v_lshrrev_b32 v45, 5, v29\n"
"v_lshrrev_b32 v46, 3, v44\n"
"v_and_b32 v47, 7, v44\n"
"v_mov_b32_e64 v16, v29\n"
"s_mul_i32 s66, s58, 4352\n"
"s_add_u32 s66, s66, 6144\n"
"v_mul_u32_u24 v48, 3264, v45\n"
"v_add_u32 v48, s66, v48\n"
"v_add_u32 v49, 0, v44\n"
"s_add_u32 s60, s60, 1\n"
"s_cmp_eq_u32 s60, 6\n"
"s_cselect_b32 s60, 0, s60\n"
"s_lshl_b32 s86, s60, 10\n"
"s_add_u32 s86, s86, s73\n"
"s_mul_i32 s87, s60, 12288\n"
"s_add_u32 s87, s87, s70\n"
"s_add_u32 m0, s86, 0\n"
"s_nop 0\n"
"buffer_load_dwordx4 v7, s[52:55], s87 offen offset:0 lds\n"
"v_lshl_add_u32 v50, v49, 5, v48\n"
"v_and_b32_e64 v49, 8, v49\n"
"v_lshl_add_u32 v26, v49, 1, v50\n"
"v_xor_b32 v27, 16, v26\n"
"v_add_u32 v49, 1, v44\n"
"v_lshl_add_u32 v50, v49, 5, v48\n"
"v_and_b32_e64 v49, 8, v49\n"
"v_lshl_add_u32 v28, v49, 1, v50\n"
"v_xor_b32 v29, 16, v28\n"
"v_add_u32 v49, 2, v44\n"
"v_lshl_add_u32 v50, v49, 5, v48\n"
"v_and_b32 v49, 8, v49\n"
"s_add_u32 m0, s86, 6080\n"
"s_nop 0\n"
"buffer_load_dwordx4 v7, s[52:55], s87 offen offset:64 lds\n"
"v_lshl_add_u32 v30, v49, 1, v50\n"
"v_xor_b32 v31, 16, v30\n"
"s_lshl_b32 s68, s58, 10\n"
"s_add_u32 s28, s28, s68\n"
"v_and_b32 v41, 3, v47\n"
"v_mul_u32_u24 v41, 0x1111, v41\n"
"s_movk_i32 s66, 2368\n"
"v_cmp_eq_u32 s[68:69], 1, v45\n"
"v_lshlrev_b32 v48, 3, v47\n"
"v_add_u32 v48, 147456, v48\n"
"v_mov_b32_e64 v49, 0\n"
"v_mov_b32 v51, 0\n"
"s_add_u32 m0, s86, 12160\n"
"s_nop 0\n"
"buffer_load_dwordx4 v7, s[52:55], s87 offen offset:128 lds\n"
"v_cndmask_b32 v49, v49, v51, s[68:69]\n"
"v_mov_b32_e64 v50, 0\n"
"v_mov_b32 v51, 768\n"
"v_cndmask_b32 v50, v50, v51, s[68:69]\n"
"v_add_u32 v50, v50, v48\n"
"v_sub_u32 v49, v49, v46\n"
"v_min_u32_e64 v51, 3, v49\n"
"v_mad_u32_u24 v32, v51, s66, v50\n"
"v_add_u32 v49, 3, v49\n"
"v_min_u32 v51, 3, v49\n"
"v_mad_u32_u24 v33, v51, s66, v50\n"
"v_mov_b32 v49, 0\n"
"s_add_u32 s60, s60, 1\n"
"s_cmp_eq_u32 s60, 6\n"
"s_cselect_b32 s60, 0, s60\n"
"s_lshl_b32 s86, s60, 10\n"
"s_add_u32 s86, s86, s73\n"
"s_mul_i32 s87, s60, 12288\n"
"s_add_u32 s87, s87, s70\n"
"s_add_u32 m0, s86, 0\n"
"s_nop 0\n"
"buffer_load_dwordx4 v7, s[52:55], s87 offen offset:0 lds\n"
"v_mov_b32 v51, 1\n"
"v_cndmask_b32 v49, v49, v51, s[68:69]\n"
"v_mov_b32 v50, 1536\n"
"v_mov_b32_e64 v51, 0\n"
"v_cndmask_b32 v50, v50, v51, s[68:69]\n"
"v_add_u32 v50, v50, v48\n"
"v_sub_u32 v49, v49, v46\n"
"v_min_u32_e64 v51, 3, v49\n"
"v_mad_u32_u24 v34, v51, s66, v50\n"
"v_add_u32 v49, 3, v49\n"
"v_min_u32 v51, 3, v49\n"
"v_mad_u32_u24 v35, v51, s66, v50\n"
"s_add_u32 m0, s86, 6080\n"
"s_nop 0\n"
"buffer_load_dwordx4 v7, s[52:55], s87 offen offset:64 lds\n"
"v_mov_b32 v49, 1\n"
"v_mov_b32_e64 v51, 1\n"
"v_cndmask_b32 v49, v49, v51, s[68:69]\n"
"v_mov_b32 v50, 768\n"
"v_mov_b32 v51, 1536\n"
"v_cndmask_b32 v50, v50, v51, s[68:69]\n"
"v_add_u32 v50, v50, v48\n"
"v_sub_u32 v49, v49, v46\n"
"v_min_u32_e64 v51, 3, v49\n"
"v_mad_u32_u24 v36, v51, s66, v50\n"
"v_add_u32 v49, 3, v49\n"
"v_min_u32 v51, 3, v49\n"
"v_mad_u32_u24 v37, v51, s66, v50\n"
"s_add_u32 m0, s86, 12160\n"
"s_nop 0\n"
"buffer_load_dwordx4 v7, s[52:55], s87 offen offset:128 lds\n"
"v_mov_b32 v49, 2\n"
"v_mov_b32_e64 v51, 2\n"
"v_cndmask_b32 v49, v49, v51, s[68:69]\n"
"v_mov_b32_e64 v50, 0\n"
"v_mov_b32 v51, 768\n"
"v_cndmask_b32 v50, v50, v51, s[68:69]\n"
"v_add_u32 v50, v50, v48\n"
"v_sub_u32 v49, v49, v46\n"
"v_min_u32_e64 v51, 3, v49\n"
"v_mad_u32_u24 v38, v51, s66, v50\n"
"v_add_u32 v49, 3, v49\n"
"v_min_u32 v51, 3, v49\n"
"buffer_load_dwordx4 v[64:67], v22, s[36:39], s88 offen offset:0\n"
"v_mad_u32_u24 v39, v51, s66, v50\n"
"v_mul_u32_u24 v49, 3, v45\n"
"v_add_u32 v49, 2, v49\n"
"v_sub_u32_e64 v49, v49, v46\n"
"v_mul_u32_u24 v50, 1664, v49\n"
"v_add_u32 v50, 156928, v50\n"
"v_mov_b32 v51, 154752\n"
"v_cmp_gt_u32 vcc, 3, v49\n"
"s_nop 1\n"
"v_cndmask_b32_e64 v50, v51, v50, vcc\n"
"v_lshl_add_u32 v40, v47, 4, v50\n"
"AW a192,v26\n"
"AW a193,v27\n"
"buffer_load_dwordx4 v[68:71], v22, s[36:39], s89 offen offset:0\n"
"AW a194,v28\n"
"AW a195,v29\n"
"AW a196,v30\n"
"AW a197,v31\n"
"AW a198,v32\n"
"AW a199,v33\n"
"AW a200,v34\n"
"AW a201,v35\n"
"AW a202,v36\n"
"AW a203,v37\n"
"AW a204,v38\n"
"AW a205,v39\n"
"buffer_load_dwordx4 v[72:75], v22, s[36:39], s90 offen offset:0\n"
"AW a206,v40\n"
"AW a207,v41\n"
"s_mov_b32 s56, 0x44ea44ea\n"
"v_and_b32 v121, 4, v16\n"
"v_cmp_eq_u32 vcc, 0, v121\n"
"s_mul_i32 s68, s58, 2368\n"
"s_add_u32 s68, s68, 147456\n"
"v_lshlrev_b32 v123, 3, v16\n"
"v_add_u32 v123, s68, v123\n"
"s_waitcnt vmcnt(22)\n"
"v_cndmask_b32 v104, 0, v52, vcc\n"
"v_cndmask_b32 v105, v52, 0, vcc\n"
"s_waitcnt vmcnt(21)\n"
"v_cndmask_b32 v106, 0, v53, vcc\n"
"buffer_load_dwordx4 v[76:79], v22, s[36:39], s91 offen offset:0\n"
"v_cndmask_b32 v107, v53, 0, vcc\n"
"s_waitcnt vmcnt(21)\n"
"v_cndmask_b32 v108, 0, v54, vcc\n"
"v_cndmask_b32 v109, v54, 0, vcc\n"
"s_waitcnt vmcnt(20)\n"
"v_cndmask_b32 v110, 0, v55, vcc\n"
"v_cndmask_b32 v111, v55, 0, vcc\n"
"s_waitcnt vmcnt(19)\n"
"v_cndmask_b32 v112, 0, v56, vcc\n"
"v_cndmask_b32 v113, v56, 0, vcc\n"
"v_and_b32 v5, 1, v16\n"
"v_lshlrev_b32 v5, 4, v5\n"
"v_lshlrev_b32 v55, v5, v55\n"
"v_lshlrev_b32 v56, v5, v56\n"
"v_bfe_u32 v5, v16, 1, 2\n"
"s_mul_i32 s68, s58, 1664\n"
"buffer_load_dwordx4 v[80:83], v22, s[36:39], s92 offen offset:0\n"
"s_add_u32 s68, s68, 156928\n"
"v_add_u32 v122, s68, v0\n"
"v_cmp_eq_u32 vcc, 0, v5\n"
"s_nop 1\n"
"v_cndmask_b32 v116, 0, v55, vcc\n"
"v_cndmask_b32 v124, 0, v56, vcc\n"
"v_cmp_eq_u32 vcc, 1, v5\n"
"s_nop 1\n"
"v_cndmask_b32 v117, 0, v55, vcc\n"
"v_cndmask_b32 v125, 0, v56, vcc\n"
"v_cmp_eq_u32 vcc, 2, v5\n"
"s_nop 1\n"
"v_cndmask_b32 v118, 0, v55, vcc\n"
"v_cndmask_b32 v126, 0, v56, vcc\n"
"v_cmp_eq_u32 vcc, 3, v5\n"
"buffer_load_dwordx4 v[84:87], v22, s[36:39], s93 offen offset:0\n"
"s_nop 0\n"
"v_cndmask_b32 v119, 0, v55, vcc\n"
"v_cndmask_b32_e64 v127, 0, v56, vcc\n"
"ds_write_b64 v123, v[104:105] offset:0\n"
"ds_write_b64 v123, v[106:107] offset:512\n"
"ds_write_b64 v123, v[108:109] offset:1024\n"
"ds_write_b64 v123, v[110:111] offset:1536\n"
"ds_write_b128 v122, v[116:119]\n"
"s_mov_b32 exec_hi, 0\n"
"ds_write_b64 v123, v[112:113] offset:2048\n"
"ds_write_b128 v122, v[124:127] offset:1024\n"
"s_mov_b64 exec, -1\n"
"v_lshlrev_b32_e64 v6, 8, v45\n"
"v_bfe_u32 v5, v16, 2, 2\n"
"v_lshl_add_u32 v6, v5, 6, v6\n"
"v_bfe_u32 v5, v16, 4, 1\n"
"v_lshl_add_u32 v6, v5, 5, v6\n"
"v_and_b32_e64 v5, 3, v16\n"
"v_lshl_add_u32 v6, v5, 3, v6\n"
"v_add_u32_e64 v6, s73, v6\n"
"v_bfe_u32 v5, v44, 1, 1\n"
"v_lshl_add_u32 v5, v46, 1, v5\n"
"v_mov_b32 v57, 136\n"
"v_lshrrev_b32 v5, v5, v57\n"
"v_and_b32 v5, 1, v5\n"
"v_cmp_eq_u32 vcc, 1, v5\n"
"s_lshl_b32 s68, s58, 2\n"
"s_add_u32 s68, s68, 72832\n"
"v_mov_b32_e64 v57, s68\n"
"v_mov_b32 v58, 72832\n"
"s_lshl_b32 s68, s58, 6\n"
"v_add_u32 v60, s68, v16\n"
"v_lshlrev_b32_e64 v61, 2, v60\n"
"v_add_u32 v62, 65536, v61\n"
"v_lshlrev_b32 v60, 17, v60\n"
"v_or_b32 v60, 0x8000, v60\n"
"v_mov_b32 v59, 0x409d30fc\n"
"s_waitcnt vmcnt(6)\n"
"buffer_load_dwordx4 v[88:91], v22, s[36:39], s94 offen offset:0\n"
"ds_read_b64_tr_b16 v[184:185], v6 offset:0\n"
"ds_read_b64_tr_b16 v[186:187], v6 offset:512\n"
"ds_read_b64_tr_b16 v[188:189], v6 offset:1024\n"
"ds_read_b64_tr_b16 v[190:191], v6 offset:1536\n"
"ds_read_b64_tr_b16 v[192:193], v6 offset:2048\n"
"ds_read_b64_tr_b16 v[194:195], v6 offset:2560\n"
"ds_read_b64_tr_b16 v[196:197], v6 offset:3072\n"
"ds_read_b64_tr_b16 v[198:199], v6 offset:3584\n"
"ds_read_b64_tr_b16 v[200:201], v6 offset:4096\n"
"ds_read_b64_tr_b16 v[202:203], v6 offset:4608\n"
"ds_read_b64_tr_b16 v[204:205], v6 offset:5120\n"
"ds_read_b64_tr_b16 v[206:207], v6 offset:5632\n"
"ds_read_b64_tr_b16 v[208:209], v6 offset:6144\n"
"ds_read_b64_tr_b16 v[210:211], v6 offset:6656\n"
"ds_read_b64_tr_b16 v[212:213], v6 offset:7168\n"
"ds_read_b64_tr_b16 v[214:215], v6 offset:7680\n"
"ds_read_b64_tr_b16 v[216:217], v6 offset:8192\n"
"ds_read_b64_tr_b16 v[218:219], v6 offset:8704\n"
"ds_read_b64_tr_b16 v[220:221], v6 offset:9216\n"
"ds_read_b64_tr_b16 v[222:223], v6 offset:9728\n"
"ds_read_b64_tr_b16 v[224:225], v6 offset:10240\n"
"ds_read_b64_tr_b16 v[226:227], v6 offset:10752\n"
"ds_read_b64_tr_b16 v[228:229], v6 offset:11264\n"
"ds_read_b64_tr_b16 v[230:231], v6 offset:11776\n"
"ds_read_b64_tr_b16 v[232:233], v6 offset:12288\n"
"ds_read_b64_tr_b16 v[234:235], v6 offset:12800\n"
"ds_read_b64_tr_b16 v[236:237], v6 offset:13312\n"
"ds_read_b64_tr_b16 v[238:239], v6 offset:13824\n"
"ds_read_b64_tr_b16 v[240:241], v6 offset:14336\n"
"ds_read_b64_tr_b16 v[242:243], v6 offset:14848\n"
"ds_read_b64_tr_b16 v[244:245], v6 offset:15360\n"
"ds_read_b64_tr_b16 v[246:247], v6 offset:15872\n"
"ds_read_b64_tr_b16 v[248:249], v6 offset:16384\n"
"ds_read_b64_tr_b16 v[250:251], v6 offset:16896\n"
"ds_read_b64_tr_b16 v[252:253], v6 offset:17408\n"
"ds_read_b64_tr_b16 v[254:255], v6 offset:17920\n"
"WL 14\n"
"s_nop 0\n"
"v_dot2_f32_bf16 v116, v184, v184, 0\n"
"v_dot2_f32_bf16 v117, v185, v185, 0\n"
"v_dot2_f32_bf16 v116, v186, v186, v116\n"
"v_dot2_f32_bf16 v117, v187, v187, v117\n"
"v_dot2_f32_bf16 v116, v188, v188, v116\n"
"v_dot2_f32_bf16 v117, v189, v189, v117\n"
"v_dot2_f32_bf16 v116, v190, v190, v116\n"
"v_dot2_f32_bf16 v117, v191, v191, v117\n"
"v_dot2_f32_bf16 v116, v192, v192, v116\n"
"v_dot2_f32_bf16 v117, v193, v193, v117\n"
"v_dot2_f32_bf16 v116, v194, v194, v116\n"
"v_dot2_f32_bf16 v117, v195, v195, v117\n"
"v_dot2_f32_bf16 v116, v196, v196, v116\n"
"v_dot2_f32_bf16 v117, v197, v197, v117\n"
"v_dot2_f32_bf16 v116, v198, v198, v116\n"
"v_dot2_f32_bf16 v117, v199, v199, v117\n"
"v_dot2_f32_bf16 v116, v200, v200, v116\n"
"v_dot2_f32_bf16 v117, v201, v201, v117\n"
"v_dot2_f32_bf16 v116, v202, v202, v116\n"
"v_dot2_f32_bf16 v117, v203, v203, v117\n"
"v_dot2_f32_bf16 v116, v204, v204, v116\n"
"v_dot2_f32_bf16 v117, v205, v205, v117\n"
"v_dot2_f32_bf16 v116, v206, v206, v116\n"
"v_dot2_f32_bf16 v117, v207, v207, v117\n"
"v_dot2_f32_bf16 v118, v208, v208, 0\n"
"buffer_load_dwordx4 v[92:95], v22, s[36:39], s95 offen offset:0\n"
"v_dot2_f32_bf16 v119, v209, v209, 0\n"
"v_dot2_f32_bf16 v118, v210, v210, v118\n"
"v_dot2_f32_bf16 v119, v211, v211, v119\n"
"v_dot2_f32_bf16 v118, v212, v212, v118\n"
"v_dot2_f32_bf16 v119, v213, v213, v119\n"
"v_dot2_f32_bf16 v118, v214, v214, v118\n"
"v_dot2_f32_bf16 v119, v215, v215, v119\n"
"v_dot2_f32_bf16 v118, v216, v216, v118\n"
"v_dot2_f32_bf16 v119, v217, v217, v119\n"
"v_dot2_f32_bf16 v118, v218, v218, v118\n"
"v_dot2_f32_bf16 v119, v219, v219, v119\n"
"v_dot2_f32_bf16 v118, v220, v220, v118\n"
"v_dot2_f32_bf16 v119, v221, v221, v119\n"
"v_dot2_f32_bf16 v118, v222, v222, v118\n"
"v_dot2_f32_bf16 v119, v223, v223, v119\n"
"v_dot2_f32_bf16 v118, v224, v224, v118\n"
"v_dot2_f32_bf16 v119, v225, v225, v119\n"
"v_dot2_f32_bf16 v118, v226, v226, v118\n"
"v_dot2_f32_bf16 v119, v227, v227, v119\n"
"WL 0\n"
"s_nop 0\n"
"v_dot2_f32_bf16 v118, v228, v228, v118\n"
"v_dot2_f32_bf16 v119, v229, v229, v119\n"
"v_dot2_f32_bf16 v118, v230, v230, v118\n"
"v_dot2_f32_bf16 v119, v231, v231, v119\n"
"v_dot2_f32_bf16 v120, v232, v232, 0\n"
"v_dot2_f32_bf16 v121, v233, v233, 0\n"
"v_dot2_f32_bf16 v120, v234, v234, v120\n"
"v_dot2_f32_bf16 v121, v235, v235, v121\n"
"v_dot2_f32_bf16 v120, v236, v236, v120\n"
"v_dot2_f32_bf16 v121, v237, v237, v121\n"
"v_dot2_f32_bf16 v120, v238, v238, v120\n"
"v_dot2_f32_bf16 v121, v239, v239, v121\n"
"v_dot2_f32_bf16 v120, v240, v240, v120\n"
"v_dot2_f32_bf16 v121, v241, v241, v121\n"
"v_dot2_f32_bf16 v120, v242, v242, v120\n"
"v_dot2_f32_bf16 v121, v243, v243, v121\n"
"v_dot2_f32_bf16 v120, v244, v244, v120\n"
"v_dot2_f32_bf16 v121, v245, v245, v121\n"
"v_dot2_f32_bf16 v120, v246, v246, v120\n"
"v_dot2_f32_bf16 v121, v247, v247, v121\n"
"v_dot2_f32_bf16 v120, v248, v248, v120\n"
"v_dot2_f32_bf16 v121, v249, v249, v121\n"
"v_dot2_f32_bf16 v120, v250, v250, v120\n"
"v_dot2_f32_bf16 v121, v251, v251, v121\n"
"v_dot2_f32_bf16 v120, v252, v252, v120\n"
"v_dot2_f32_bf16 v121, v253, v253, v121\n"
"v_dot2_f32_bf16 v120, v254, v254, v120\n"
"v_dot2_f32_bf16 v121, v255, v255, v121\n"
"s_sub_u32 s70, s73, 73728\n"
"v_add_u32 v7, s70, v1\n"
"v_add_f32 v116, v116, v117\n"
"v_add_f32 v118, v118, v119\n"
"v_add_f32 v120, v120, v121\n"
"v_mov_b32 v117, v116\n"
"v_mov_b32 v119, v118\n"
"v_mov_b32 v121, v120\n"
"v_permlane32_swap_b32 v116, v117\n"
"v_permlane32_swap_b32 v118, v119\n"
"v_permlane32_swap_b32 v120, v121\n"
"v_add_f32 v116, v116, v117\n"
"v_add_f32 v118, v118, v119\n"
"v_add_f32_e64 v120, v120, v121\n"
"buffer_load_dwordx4 v[96:99], v22, s[36:39], s81 offen offset:0\n"
"v_max3_f32 v120, v116, v118, v120\n"
"s_nop 1\n"
"v_max_u32_dpp v120, v120, v120 row_ror:1 row_mask:0xf bank_mask:0xf\n"
"s_nop 1\n"
"v_max_u32_dpp v120, v120, v120 row_ror:2 row_mask:0xf bank_mask:0xf\n"
"s_nop 1\n"
"v_max_u32_dpp v120, v120, v120 row_ror:4 row_mask:0xf bank_mask:0xf\n"
"s_nop 1\n"
"v_max_u32_dpp v120, v120, v120 row_ror:8 row_mask:0xf bank_mask:0xf\n"
"v_mov_b32 v121, v120\n"
"s_nop 1\n"
"v_permlane16_swap_b32 v120, v121\n"
"v_max_u32 v120, v120, v121\n"
"ds_write_b32 v57, v120\n"
"WL 0\n"
"s_barrier\n"
"ds_read_b128 v[116:119], v58\n"
"WL 0\n"
"v_max_u32 v116, v116, v117\n"
"v_max3_u32 v116, v116, v118, v119\n"
"v_mul_f32 v116, 0x41ad999a, v116\n"
"s_nop 0\n"
"v_readfirstlane_b32 s68, v116\n"
"s_nop 0\n"
"s_lshr_b32 s68, s68, 23\n"
"s_add_u32 s68, s68, 3\n"
"s_lshr_b32 s68, s68, 1\n"
"s_min_u32 s68, s68, 77\n"
"s_add_u32 s69, s68, 39\n"
"s_sub_i32 s86, 69, s68\n"
"s_max_i32 s86, s86, -14\n"
"s_min_i32 s86, s86, 14\n"
"s_mul_i32 s70, s69, 0x00800080\n"
"v_mov_b32 v5, s70\n"
"s_sub_u32 s57, 15, s86\n"
"s_mul_i32 s57, s57, 0x04000400\n"
"v_cndmask_b32 v5, 0, v5, vcc\n"
"s_add_u32 s72, s86, 127\n"
"s_lshl_b32 s72, s72, 23\n"
"s_lshl_b32 s21, s69, 23\n"
"s_sub_u32 s21, 0xbeeff558, s21\n"
"v_add_u32 v184, v5, v184\n"
"v_add_u32 v185, v5, v185\n"
"v_add_u32 v186, v5, v186\n"
"v_add_u32 v187, v5, v187\n"
"v_add_u32_e64 v168, 0x0, v60\n"
"v_add_u32 v172, 0x10000, v60\n"
"v_add_u32 v176, 0x2000000, v60\n"
"v_add_u32 v180, 0x2010000, v60\n"
"v_ldexp_f32 v168, v168, s69\n"
"v_ldexp_f32 v172, v172, s69\n"
"v_ldexp_f32 v176, v176, s69\n"
"ds_write_b128 v7, v[184:187] offset:0\n"
"v_add_u32 v188, v5, v188\n"
"v_add_u32 v189, v5, v189\n"
"v_add_u32 v190, v5, v190\n"
"v_add_u32 v191, v5, v191\n"
"v_ldexp_f32 v180, v180, s69\n"
"v_fma_f32 v169, v168, v168, v59\n"
"v_fma_f32 v173, v172, v172, v59\n"
"v_fma_f32 v177, v176, v176, v59\n"
"v_fma_f32 v181, v180, v180, v59\n"
"v_mul_f32 v169, v169, v168\n"
"v_mul_f32 v173, v173, v172\n"
"ds_write_b128 v7, v[188:191] offset:1024\n"
"v_add_u32 v192, v5, v192\n"
"v_add_u32 v193, v5, v193\n"
"v_add_u32 v194, v5, v194\n"
"v_add_u32 v195, v5, v195\n"
"buffer_load_dwordx4 v[100:103], v23, s[36:39], 0 offen offset:0\n"
"v_mul_f32 v177, v177, v176\n"
"v_mul_f32 v181, v181, v180\n"
"v_exp_f32 v169, v169\n"
"v_exp_f32 v173, v173\n"
"v_exp_f32 v177, v177\n"
"v_exp_f32 v181, v181\n"
"v_mul_f32 v168, s72, v168\n"
"v_mul_f32 v172, s72, v172\n"
"ds_write_b128 v7, v[192:195] offset:2048\n"
"v_add_u32 v196, v5, v196\n"
"v_add_u32 v197, v5, v197\n"
"v_add_u32 v198, v5, v198\n"
"v_add_u32 v199, v5, v199\n"
"v_mul_f32 v176, s72, v176\n"
"v_mul_f32 v180, s72, v180\n"
"v_add_f32 v169, 1.0, v169\n"
"v_add_f32 v173, 1.0, v173\n"
"v_add_f32 v177, 1.0, v177\n"
"v_add_f32 v181, 1.0, v181\n"
"v_rcp_f32_e64 v169, v169\n"
"ds_write_b128 v7, v[196:199] offset:3072\n"
"v_add_u32 v200, v5, v200\n"
"v_add_u32 v201, v5, v201\n"
"v_add_u32 v202, v5, v202\n"
"v_add_u32 v203, v5, v203\n"
"v_rcp_f32 v173, v173\n"
"v_rcp_f32 v177, v177\n"
"v_rcp_f32_e64 v181, v181\n"
"v_fma_f32 v170, v168, v169, -v168\n"
"v_fma_f32 v174, v172, v173, -v172\n"
"v_fma_f32 v178, v176, v177, -v176\n"
"v_fma_f32 v182, v180, v181, -v180\n"
"ds_write_b128 v7, v[200:203] offset:4096\n"
"v_add_u32 v204, v5, v204\n"
"v_add_u32 v205, v5, v205\n"
"v_add_u32 v206, v5, v206\n"
"v_add_u32 v207, v5, v207\n"
"v_mul_f32 v169, v168, v169\n"
"v_mul_f32 v173, v172, v173\n"
"v_mul_f32 v177, v176, v177\n"
"v_mul_f32 v181, v180, v181\n"
"CH v168,v169,v173\n"
"CH v171,v170,v174\n"
"CH v176,v177,v181\n"
"CH v179,v178,v182\n"
"ds_write_b128 v7, v[204:207] offset:5120\n"
"ds_write_b32 v61, v168 offset:0\n"
"ds_write_b32 v62, v171 offset:0\n"
"ds_write_b32 v61, v176 offset:1024\n"
"ds_write_b32 v62, v179 offset:1024\n"
"v_add_u32 v208, v5, v208\n"
"v_add_u32 v209, v5, v209\n"
"v_add_u32 v210, v5, v210\n"
"v_add_u32 v211, v5, v211\n"
"v_add_u32 v168, 0x4000000, v60\n"
"v_add_u32 v172, 0x4010000, v60\n"
"v_add_u32 v176, 0x6000000, v60\n"
"v_add_u32 v180, 0x6010000, v60\n"
"v_ldexp_f32 v168, v168, s69\n"
"v_ldexp_f32 v172, v172, s69\n"
"v_ldexp_f32 v176, v176, s69\n"
"buffer_load_dwordx4 v[128:131], v22, s[36:39], s88 offen offset:64\n"
"ds_write_b128 v7, v[208:211] offset:6144\n"
"v_add_u32 v212, v5, v212\n"
"v_add_u32 v213, v5, v213\n"
"v_add_u32 v214, v5, v214\n"
"v_add_u32 v215, v5, v215\n"
"v_ldexp_f32 v180, v180, s69\n"
"v_fma_f32 v169, v168, v168, v59\n"
"v_fma_f32 v173, v172, v172, v59\n"
"v_fma_f32 v177, v176, v176, v59\n"
"v_fma_f32 v181, v180, v180, v59\n"
"v_mul_f32 v169, v169, v168\n"
"v_mul_f32 v173, v173, v172\n"
"ds_write_b128 v7, v[212:215] offset:7168\n"
"v_add_u32 v216, v5, v216\n"
"v_add_u32 v217, v5, v217\n"
"v_add_u32 v218, v5, v218\n"
"v_add_u32 v219, v5, v219\n"
"v_mul_f32 v177, v177, v176\n"
"v_mul_f32 v181, v181, v180\n"
"v_exp_f32 v169, v169\n"
"v_exp_f32 v173, v173\n"
"v_exp_f32 v177, v177\n"
"v_exp_f32 v181, v181\n"
"v_mul_f32 v168, s72, v168\n"
"v_mul_f32 v172, s72, v172\n"
"ds_write_b128 v7, v[216:219] offset:8192\n"
"v_add_u32 v220, v5, v220\n"
"v_add_u32 v221, v5, v221\n"
"v_add_u32 v222, v5, v222\n"
"v_add_u32 v223, v5, v223\n"
"v_mul_f32 v176, s72, v176\n"
"v_mul_f32 v180, s72, v180\n"
"v_add_f32 v169, 1.0, v169\n"
"v_add_f32 v173, 1.0, v173\n"
"v_add_f32 v177, 1.0, v177\n"
"v_add_f32 v181, 1.0, v181\n"
"v_rcp_f32_e64 v169, v169\n"
"ds_write_b128 v7, v[220:223] offset:9216\n"
"v_add_u32 v224, v5, v224\n"
"v_add_u32 v225, v5, v225\n"
"v_add_u32 v226, v5, v226\n"
"v_add_u32 v227, v5, v227\n"
"v_rcp_f32 v173, v173\n"
"v_rcp_f32 v177, v177\n"
"v_rcp_f32_e64 v181, v181\n"
"v_fma_f32 v170, v168, v169, -v168\n"
"v_fma_f32 v174, v172, v173, -v172\n"
"v_fma_f32 v178, v176, v177, -v176\n"
"v_fma_f32 v182, v180, v181, -v180\n"
"ds_write_b128 v7, v[224:227] offset:10240\n"
"v_add_u32 v228, v5, v228\n"
"v_add_u32 v229, v5, v229\n"
"v_add_u32 v230, v5, v230\n"
"v_add_u32 v231, v5, v231\n"
"v_mul_f32 v169, v168, v169\n"
"v_mul_f32 v173, v172, v173\n"
"v_mul_f32 v177, v176, v177\n"
"v_mul_f32 v181, v180, v181\n"
"CH v168,v169,v173\n"
"CH v171,v170,v174\n"
"CH v176,v177,v181\n"
"buffer_load_dwordx4 v[132:135], v22, s[36:39], s89 offen offset:64\n"
"CH v179,v178,v182\n"
"ds_write_b128 v7, v[228:231] offset:11264\n"
"ds_write_b32 v61, v168 offset:2048\n"
"ds_write_b32 v62, v171 offset:2048\n"
"ds_write_b32 v61, v176 offset:3072\n"
"ds_write_b32 v62, v179 offset:3072\n"
"v_add_u32 v232, v5, v232\n"
"v_add_u32 v233, v5, v233\n"
"v_add_u32 v234, v5, v234\n"
"v_add_u32 v235, v5, v235\n"
"v_add_u32 v168, 0x8000000, v60\n"
"v_add_u32 v172, 0x8010000, v60\n"
"v_add_u32 v176, 0xa000000, v60\n"
"v_add_u32 v180, 0xa010000, v60\n"
"v_ldexp_f32 v168, v168, s69\n"
"v_ldexp_f32 v172, v172, s69\n"
"v_ldexp_f32 v176, v176, s69\n"
"ds_write_b128 v7, v[232:235] offset:12288\n"
"v_add_u32 v236, v5, v236\n"
"v_add_u32 v237, v5, v237\n"
"v_add_u32 v238, v5, v238\n"
"v_add_u32 v239, v5, v239\n"
"v_ldexp_f32 v180, v180, s69\n"
"v_fma_f32 v169, v168, v168, v59\n"
"v_fma_f32 v173, v172, v172, v59\n"
"v_fma_f32 v177, v176, v176, v59\n"
"v_fma_f32 v181, v180, v180, v59\n"
"v_mul_f32 v169, v169, v168\n"
"v_mul_f32 v173, v173, v172\n"
"ds_write_b128 v7, v[236:239] offset:13312\n"
"v_lshrrev_b32 v104, 1, v16\n"
"v_and_b32 v105, 1, v16\n"
"s_mul_i32 s67, s58, 160\n"
"s_add_u32 s74, s67, 128\n"
"s_min_u32 s74, s74, 580\n"
"s_lshl_b32 s34, s67, 5\n"
"s_lshl_b32 s35, s74, 5\n"
"s_sub_u32 s75, s64, 192\n"
"s_add_u32 s75, s75, s23\n"
"s_lshl_b32 s62, s65, 5\n"
"s_sub_u32 s62, s62, 1\n"
"s_mov_b32 s68, 91776\n"
"s_movk_i32 s63, 512\n"
"v_bfrev_b32 v106, 1\n"
"s_add_u32 s66, s67, 0\n"
"v_add_u32_e64 v107, s66, v104\n"
"v_mul_u32_u24 v108, 1928, v107\n"
"v_lshrrev_b32_e64 v108, 16, v108\n"
"v_mul_u32_u24 v8, 192, v107\n"
"v_mad_u32_u24 v8, v108, s68, v8\n"
"v_mul_u32_u24 v108, 34, v108\n"
"v_sub_u32 v107, v107, v108\n"
"v_bfe_u32 v108, v107, 3, 1\n"
"v_xor_b32 v108, v108, v105\n"
"v_lshl_add_u32 v8, v108, 4, v8\n"
"v_add_u32 v107, s62, v107\n"
"v_cmp_gt_u32 vcc, s63, v107\n"
"v_add_u32 v8, s75, v8\n"
"s_nop 0\n"
"v_cndmask_b32 v8, v106, v8, vcc\n"
"s_add_u32 s66, s67, 32\n"
"v_add_u32 v107, s66, v104\n"
"buffer_load_dwordx4 v[136:139], v22, s[36:39], s90 offen offset:64\n"
"v_mul_u32_u24 v108, 1928, v107\n"
"v_lshrrev_b32_e64 v108, 16, v108\n"
"v_mul_u32_u24 v11, 192, v107\n"
"v_mad_u32_u24 v11, v108, s68, v11\n"
"v_mul_u32_u24 v108, 34, v108\n"
"v_sub_u32 v107, v107, v108\n"
"v_bfe_u32 v108, v107, 3, 1\n"
"v_xor_b32 v108, v108, v105\n"
"v_lshl_add_u32 v11, v108, 4, v11\n"
"v_add_u32 v107, s62, v107\n"
"v_cmp_gt_u32 vcc, s63, v107\n"
"v_add_u32 v11, s75, v11\n"
"s_nop 0\n"
"v_cndmask_b32 v11, v106, v11, vcc\n"
"s_add_u32 s66, s67, 64\n"
"v_add_u32 v107, s66, v104\n"
"v_mul_u32_u24 v108, 1928, v107\n"
"v_lshrrev_b32_e64 v108, 16, v108\n"
"v_mul_u32_u24 v12, 192, v107\n"
"v_mad_u32_u24 v12, v108, s68, v12\n"
"v_mul_u32_u24 v108, 34, v108\n"
"v_sub_u32 v107, v107, v108\n"
"v_bfe_u32 v108, v107, 3, 1\n"
"v_xor_b32 v108, v108, v105\n"
"v_lshl_add_u32 v12, v108, 4, v12\n"
"v_add_u32 v107, s62, v107\n"
"v_cmp_gt_u32 vcc, s63, v107\n"
"v_add_u32 v12, s75, v12\n"
"s_nop 0\n"
"v_cndmask_b32 v12, v106, v12, vcc\n"
"s_add_u32 s66, s67, 96\n"
"v_add_u32_e64 v107, s66, v104\n"
"v_mul_u32_u24 v108, 1928, v107\n"
"v_lshrrev_b32_e64 v108, 16, v108\n"
"v_mul_u32_u24 v13, 192, v107\n"
"v_mad_u32_u24 v13, v108, s68, v13\n"
"v_mul_u32_u24 v108, 34, v108\n"
"v_sub_u32 v107, v107, v108\n"
"v_bfe_u32 v108, v107, 3, 1\n"
"v_xor_b32 v108, v108, v105\n"
"v_lshl_add_u32 v13, v108, 4, v13\n"
"v_add_u32 v107, s62, v107\n"
"v_cmp_gt_u32 vcc, s63, v107\n"
"v_add_u32 v13, s75, v13\n"
"s_nop 0\n"
"v_cndmask_b32 v13, v106, v13, vcc\n"
"v_add_u32_e64 v107, s74, v104\n"
"v_mul_u32_u24 v108, 1928, v107\n"
"v_lshrrev_b32_e64 v108, 16, v108\n"
"v_mul_u32_u24 v15, 192, v107\n"
"v_mad_u32_u24 v15, v108, s68, v15\n"
"v_mul_u32_u24 v108, 34, v108\n"
"v_sub_u32 v107, v107, v108\n"
"v_bfe_u32 v108, v107, 3, 1\n"
"v_xor_b32 v108, v108, v105\n"
"v_lshl_add_u32 v15, v108, 4, v15\n"
"v_add_u32 v107, s62, v107\n"
"v_cmp_gt_u32 vcc, s63, v107\n"
"v_add_u32 v15, s75, v15\n"
"s_nop 0\n"
"v_cndmask_b32 v15, v106, v15, vcc\n"
"s_waitcnt vmcnt(12)\n"
"ds_write_b128 v19, v[64:67] offset:0\n"
"s_waitcnt vmcnt(11)\n"
"ds_write_b128 v20, v[68:71] offset:0\n"
"v_add_u32 v240, v5, v240\n"
"v_add_u32 v241, v5, v241\n"
"buffer_load_dwordx4 v[140:143], v22, s[36:39], s91 offen offset:64\n"
"v_add_u32 v242, v5, v242\n"
"v_add_u32 v243, v5, v243\n"
"v_mul_f32 v177, v177, v176\n"
"v_mul_f32 v181, v181, v180\n"
"v_exp_f32 v169, v169\n"
"v_exp_f32 v173, v173\n"
"v_exp_f32 v177, v177\n"
"v_exp_f32 v181, v181\n"
"v_mul_f32 v168, s72, v168\n"
"v_mul_f32 v172, s72, v172\n"
"ds_write_b128 v7, v[240:243] offset:14336\n"
"s_waitcnt vmcnt(11)\n"
"ds_write_b128 v19, v[72:75] offset:1088\n"
"s_waitcnt vmcnt(10)\n"
"ds_write_b128 v20, v[76:79] offset:1088\n"
"v_add_u32 v244, v5, v244\n"
"v_add_u32 v245, v5, v245\n"
"v_add_u32 v246, v5, v246\n"
"v_add_u32 v247, v5, v247\n"
"v_mul_f32 v176, s72, v176\n"
"v_mul_f32 v180, s72, v180\n"
"v_add_f32 v169, 1.0, v169\n"
"v_add_f32 v173, 1.0, v173\n"
"v_add_f32 v177, 1.0, v177\n"
"v_add_f32 v181, 1.0, v181\n"
"v_rcp_f32_e64 v169, v169\n"
"ds_write_b128 v7, v[244:247] offset:15360\n"
"s_waitcnt vmcnt(9)\n"
"ds_write_b128 v19, v[80:83] offset:2176\n"
"s_waitcnt vmcnt(8)\n"
"ds_write_b128 v20, v[84:87] offset:2176\n"
"v_add_u32 v248, v5, v248\n"
"v_add_u32 v249, v5, v249\n"
"v_add_u32 v250, v5, v250\n"
"v_add_u32 v251, v5, v251\n"
"v_rcp_f32 v173, v173\n"
"v_rcp_f32 v177, v177\n"
"v_rcp_f32_e64 v181, v181\n"
"v_fma_f32 v170, v168, v169, -v168\n"
"v_fma_f32 v174, v172, v173, -v172\n"
"v_fma_f32 v178, v176, v177, -v176\n"
"v_fma_f32 v182, v180, v181, -v180\n"
"ds_write_b128 v7, v[248:251] offset:16384\n"
"s_waitcnt vmcnt(7)\n"
"ds_write_b128 v19, v[88:91] offset:3264\n"
"s_waitcnt vmcnt(6)\n"
"ds_write_b128 v20, v[92:95] offset:3264\n"
"v_add_u32 v252, v5, v252\n"
"v_add_u32 v253, v5, v253\n"
"v_add_u32 v254, v5, v254\n"
"v_add_u32 v255, v5, v255\n"
"v_mul_f32 v169, v168, v169\n"
"v_mul_f32 v173, v172, v173\n"
"v_mul_f32 v177, v176, v177\n"
"v_mul_f32 v181, v180, v181\n"
"CH v168,v169,v173\n"
"CH v171,v170,v174\n"
"CH v176,v177,v181\n"
"CH v179,v178,v182\n"
"ds_write_b128 v7, v[252:255] offset:17408\n"
"ds_write_b32 v61, v168 offset:4096\n"
"ds_write_b32 v62, v171 offset:4096\n"
"ds_write_b32 v61, v176 offset:5120\n"
"ds_write_b32 v62, v179 offset:5120\n"
"s_waitcnt vmcnt(5)\n"
"ds_write_b128 v19, v[96:99] offset:4352\n"
"s_waitcnt vmcnt(4)\n"
"ds_write_b128 v21, v[100:103] offset:0\n"
"WL 0\n"
"s_barrier\n"
"LD a[0:3],v30,21952\n"
"LD a[8:11],v40,256\n"
"buffer_load_dwordx4 v[144:147], v22, s[36:39], s92 offen offset:64\n"
"LD a[12:15],v26,19776\n"
"LD a[16:19],v28,19776\n"
"ds_read_b64 a[20:21], v32 offset:128\n"
"ds_read_b64 a[22:23], v33 offset:128\n"
"LD a[24:27],v30,19776\n"
"LD a[28:31],v26,20864\n"
"ds_read_b64 a[32:33], v34 offset:128\n"
"ds_read_b64 a[34:35], v35 offset:128\n"
"buffer_load_dwordx4 v[148:151], v22, s[36:39], s93 offen offset:64\n"
"LD a[216:219],v28,20864\n"
"LD a[220:223],v30,20864\n"
"ds_read_b64 a[224:225], v36 offset:128\n"
"ds_read_b64 a[226:227], v37 offset:128\n"
"WL 12\n"
"s_nop 0\n"
"MZ v[96:111],a[8:11],a[0:3]\n"
"buffer_load_dwordx4 v[152:155], v22, s[36:39], s94 offen offset:64\n"
"LD a[228:231],v26,21952\n"
"LD a[232:235],v28,21952\n"
"ds_read_b64 a[236:237], v38 offset:128\n"
"ds_read_b64 a[238:239], v39 offset:128\n"
"WL 12\n"
"v_smfmac_f32_32x32x32_bf16 v[96:111], a[20:23], a[12:19], v41\n"
"LD a[240:243],v31,21952\n"
"LD a[248:251],v40,384\n"
"WL 10\n"
"v_smfmac_f32_32x32x32_bf16 v[96:111], a[32:35], a[24:31], v41\n"
"buffer_load_dwordx4 v[156:159], v22, s[36:39], s95 offen offset:64\n"
"LD a[0:3],v27,19776\n"
"LD a[4:7],v29,19776\n"
"ds_read_b64 a[8:9], v32 offset:192\n"
"ds_read_b64 a[10:11], v33 offset:192\n"
"WL 10\n"
"v_smfmac_f32_32x32x32_bf16 v[96:111], a[224:227], a[216:223], v41\n"
"LD a[12:15],v31,19776\n"
"LD a[16:19],v27,20864\n"
"ds_read_b64 a[20:21], v34 offset:192\n"
"ds_read_b64 a[22:23], v35 offset:192\n"
"WL 10\n"
"v_smfmac_f32_32x32x32_bf16 v[96:111], a[236:239], a[228:235], v41\n"
"buffer_load_dwordx4 v[160:163], v22, s[36:39], s81 offen offset:64\n"
"LD a[24:27],v29,20864\n"
"LD a[28:31],v31,20864\n"
"ds_read_b64 a[32:33], v36 offset:192\n"
"ds_read_b64 a[34:35], v37 offset:192\n"
"WL 12\n"
"MZ v[112:127],a[248:251],a[240:243]\n"
"LD a[216:219],v27,21952\n"
"LD a[220:223],v29,21952\n"
"ds_read_b64 a[224:225], v38 offset:192\n"
"ds_read_b64 a[226:227], v39 offset:192\n"
"s_nop 0\n"
"v_pk_mul_f32 v[50:51], v[96:97], v[96:97]\n"
"v_pk_mul_f32 v[54:55], v[100:101], v[100:101]\n"
"v_pk_mul_f32 v[58:59], v[104:105], v[104:105]\n"
"v_pk_mul_f32 v[62:63], v[108:109], v[108:109]\n"
"v_pk_add_f32 v[48:49], v[96:97], v[98:99]\n"
"v_pk_fma_f32 v[50:51], v[98:99], v[98:99], v[50:51]\n"
"v_pk_add_f32 v[52:53], v[100:101], v[102:103]\n"
"v_pk_fma_f32 v[54:55], v[102:103], v[102:103], v[54:55]\n"
"WL 12\n"
"v_smfmac_f32_32x32x32_bf16 v[112:127], a[8:11], a[0:7], v41\n"
"buffer_load_dwordx4 v[164:167], v23, s[36:39], 0 offen offset:64\n"
"LD a[228:231],v30,2176\n"
"LD a[236:239],v40,0\n"
"WL 10\n"
"v_smfmac_f32_32x32x32_bf16 v[112:127], a[20:23], a[12:19], v41\n"
"buffer_load_dwordx4 a[80:83], v22, s[36:39], s88 offen offset:128\n"
"LD a[240:243],v26,0\n"
"LD a[244:247],v28,0\n"
"ds_read_b64 a[248:249], v32 offset:0\n"
"ds_read_b64 a[250:251], v33 offset:0\n"
"v_pk_add_f32 v[56:57], v[104:105], v[106:107]\n"
"v_pk_fma_f32 v[58:59], v[106:107], v[106:107], v[58:59]\n"
"v_pk_add_f32 v[60:61], v[108:109], v[110:111]\n"
"v_pk_fma_f32 v[62:63], v[110:111], v[110:111], v[62:63]\n"
"WL 10\n"
"v_smfmac_f32_32x32x32_bf16 v[112:127], a[32:35], a[24:31], v41\n"
"LD a[0:3],v30,0\n"
"LD a[4:7],v26,1088\n"
"ds_read_b64 a[8:9], v34 offset:0\n"
"ds_read_b64 a[10:11], v35 offset:0\n"
"WL 10\n"
"v_smfmac_f32_32x32x32_bf16 v[112:127], a[224:227], a[216:223], v41\n"
"buffer_load_dwordx4 a[84:87], v22, s[36:39], s89 offen offset:128\n"
"LD a[12:15],v28,1088\n"
"LD a[16:19],v30,1088\n"
"ds_read_b64 a[20:21], v36 offset:0\n"
"ds_read_b64 a[22:23], v37 offset:0\n"
"s_barrier\n"
"WL 12\n"
"MZ v[64:79],a[236:239],a[228:231]\n"
"LD a[24:27],v26,2176\n"
"LD a[28:31],v28,2176\n"
"ds_read_b64 a[32:33], v38 offset:0\n"
"ds_read_b64 a[34:35], v39 offset:0\n"
"v_pk_add_f32 v[48:49], v[48:49], v[112:113]\n"
"v_pk_fma_f32 v[50:51], v[112:113], v[112:113], v[50:51]\n"
"v_pk_add_f32 v[52:53], v[52:53], v[116:117]\n"
"v_pk_fma_f32 v[54:55], v[116:117], v[116:117], v[54:55]\n"
"v_pk_add_f32 v[56:57], v[56:57], v[120:121]\n"
"v_pk_fma_f32 v[58:59], v[120:121], v[120:121], v[58:59]\n"
"v_pk_add_f32 v[60:61], v[60:61], v[124:125]\n"
"v_pk_fma_f32 v[62:63], v[124:125], v[124:125], v[62:63]\n"
"WL 12\n"
"v_smfmac_f32_32x32x32_bf16 v[64:79], a[248:251], a[240:247], v41\n"
"buffer_load_dwordx4 a[88:91], v22, s[36:39], s90 offen offset:128\n"
"LD a[216:219],v31,2176\n"
"LD a[224:227],v40,128\n"
"WL 10\n"
"v_smfmac_f32_32x32x32_bf16 v[64:79], a[8:11], a[0:7], v41\n"
"LD a[228:231],v27,0\n"
"LD a[232:235],v29,0\n"
"ds_read_b64 a[236:237], v32 offset:64\n"
"ds_read_b64 a[238:239], v33 offset:64\n"
"v_pk_add_f32 v[48:49], v[48:49], v[114:115]\n"
"v_pk_fma_f32 v[50:51], v[114:115], v[114:115], v[50:51]\n"
"v_pk_add_f32 v[52:53], v[52:53], v[118:119]\n"
"v_pk_fma_f32 v[54:55], v[118:119], v[118:119], v[54:55]\n"
"v_pk_add_f32 v[56:57], v[56:57], v[122:123]\n"
"v_pk_fma_f32 v[58:59], v[122:123], v[122:123], v[58:59]\n"
"v_pk_add_f32 v[60:61], v[60:61], v[126:127]\n"
"v_pk_fma_f32 v[62:63], v[126:127], v[126:127], v[62:63]\n"
"WL 9\n"
"s_nop 0\n"
"v_smfmac_f32_32x32x32_bf16 v[64:79], a[20:23], a[12:19], v41\n"
"buffer_load_dwordx4 a[92:95], v22, s[36:39], s91 offen offset:128\n"
"LD a[240:243],v31,0\n"
"LD a[244:247],v27,1088\n"
"ds_read_b64 a[248:249], v34 offset:64\n"
"ds_read_b64 a[250:251], v35 offset:64\n"
"WL 10\n"
"v_smfmac_f32_32x32x32_bf16 v[64:79], a[32:35], a[24:31], v41\n"
"LD a[0:3],v29,1088\n"
"LD a[4:7],v31,1088\n"
"ds_read_b64 a[8:9], v36 offset:64\n"
"ds_read_b64 a[10:11], v37 offset:64\n"
"WL 12\n"
"MZ v[80:95],a[224:227],a[216:219]\n"
"buffer_load_dwordx4 a[96:99], v22, s[36:39], s92 offen offset:128\n"
"LD a[12:15],v27,2176\n"
"LD a[16:19],v29,2176\n"
"ds_read_b64 a[20:21], v38 offset:64\n"
"ds_read_b64 a[22:23], v39 offset:64\n"
"WL 12\n"
"v_smfmac_f32_32x32x32_bf16 v[80:95], a[236:239], a[228:235], v41\n"
"WL 8\n"
"v_smfmac_f32_32x32x32_bf16 v[80:95], a[248:251], a[240:247], v41\n"
"WL 4\n"
"v_smfmac_f32_32x32x32_bf16 v[80:95], a[8:11], a[0:7], v41\n"
"WL 0\n"
"v_smfmac_f32_32x32x32_bf16 v[80:95], a[20:23], a[12:19], v41\n"
"v_pk_add_f32 v[48:49], v[48:49], v[64:65]\n"
"v_pk_fma_f32 v[50:51], v[64:65], v[64:65], v[50:51]\n"
"v_pk_add_f32 v[52:53], v[52:53], v[68:69]\n"
"v_pk_fma_f32 v[54:55], v[68:69], v[68:69], v[54:55]\n"
"v_pk_add_f32 v[56:57], v[56:57], v[72:73]\n"
"v_pk_fma_f32 v[58:59], v[72:73], v[72:73], v[58:59]\n"
"v_pk_add_f32 v[60:61], v[60:61], v[76:77]\n"
"v_pk_fma_f32 v[62:63], v[76:77], v[76:77], v[62:63]\n"
"v_pk_add_f32 v[48:49], v[48:49], v[66:67]\n"
"v_pk_fma_f32 v[50:51], v[66:67], v[66:67], v[50:51]\n"
"v_pk_add_f32 v[52:53], v[52:53], v[70:71]\n"
"v_pk_fma_f32 v[54:55], v[70:71], v[70:71], v[54:55]\n"
"v_pk_add_f32 v[56:57], v[56:57], v[74:75]\n"
"v_pk_fma_f32 v[58:59], v[74:75], v[74:75], v[58:59]\n"
"v_pk_add_f32 v[60:61], v[60:61], v[78:79]\n"
"v_pk_fma_f32 v[62:63], v[78:79], v[78:79], v[62:63]\n"
"v_pk_add_f32 v[48:49], v[48:49], v[80:81]\n"
"v_pk_fma_f32 v[50:51], v[80:81], v[80:81], v[50:51]\n"
"v_pk_add_f32 v[52:53], v[52:53], v[84:85]\n"
"v_pk_fma_f32 v[54:55], v[84:85], v[84:85], v[54:55]\n"
"v_pk_add_f32 v[56:57], v[56:57], v[88:89]\n"
"v_pk_fma_f32 v[58:59], v[88:89], v[88:89], v[58:59]\n"
"v_pk_add_f32 v[60:61], v[60:61], v[92:93]\n"
"v_pk_fma_f32 v[62:63], v[92:93], v[92:93], v[62:63]\n"
"v_pk_add_f32 v[48:49], v[48:49], v[82:83]\n"
"v_pk_fma_f32 v[50:51], v[82:83], v[82:83], v[50:51]\n"
"v_pk_add_f32 v[52:53], v[52:53], v[86:87]\n"
"v_pk_fma_f32 v[54:55], v[86:87], v[86:87], v[54:55]\n"
"v_pk_add_f32 v[56:57], v[56:57], v[90:91]\n"
"v_pk_fma_f32 v[58:59], v[90:91], v[90:91], v[58:59]\n"
"v_pk_add_f32 v[60:61], v[60:61], v[94:95]\n"
"v_pk_fma_f32 v[62:63], v[94:95], v[94:95], v[62:63]\n"
"s_waitcnt vmcnt(14)\n"
"ds_write_b128 v19, v[128:131] offset:19776\n"
"s_waitcnt vmcnt(13)\n"
"ds_write_b128 v20, v[132:135] offset:19776\n"
"s_waitcnt vmcnt(12)\n"
"ds_write_b128 v19, v[136:139] offset:20864\n"
"s_waitcnt vmcnt(11)\n"
"ds_write_b128 v20, v[140:143] offset:20864\n"
"s_waitcnt vmcnt(10)\n"
"ds_write_b128 v19, v[144:147] offset:21952\n"
"s_waitcnt vmcnt(9)\n"
"ds_write_b128 v20, v[148:151] offset:21952\n"
"s_waitcnt vmcnt(8)\n"
"ds_write_b128 v19, v[152:155] offset:23040\n"
"s_waitcnt vmcnt(7)\n"
"ds_write_b128 v20, v[156:159] offset:23040\n"
"s_waitcnt vmcnt(6)\n"
"ds_write_b128 v19, v[160:163] offset:24128\n"
"s_waitcnt vmcnt(5)\n"
"ds_write_b128 v21, v[164:167] offset:19776\n"
"WL 0\n"
"s_barrier\n"
"buffer_load_dwordx4 a[100:103], v22, s[36:39], s93 offen offset:128\n"
"LD a[24:27],v30,21952\n"
"LD a[32:35],v40,512\n"
"LD a[216:219],v26,19776\n"
"LD a[220:223],v28,19776\n"
"ds_read_b64 a[224:225], v32 offset:256\n"
"ds_read_b64 a[226:227], v33 offset:256\n"
"buffer_load_dwordx4 a[104:107], v22, s[36:39], s94 offen offset:128\n"
"LD a[228:231],v30,19776\n"
"LD a[232:235],v26,20864\n"
"ds_read_b64 a[236:237], v34 offset:256\n"
"ds_read_b64 a[238:239], v35 offset:256\n"
"LD a[240:243],v28,20864\n"
"LD a[244:247],v30,20864\n"
"ds_read_b64 a[248:249], v36 offset:256\n"
"ds_read_b64 a[250:251], v37 offset:256\n"
"WL 12\n"
"MZ v[128:143],a[32:35],a[24:27]\n"
"buffer_load_dwordx4 a[108:111], v22, s[36:39], s95 offen offset:128\n"
"LD a[0:3],v26,21952\n"
"LD a[4:7],v28,21952\n"
"ds_read_b64 a[8:9], v38 offset:256\n"
"ds_read_b64 a[10:11], v39 offset:256\n"
"WL 12\n"
"v_smfmac_f32_32x32x32_bf16 v[128:143], a[224:227], a[216:223], v41\n"
"LD a[12:15],v31,21952\n"
"LD a[20:23],v40,640\n"
"WL 10\n"
"v_smfmac_f32_32x32x32_bf16 v[128:143], a[236:239], a[228:235], v41\n"
"buffer_load_dwordx4 a[112:115], v22, s[36:39], s81 offen offset:128\n"
"LD a[24:27],v27,19776\n"
"LD a[28:31],v29,19776\n"
"ds_read_b64 a[32:33], v32 offset:320\n"
"ds_read_b64 a[34:35], v33 offset:320\n"
"WL 10\n"
"v_smfmac_f32_32x32x32_bf16 v[128:143], a[248:251], a[240:247], v41\n"
"LD a[216:219],v31,19776\n"
"LD a[220:223],v27,20864\n"
"ds_read_b64 a[224:225], v34 offset:320\n"
"ds_read_b64 a[226:227], v35 offset:320\n"
"WL 10\n"
"v_smfmac_f32_32x32x32_bf16 v[128:143], a[8:11], a[0:7], v41\n"
"buffer_load_dwordx4 a[116:119], v23, s[36:39], 0 offen offset:128\n"
"LD a[228:231],v29,20864\n"
"LD a[232:235],v31,20864\n"
"ds_read_b64 a[236:237], v36 offset:320\n"
"ds_read_b64 a[238:239], v37 offset:320\n"
"WL 12\n"
"MZ v[144:159],a[20:23],a[12:15]\n"
"LD a[240:243],v27,21952\n"
"LD a[244:247],v29,21952\n"
"ds_read_b64 a[248:249], v38 offset:320\n"
"ds_read_b64 a[250:251], v39 offset:320\n"
"WL 12\n"
"v_smfmac_f32_32x32x32_bf16 v[144:159], a[32:35], a[24:31], v41\n"
"LD a[0:3],v30,41728\n"
"LD a[8:11],v40,768\n"
"WL 10\n"
"v_smfmac_f32_32x32x32_bf16 v[144:159], a[224:227], a[216:223], v41\n"
"LD a[12:15],v26,39552\n"
"LD a[16:19],v28,39552\n"
"ds_read_b64 a[20:21], v32 offset:384\n"
"ds_read_b64 a[22:23], v33 offset:384\n"
"WL 10\n"
"v_smfmac_f32_32x32x32_bf16 v[144:159], a[236:239], a[228:235], v41\n"
"LD a[24:27],v30,39552\n"
"LD a[28:31],v26,40640\n"
"ds_read_b64 a[32:33], v34 offset:384\n"
"ds_read_b64 a[34:35], v35 offset:384\n"
"WL 10\n"
"v_smfmac_f32_32x32x32_bf16 v[144:159], a[248:251], a[240:247], v41\n"
"LD a[216:219],v28,40640\n"
"LD a[220:223],v30,40640\n"
"ds_read_b64 a[224:225], v36 offset:384\n"
"ds_read_b64 a[226:227], v37 offset:384\n"
"s_barrier\n"
"s_waitcnt vmcnt(9)\n"
"ds_write_b128 v19, a[80:83] offset:0\n"
"s_waitcnt vmcnt(8)\n"
"ds_write_b128 v20, a[84:87] offset:0\n"
"v_pk_add_f32 v[48:49], v[48:49], v[128:129]\n"
"v_pk_fma_f32 v[50:51], v[128:129], v[128:129], v[50:51]\n"
"v_pk_add_f32 v[52:53], v[52:53], v[132:133]\n"
"v_pk_fma_f32 v[54:55], v[132:133], v[132:133], v[54:55]\n"
"s_waitcnt vmcnt(7)\n"
"ds_write_b128 v19, a[88:91] offset:1088\n"
"v_pk_add_f32 v[56:57], v[56:57], v[136:137]\n"
"v_pk_fma_f32 v[58:59], v[136:137], v[136:137], v[58:59]\n"
"v_pk_add_f32 v[60:61], v[60:61], v[140:141]\n"
"v_pk_fma_f32 v[62:63], v[140:141], v[140:141], v[62:63]\n"
"s_waitcnt vmcnt(6)\n"
"ds_write_b128 v20, a[92:95] offset:1088\n"
"v_pk_add_f32 v[48:49], v[48:49], v[130:131]\n"
"v_pk_fma_f32 v[50:51], v[130:131], v[130:131], v[50:51]\n"
"v_pk_add_f32 v[52:53], v[52:53], v[134:135]\n"
"v_pk_fma_f32 v[54:55], v[134:135], v[134:135], v[54:55]\n"
"s_waitcnt vmcnt(5)\n"
"ds_write_b128 v19, a[96:99] offset:2176\n"
"v_pk_add_f32 v[56:57], v[56:57], v[138:139]\n"
"v_pk_fma_f32 v[58:59], v[138:139], v[138:139], v[58:59]\n"
"v_pk_add_f32 v[60:61], v[60:61], v[142:143]\n"
"v_pk_fma_f32 v[62:63], v[142:143], v[142:143], v[62:63]\n"
"s_waitcnt vmcnt(4)\n"
"ds_write_b128 v20, a[100:103] offset:2176\n"
"v_pk_add_f32 v[48:49], v[48:49], v[144:145]\n"
"v_pk_fma_f32 v[50:51], v[144:145], v[144:145], v[50:51]\n"
"v_pk_add_f32 v[52:53], v[52:53], v[148:149]\n"
"v_pk_fma_f32 v[54:55], v[148:149], v[148:149], v[54:55]\n"
"s_waitcnt vmcnt(3)\n"
"ds_write_b128 v19, a[104:107] offset:3264\n"
"v_pk_add_f32 v[56:57], v[56:57], v[152:153]\n"
"v_pk_fma_f32 v[58:59], v[152:153], v[152:153], v[58:59]\n"
"v_pk_add_f32 v[60:61], v[60:61], v[156:157]\n"
"v_pk_fma_f32 v[62:63], v[156:157], v[156:157], v[62:63]\n"
"s_waitcnt vmcnt(2)\n"
"ds_write_b128 v20, a[108:111] offset:3264\n"
"v_pk_add_f32 v[48:49], v[48:49], v[146:147]\n"
"v_pk_fma_f32 v[50:51], v[146:147], v[146:147], v[50:51]\n"
"v_pk_add_f32 v[52:53], v[52:53], v[150:151]\n"
"v_pk_fma_f32 v[54:55], v[150:151], v[150:151], v[54:55]\n"
"s_waitcnt vmcnt(1)\n"
"ds_write_b128 v19, a[112:115] offset:4352\n"
"v_pk_add_f32 v[56:57], v[56:57], v[154:155]\n"
"v_pk_fma_f32 v[58:59], v[154:155], v[154:155], v[58:59]\n"
"v_pk_add_f32 v[60:61], v[60:61], v[158:159]\n"
"v_pk_fma_f32 v[62:63], v[158:159], v[158:159], v[62:63]\n"
"s_waitcnt vmcnt(0)\n"
"ds_write_b128 v21, a[116:119] offset:0\n"
"WL 5\n"
"MZ v[160:175],a[8:11],a[0:3]\n"
"LD a[228:231],v26,41728\n"
"LD a[232:235],v28,41728\n"
"ds_read_b64 a[236:237], v38 offset:384\n"
"ds_read_b64 a[238:239], v39 offset:384\n"
"v_smfmac_f32_32x32x32_bf16 v[160:175], a[20:23], a[12:19], v41\n"
"LD a[240:243],v31,41728\n"
"LD a[248:251],v40,896\n"
"v_smfmac_f32_32x32x32_bf16 v[160:175], a[32:35], a[24:31], v41\n"
"LD a[0:3],v27,39552\n"
"LD a[4:7],v29,39552\n"
"ds_read_b64 a[8:9], v32 offset:448\n"
"ds_read_b64 a[10:11], v33 offset:448\n"
"v_smfmac_f32_32x32x32_bf16 v[160:175], a[224:227], a[216:223], v41\n"
"LD a[12:15],v31,39552\n"
"LD a[16:19],v27,40640\n"
"ds_read_b64 a[20:21], v34 offset:448\n"
"ds_read_b64 a[22:23], v35 offset:448\n"
"WL 10\n"
"v_smfmac_f32_32x32x32_bf16 v[160:175], a[236:239], a[228:235], v41\n"
"LD a[24:27],v29,40640\n"
"LD a[28:31],v31,40640\n"
"ds_read_b64 a[32:33], v36 offset:448\n"
"ds_read_b64 a[34:35], v37 offset:448\n"
"WL 12\n"
"MZ v[176:191],a[248:251],a[240:243]\n"
"LD a[216:219],v27,41728\n"
"LD a[220:223],v29,41728\n"
"ds_read_b64 a[224:225], v38 offset:448\n"
"ds_read_b64 a[226:227], v39 offset:448\n"
"s_nop 1\n"
"v_pk_add_f32 v[48:49], v[48:49], v[160:161]\n"
"v_pk_fma_f32 v[50:51], v[160:161], v[160:161], v[50:51]\n"
"v_pk_add_f32 v[52:53], v[52:53], v[164:165]\n"
"v_pk_fma_f32 v[54:55], v[164:165], v[164:165], v[54:55]\n"
"v_pk_add_f32 v[56:57], v[56:57], v[168:169]\n"
"v_pk_fma_f32 v[58:59], v[168:169], v[168:169], v[58:59]\n"
"v_pk_add_f32 v[60:61], v[60:61], v[172:173]\n"
"v_pk_fma_f32 v[62:63], v[172:173], v[172:173], v[62:63]\n"
"s_barrier\n"
"WL 12\n"
"v_smfmac_f32_32x32x32_bf16 v[176:191], a[8:11], a[0:7], v41\n"
"LD a[228:231],v30,2176\n"
"LD a[236:239],v40,1024\n"
"WL 10\n"
"v_smfmac_f32_32x32x32_bf16 v[176:191], a[20:23], a[12:19], v41\n"
"LD a[240:243],v26,0\n"
"LD a[244:247],v28,0\n"
"ds_read_b64 a[248:249], v32 offset:512\n"
"ds_read_b64 a[250:251], v33 offset:512\n"
"v_pk_add_f32 v[48:49], v[48:49], v[162:163]\n"
"v_pk_fma_f32 v[50:51], v[162:163], v[162:163], v[50:51]\n"
"v_pk_add_f32 v[52:53], v[52:53], v[166:167]\n"
"v_pk_fma_f32 v[54:55], v[166:167], v[166:167], v[54:55]\n"
"v_pk_add_f32 v[56:57], v[56:57], v[170:171]\n"
"v_pk_fma_f32 v[58:59], v[170:171], v[170:171], v[58:59]\n"
"v_pk_add_f32 v[60:61], v[60:61], v[174:175]\n"
"v_pk_fma_f32 v[62:63], v[174:175], v[174:175], v[62:63]\n"
"WL 8\n"
"v_smfmac_f32_32x32x32_bf16 v[176:191], a[32:35], a[24:31], v41\n"
"LD a[0:3],v30,0\n"
"LD a[4:7],v26,1088\n"
"ds_read_b64 a[8:9], v34 offset:512\n"
"ds_read_b64 a[10:11], v35 offset:512\n"
"WL 10\n"
"v_smfmac_f32_32x32x32_bf16 v[176:191], a[224:227], a[216:223], v41\n"
"LD a[12:15],v28,1088\n"
"LD a[16:19],v30,1088\n"
"ds_read_b64 a[20:21], v36 offset:512\n"
"ds_read_b64 a[22:23], v37 offset:512\n"
"WL 12\n"
"MZ v[192:207],a[236:239],a[228:231]\n"
"LD a[24:27],v26,2176\n"
"LD a[28:31],v28,2176\n"
"ds_read_b64 a[32:33], v38 offset:512\n"
"ds_read_b64 a[34:35], v39 offset:512\n"
"s_nop 1\n"
"v_pk_add_f32 v[48:49], v[48:49], v[176:177]\n"
"v_pk_fma_f32 v[50:51], v[176:177], v[176:177], v[50:51]\n"
"v_pk_add_f32 v[52:53], v[52:53], v[180:181]\n"
"v_pk_fma_f32 v[54:55], v[180:181], v[180:181], v[54:55]\n"
"v_pk_add_f32 v[56:57], v[56:57], v[184:185]\n"
"v_pk_fma_f32 v[58:59], v[184:185], v[184:185], v[58:59]\n"
"v_pk_add_f32 v[60:61], v[60:61], v[188:189]\n"
"v_pk_fma_f32 v[62:63], v[188:189], v[188:189], v[62:63]\n"
"WL 12\n"
"v_smfmac_f32_32x32x32_bf16 v[192:207], a[248:251], a[240:247], v41\n"
"LD a[216:219],v31,2176\n"
"LD a[224:227],v40,1152\n"
"WL 10\n"
"v_smfmac_f32_32x32x32_bf16 v[192:207], a[8:11], a[0:7], v41\n"
"LD a[228:231],v27,0\n"
"LD a[232:235],v29,0\n"
"ds_read_b64 a[236:237], v32 offset:576\n"
"ds_read_b64 a[238:239], v33 offset:576\n"
"v_pk_add_f32 v[48:49], v[48:49], v[178:179]\n"
"v_pk_fma_f32 v[50:51], v[178:179], v[178:179], v[50:51]\n"
"v_pk_add_f32 v[52:53], v[52:53], v[182:183]\n"
"v_pk_fma_f32 v[54:55], v[182:183], v[182:183], v[54:55]\n"
"v_pk_add_f32 v[56:57], v[56:57], v[186:187]\n"
"v_pk_fma_f32 v[58:59], v[186:187], v[186:187], v[58:59]\n"
"v_pk_add_f32 v[60:61], v[60:61], v[190:191]\n"
"v_pk_fma_f32 v[62:63], v[190:191], v[190:191], v[62:63]\n"
"WL 9\n"
"v_smfmac_f32_32x32x32_bf16 v[192:207], a[20:23], a[12:19], v41\n"
"LD a[240:243],v31,0\n"
"LD a[244:247],v27,1088\n"
"ds_read_b64 a[248:249], v34 offset:576\n"
"ds_read_b64 a[250:251], v35 offset:576\n"
"WL 10\n"
"v_smfmac_f32_32x32x32_bf16 v[192:207], a[32:35], a[24:31], v41\n"
"LD a[0:3],v29,1088\n"
"LD a[4:7],v31,1088\n"
"ds_read_b64 a[8:9], v36 offset:576\n"
"ds_read_b64 a[10:11], v37 offset:576\n"
"WL 12\n"
"MZ v[208:223],a[224:227],a[216:219]\n"
"LD a[12:15],v27,2176\n"
"LD a[16:19],v29,2176\n"
"ds_read_b64 a[20:21], v38 offset:576\n"
"ds_read_b64 a[22:23], v39 offset:576\n"
"s_nop 1\n"
"v_pk_add_f32 v[48:49], v[48:49], v[192:193]\n"
"v_pk_fma_f32 v[50:51], v[192:193], v[192:193], v[50:51]\n"
"v_pk_add_f32 v[52:53], v[52:53], v[196:197]\n"
"v_pk_fma_f32 v[54:55], v[196:197], v[196:197], v[54:55]\n"
"v_pk_add_f32 v[56:57], v[56:57], v[200:201]\n"
"v_pk_fma_f32 v[58:59], v[200:201], v[200:201], v[58:59]\n"
"v_pk_add_f32 v[60:61], v[60:61], v[204:205]\n"
"v_pk_fma_f32 v[62:63], v[204:205], v[204:205], v[62:63]\n"
"WL 12\n"
"v_smfmac_f32_32x32x32_bf16 v[208:223], a[236:239], a[228:235], v41\n"
"LD a[24:27],v30,21952\n"
"LD a[32:35],v40,1280\n"
"WL 10\n"
"v_smfmac_f32_32x32x32_bf16 v[208:223], a[248:251], a[240:247], v41\n"
"LD a[216:219],v26,19776\n"
"LD a[220:223],v28,19776\n"
"ds_read_b64 a[224:225], v32 offset:640\n"
"ds_read_b64 a[226:227], v33 offset:640\n"
"v_pk_add_f32 v[48:49], v[48:49], v[194:195]\n"
"v_pk_fma_f32 v[50:51], v[194:195], v[194:195], v[50:51]\n"
"v_pk_add_f32 v[52:53], v[52:53], v[198:199]\n"
"v_pk_fma_f32 v[54:55], v[198:199], v[198:199], v[54:55]\n"
"v_pk_add_f32 v[56:57], v[56:57], v[202:203]\n"
"v_pk_fma_f32 v[58:59], v[202:203], v[202:203], v[58:59]\n"
"v_pk_add_f32 v[60:61], v[60:61], v[206:207]\n"
"v_pk_fma_f32 v[62:63], v[206:207], v[206:207], v[62:63]\n"
"WL 9\n"
"v_smfmac_f32_32x32x32_bf16 v[208:223], a[8:11], a[0:7], v41\n"
"LD a[228:231],v30,19776\n"
"LD a[232:235],v26,20864\n"
"ds_read_b64 a[236:237], v34 offset:640\n"
"ds_read_b64 a[238:239], v35 offset:640\n"
"WL 10\n"
"v_smfmac_f32_32x32x32_bf16 v[208:223], a[20:23], a[12:19], v41\n"
"LD a[240:243],v28,20864\n"
"LD a[244:247],v30,20864\n"
"ds_read_b64 a[248:249], v36 offset:640\n"
"ds_read_b64 a[250:251], v37 offset:640\n"
"WL 12\n"
"MZ v[224:239],a[32:35],a[24:27]\n"
"LD a[0:3],v26,21952\n"
"LD a[4:7],v28,21952\n"
"ds_read_b64 a[8:9], v38 offset:640\n"
"ds_read_b64 a[10:11], v39 offset:640\n"
"s_nop 1\n"
"v_pk_add_f32 v[48:49], v[48:49], v[208:209]\n"
"v_pk_fma_f32 v[50:51], v[208:209], v[208:209], v[50:51]\n"
"v_pk_add_f32 v[52:53], v[52:53], v[212:213]\n"
"v_pk_fma_f32 v[54:55], v[212:213], v[212:213], v[54:55]\n"
"v_pk_add_f32 v[56:57], v[56:57], v[216:217]\n"
"v_pk_fma_f32 v[58:59], v[216:217], v[216:217], v[58:59]\n"
"v_pk_add_f32 v[60:61], v[60:61], v[220:221]\n"
"v_pk_fma_f32 v[62:63], v[220:221], v[220:221], v[62:63]\n"
"WL 12\n"
"v_smfmac_f32_32x32x32_bf16 v[224:239], a[224:227], a[216:223], v41\n"
"LD a[12:15],v31,21952\n"
"LD a[20:23],v40,1408\n"
"WL 10\n"
"v_smfmac_f32_32x32x32_bf16 v[224:239], a[236:239], a[228:235], v41\n"
"LD a[24:27],v27,19776\n"
"LD a[28:31],v29,19776\n"
"ds_read_b64 a[32:33], v32 offset:704\n"
"ds_read_b64 a[34:35], v33 offset:704\n"
"v_pk_add_f32 v[48:49], v[48:49], v[210:211]\n"
"v_pk_fma_f32 v[50:51], v[210:211], v[210:211], v[50:51]\n"
"v_pk_add_f32 v[52:53], v[52:53], v[214:215]\n"
"v_pk_fma_f32 v[54:55], v[214:215], v[214:215], v[54:55]\n"
"v_pk_add_f32 v[56:57], v[56:57], v[218:219]\n"
"v_pk_fma_f32 v[58:59], v[218:219], v[218:219], v[58:59]\n"
"v_pk_add_f32 v[60:61], v[60:61], v[222:223]\n"
"v_pk_fma_f32 v[62:63], v[222:223], v[222:223], v[62:63]\n"
"WL 9\n"
"v_smfmac_f32_32x32x32_bf16 v[224:239], a[248:251], a[240:247], v41\n"
"LD a[216:219],v31,19776\n"
"LD a[220:223],v27,20864\n"
"ds_read_b64 a[224:225], v34 offset:704\n"
"ds_read_b64 a[226:227], v35 offset:704\n"
"WL 10\n"
"v_smfmac_f32_32x32x32_bf16 v[224:239], a[8:11], a[0:7], v41\n"
"LD a[228:231],v29,20864\n"
"LD a[232:235],v31,20864\n"
"ds_read_b64 a[236:237], v36 offset:704\n"
"ds_read_b64 a[238:239], v37 offset:704\n"
"WL 12\n"
"MZ v[240:255],a[20:23],a[12:15]\n"
"LD a[240:243],v27,21952\n"
"LD a[244:247],v29,21952\n"
"ds_read_b64 a[248:249], v38 offset:704\n"
"ds_read_b64 a[250:251], v39 offset:704\n"
"s_nop 1\n"
"v_pk_add_f32 v[48:49], v[48:49], v[224:225]\n"
"v_pk_fma_f32 v[50:51], v[224:225], v[224:225], v[50:51]\n"
"v_pk_add_f32 v[52:53], v[52:53], v[228:229]\n"
"v_pk_fma_f32 v[54:55], v[228:229], v[228:229], v[54:55]\n"
"v_pk_add_f32 v[56:57], v[56:57], v[232:233]\n"
"v_pk_fma_f32 v[58:59], v[232:233], v[232:233], v[58:59]\n"
"v_pk_add_f32 v[60:61], v[60:61], v[236:237]\n"
"v_pk_fma_f32 v[62:63], v[236:237], v[236:237], v[62:63]\n"
"WL 12\n"
"v_smfmac_f32_32x32x32_bf16 v[240:255], a[32:35], a[24:31], v41\n"
"WL 8\n"
"v_smfmac_f32_32x32x32_bf16 v[240:255], a[224:227], a[216:223], v41\n"
"v_pk_add_f32 v[48:49], v[48:49], v[226:227]\n"
"v_pk_fma_f32 v[50:51], v[226:227], v[226:227], v[50:51]\n"
"v_pk_add_f32 v[52:53], v[52:53], v[230:231]\n"
"v_pk_fma_f32 v[54:55], v[230:231], v[230:231], v[54:55]\n"
"v_pk_add_f32 v[56:57], v[56:57], v[234:235]\n"
"v_pk_fma_f32 v[58:59], v[234:235], v[234:235], v[58:59]\n"
"v_pk_add_f32 v[60:61], v[60:61], v[238:239]\n"
"v_pk_fma_f32 v[62:63], v[238:239], v[238:239], v[62:63]\n"
"WL 4\n"
"s_nop 0\n"
"v_smfmac_f32_32x32x32_bf16 v[240:255], a[236:239], a[228:235], v41\n"
"WL 0\n"
"v_smfmac_f32_32x32x32_bf16 v[240:255], a[248:251], a[240:247], v41\n"
"s_nop 11\n"
"v_pk_add_f32 v[48:49], v[48:49], v[240:241]\n"
"v_pk_fma_f32 v[50:51], v[240:241], v[240:241], v[50:51]\n"
"v_pk_add_f32 v[52:53], v[52:53], v[244:245]\n"
"v_pk_fma_f32 v[54:55], v[244:245], v[244:245], v[54:55]\n"
"v_pk_add_f32 v[56:57], v[56:57], v[248:249]\n"
"v_pk_fma_f32 v[58:59], v[248:249], v[248:249], v[58:59]\n"
"v_pk_add_f32 v[60:61], v[60:61], v[252:253]\n"
"v_pk_fma_f32 v[62:63], v[252:253], v[252:253], v[62:63]\n"
"v_pk_add_f32 v[48:49], v[48:49], v[242:243]\n"
"v_pk_fma_f32 v[50:51], v[242:243], v[242:243], v[50:51]\n"
"v_pk_add_f32 v[52:53], v[52:53], v[246:247]\n"
"v_pk_fma_f32 v[54:55], v[246:247], v[246:247], v[54:55]\n"
"v_pk_add_f32 v[56:57], v[56:57], v[250:251]\n"
"v_pk_fma_f32 v[58:59], v[250:251], v[250:251], v[58:59]\n"
"v_pk_add_f32 v[60:61], v[60:61], v[254:255]\n"
"v_pk_fma_f32 v[62:63], v[254:255], v[254:255], v[62:63]\n"
"v_add_f32 v48, v48, v49\n"
"v_add_f32 v52, v52, v53\n"
"v_add_f32 v56, v56, v57\n"
"v_add_f32 v60, v60, v61\n"
"v_add_f32 v50, v50, v51\n"
"v_add_f32 v54, v54, v55\n"
"v_add_f32 v58, v58, v59\n"
"v_add_f32 v62, v62, v63\n"
"v_permlane32_swap_b32 v48, v50\n"
"v_permlane32_swap_b32 v52, v54\n"
"v_permlane32_swap_b32 v56, v58\n"
"v_permlane32_swap_b32 v60, v62\n"
"v_add_f32 v48, v48, v50\n"
"v_add_f32 v52, v52, v54\n"
"v_add_f32 v56, v56, v58\n"
"v_add_f32 v60, v60, v62\n"
"v_mul_f32 v48, 0x3c2aaaab, v48\n"
"v_mul_f32 v52, 0x3c2aaaab, v52\n"
"v_mul_f32 v56, 0x3c2aaaab, v56\n"
"v_mul_f32 v60, 0x3c2aaaab, v60\n"
"v_mov_b32 v50, v48\n"
"v_mov_b32 v54, v52\n"
"v_mov_b32 v58, v56\n"
"v_mov_b32 v62, v60\n"
"v_permlane32_swap_b32 v48, v50\n"
"v_permlane32_swap_b32 v52, v54\n"
"v_permlane32_swap_b32 v56, v58\n"
"v_permlane32_swap_b32 v60, v62\n"
"v_fma_f32 v50, -v48, v48, v50\n"
"v_fma_f32 v54, -v52, v52, v54\n"
"v_fma_f32 v58, -v56, v56, v58\n"
"v_fma_f32 v62, -v60, v60, v62\n"
"v_max_f32 v50, 0, v50\n"
"v_max_f32 v54, 0, v54\n"
"v_max_f32 v58, 0, v58\n"
"v_max_f32 v62, 0, v62\n"
"v_add_f32 v50, 0x3727c5ac, v50\n"
"v_add_f32 v54, 0x3727c5ac, v54\n"
"v_add_f32 v58, 0x3727c5ac, v58\n"
"v_add_f32 v62, 0x3727c5ac, v62\n"
"v_rsq_f32 v50, v50\n"
"v_rsq_f32 v54, v54\n"
"v_rsq_f32 v58, v58\n"
"v_rsq_f32 v62, v62\n"
"v_mul_f32 v50, s21, v50\n"
"v_mul_f32 v54, s21, v54\n"
"v_mul_f32 v58, s21, v58\n"
"v_mul_f32 v62, s21, v62\n"
"v_mul_f32 v51, v48, v50\n"
"v_mul_f32 v55, v52, v54\n"
"v_mul_f32 v59, v56, v58\n"
"v_mul_f32 v63, v60, v62\n"
"v_and_b32 v14, 0x2f0, v0\n"
"v_and_b32 v5, 0x100, v0\n"
"v_lshl_or_b32 v14, v5, 2, v14\n"
"LD a[232:235],v1,0\n"
"LD a[236:239],v1,1024\n"
"LD a[240:243],v1,2048\n"
"LD a[244:247],v1,3072\n"
"LD a[248:251],v1,4096\n"
"LD a[252:255],v1,5120\n"
"NR v[64:65],v[50:51]\n"
"NR v[66:67],v[50:51]\n"
"NR v[80:81],v[50:51]\n"
"NR v[82:83],v[50:51]\n"
"NR v[68:69],v[54:55]\n"
"NR v[70:71],v[54:55]\n"
"NR v[84:85],v[54:55]\n"
"NR v[86:87],v[54:55]\n"
"NR v[72:73],v[58:59]\n"
"NR v[74:75],v[58:59]\n"
"NR v[88:89],v[58:59]\n"
"NR v[90:91],v[58:59]\n"
"NR v[76:77],v[62:63]\n"
"NR v[78:79],v[62:63]\n"
"NR v[92:93],v[62:63]\n"
"NR v[94:95],v[62:63]\n"
"CB v16,v64,v65\n"
"CB v17,v66,v67\n"
"CB v18,v80,v81\n"
"CB v19,v82,v83\n"
"CB v20,v68,v69\n"
"CB v21,v70,v71\n"
"CB v22,v84,v85\n"
"CB v23,v86,v87\n"
"CB v32,v72,v73\n"
"CB v33,v74,v75\n"
"CB v34,v88,v89\n"
"CB v35,v90,v91\n"
"CB v36,v76,v77\n"
"CB v37,v78,v79\n"
"CB v38,v92,v93\n"
"CB v39,v94,v95\n"
"NR v[96:97],v[50:51]\n"
"NR v[98:99],v[50:51]\n"
"NR v[112:113],v[50:51]\n"
"NR v[114:115],v[50:51]\n"
"NR v[100:101],v[54:55]\n"
"NR v[102:103],v[54:55]\n"
"NR v[116:117],v[54:55]\n"
"NR v[118:119],v[54:55]\n"
"NR v[104:105],v[58:59]\n"
"NR v[106:107],v[58:59]\n"
"NR v[120:121],v[58:59]\n"
"NR v[122:123],v[58:59]\n"
"NR v[108:109],v[62:63]\n"
"NR v[110:111],v[62:63]\n"
"NR v[124:125],v[62:63]\n"
"NR v[126:127],v[62:63]\n"
"CB v24,v96,v97\n"
"CB v25,v98,v99\n"
"CB v26,v112,v113\n"
"CB v27,v114,v115\n"
"CB v28,v100,v101\n"
"CB v29,v102,v103\n"
"CB v30,v116,v117\n"
"CB v31,v118,v119\n"
"CB v40,v104,v105\n"
"CB v41,v106,v107\n"
"CB v42,v120,v121\n"
"CB v43,v122,v123\n"
"CB v44,v108,v109\n"
"CB v45,v110,v111\n"
"CB v46,v124,v125\n"
"CB v47,v126,v127\n"
"NR v[128:129],v[50:51]\n"
"NR v[130:131],v[50:51]\n"
"NR v[144:145],v[50:51]\n"
"NR v[146:147],v[50:51]\n"
"NR v[132:133],v[54:55]\n"
"NR v[134:135],v[54:55]\n"
"NR v[148:149],v[54:55]\n"
"NR v[150:151],v[54:55]\n"
"NR v[136:137],v[58:59]\n"
"NR v[138:139],v[58:59]\n"
"NR v[152:153],v[58:59]\n"
"NR v[154:155],v[58:59]\n"
"NR v[140:141],v[62:63]\n"
"NR v[142:143],v[62:63]\n"
"NR v[156:157],v[62:63]\n"
"NR v[158:159],v[62:63]\n"
"CB v64,v128,v129\n"
"CB v65,v130,v131\n"
"CB v66,v144,v145\n"
"CB v67,v146,v147\n"
"CB v68,v132,v133\n"
"CB v69,v134,v135\n"
"CB v70,v148,v149\n"
"CB v71,v150,v151\n"
"CB v80,v136,v137\n"
"CB v81,v138,v139\n"
"CB v82,v152,v153\n"
"CB v83,v154,v155\n"
"CB v84,v140,v141\n"
"CB v85,v142,v143\n"
"CB v86,v156,v157\n"
"CB v87,v158,v159\n"
"NR v[160:161],v[50:51]\n"
"NR v[162:163],v[50:51]\n"
"NR v[176:177],v[50:51]\n"
"NR v[178:179],v[50:51]\n"
"NR v[164:165],v[54:55]\n"
"NR v[166:167],v[54:55]\n"
"NR v[180:181],v[54:55]\n"
"NR v[182:183],v[54:55]\n"
"NR v[168:169],v[58:59]\n"
"NR v[170:171],v[58:59]\n"
"NR v[184:185],v[58:59]\n"
"NR v[186:187],v[58:59]\n"
"NR v[172:173],v[62:63]\n"
"NR v[174:175],v[62:63]\n"
"NR v[188:189],v[62:63]\n"
"NR v[190:191],v[62:63]\n"
"CB v72,v160,v161\n"
"WL 0\n"
"s_nop 0\n"
"MZ v[128:143],a[232:235],v[16:19]\n"
"CB v73,v162,v163\n"
"CB v74,v176,v177\n"
"CB v75,v178,v179\n"
"CB v76,v164,v165\n"
"CB v77,v166,v167\n"
"CB v78,v180,v181\n"
"CB v79,v182,v183\n"
"CB v88,v168,v169\n"
"CB v89,v170,v171\n"
"CB v90,v184,v185\n"
"CB v91,v186,v187\n"
"CB v92,v172,v173\n"
"MB v[128:143],a[236:239],v[24:27]\n"
"CB v93,v174,v175\n"
"CB v94,v188,v189\n"
"CB v95,v190,v191\n"
"NR v[192:193],v[50:51]\n"
"NR v[194:195],v[50:51]\n"
"NR v[208:209],v[50:51]\n"
"NR v[210:211],v[50:51]\n"
"NR v[196:197],v[54:55]\n"
"NR v[198:199],v[54:55]\n"
"NR v[212:213],v[54:55]\n"
"NR v[214:215],v[54:55]\n"
"NR v[200:201],v[58:59]\n"
"NR v[202:203],v[58:59]\n"
"NR v[216:217],v[58:59]\n"
"NR v[218:219],v[58:59]\n"
"NR v[204:205],v[62:63]\n"
"NR v[206:207],v[62:63]\n"
"NR v[220:221],v[62:63]\n"
"NR v[222:223],v[62:63]\n"
"CB v96,v192,v193\n"
"MB v[128:143],a[240:243],v[64:67]\n"
"CB v97,v194,v195\n"
"CB v98,v208,v209\n"
"CB v99,v210,v211\n"
"CB v100,v196,v197\n"
"CB v101,v198,v199\n"
"CB v102,v212,v213\n"
"CB v103,v214,v215\n"
"CB v112,v200,v201\n"
"CB v113,v202,v203\n"
"CB v114,v216,v217\n"
"CB v115,v218,v219\n"
"CB v116,v204,v205\n"
"MB v[128:143],a[244:247],v[72:75]\n"
"CB v117,v206,v207\n"
"CB v118,v220,v221\n"
"CB v119,v222,v223\n"
"NR v[224:225],v[50:51]\n"
"NR v[226:227],v[50:51]\n"
"NR v[240:241],v[50:51]\n"
"NR v[242:243],v[50:51]\n"
"NR v[228:229],v[54:55]\n"
"NR v[230:231],v[54:55]\n"
"NR v[244:245],v[54:55]\n"
"NR v[246:247],v[54:55]\n"
"NR v[232:233],v[58:59]\n"
"NR v[234:235],v[58:59]\n"
"NR v[248:249],v[58:59]\n"
"NR v[250:251],v[58:59]\n"
"NR v[236:237],v[62:63]\n"
"NR v[238:239],v[62:63]\n"
"NR v[252:253],v[62:63]\n"
"NR v[254:255],v[62:63]\n"
"CB v104,v224,v225\n"
"MB v[128:143],a[248:251],v[96:99]\n"
"CB v105,v226,v227\n"
"CB v106,v240,v241\n"
"CB v107,v242,v243\n"
"CB v108,v228,v229\n"
"CB v109,v230,v231\n"
"CB v110,v244,v245\n"
"CB v111,v246,v247\n"
"CB v120,v232,v233\n"
"CB v121,v234,v235\n"
"CB v122,v248,v249\n"
"CB v123,v250,v251\n"
"CB v124,v236,v237\n"
"MB v[128:143],a[252:255],v[104:107]\n"
"CB v125,v238,v239\n"
"CB v126,v252,v253\n"
"CB v127,v254,v255\n"
"v_lshrrev_b32_e64 v224, 4, v0\n"
"v_bfe_u32 v225, v224, 2, 1\n"
"v_lshrrev_b32 v226, 5, v224\n"
"v_cmp_eq_u32 vcc, v225, v226\n"
"v_and_b32 v225, 1, v224\n"
"v_lshlrev_b32 v225, 4, v225\n"
"v_mov_b32 v226, 0x3c00\n"
"v_lshlrev_b32 v225, v225, v226\n"
"v_cndmask_b32 v225, 0, v225, vcc\n"
"v_bfe_u32 v226, v224, 3, 1\n"
"v_bfe_u32 v224, v224, 1, 1\n"
"v_lshl_or_b32 v224, v226, 1, v224\n"
"v_cmp_eq_u32 vcc, 0, v224\n"
"v_mov_b32 v210, 0\n"
"v_lshrrev_b32 v211, 2, v0\n"
"v_cndmask_b32 v56, 0, v225, vcc\n"
"v_cmp_eq_u32_e64 vcc, 1, v224\n"
"GA v128\n"
"GA v129\n"
"v_cndmask_b32 v57, 0, v225, vcc\n"
"v_cmp_eq_u32 vcc, 2, v224\n"
"CH v134,v134,v135\n"
"CH v142,v142,v143\n"
"v_cndmask_b32 v58, 0, v225, vcc\n"
"v_cmp_eq_u32 vcc, 3, v224\n"
"PF v135,v134,v134,s56\n"
"PF v143,v142,v142,s56\n"
"v_cndmask_b32_e64 v59, 0, v225, vcc\n"
"buffer_load_dword v210, v210, s[48:51], 0 offen offset:0 sc1\n"
"s_mov_b64 s[52:53], s[18:19]\n"
"s_movk_i32 s54, 768\n"
"s_lshl_b32 s68, s58, 8\n"
"buffer_load_dword v208, v211, s[52:55], s68 offen\n"
"s_mov_b64 s[52:53], s[12:13]\n"
"buffer_load_dword v212, v211, s[52:55], s68 offen\n"
"MZ v[144:159],a[232:235],v[20:23]\n"
"GL v128\n"
"GH v129\n"
"GA v130\n"
"GA v131\n"
"PM v135,v135,v134\n"
"PM v143,v143,v142\n"
"MB v[144:159],a[236:239],v[28:31]\n"
"GL v130\n"
"GH v131\n"
"EX v135,0\n"
"GA v132\n"
"GA v133\n"
"EX v143,0\n"
"MB v[144:159],a[240:243],v[68:71]\n"
"GL v132\n"
"GH v133\n"
"EX v135,1\n"
"EX v143,1\n"
"s_barrier\n"
"s_nop 0\n"
"GA v136\n"
"GA v137\n"
"MB v[144:159],a[244:247],v[76:79]\n"
"GL v136\n"
"GH v137\n"
"PF v135,v135,s57,s57\n"
"PF v143,v143,s57,s57\n"
"RC v135,0\n"
"GA v138\n"
"GA v139\n"
"RC v143,0\n"
"MB v[144:159],a[248:251],v[100:103]\n"
"GL v138\n"
"GH v139\n"
"RC v135,1\n"
"GA v140\n"
"GA v141\n"
"RC v143,1\n"
"MB v[144:159],a[252:255],v[108:111]\n"
"GL v140\n"
"GH v141\n"
"PM v134,v134,v135\n"
"PM v142,v142,v143\n"
"PM v51,v134,v134\n"
"PM v55,v142,v142\n"
"WL 6\n"
"v_or_b32 v132, v132, v133\n"
"v_or_b32 v130, v130, v131\n"
"v_or_b32 v128, v128, v129\n"
"PM v50,v132,v132\n"
"PM v49,v130,v130\n"
"PM v48,v128,v128\n"
"GA v144\n"
"GA v145\n"
"CH v150,v150,v151\n"
"s_add_u32 m0, s34, 6144\n"
"MZ v[160:175],a[232:235],v[32:35]\n"
"GL v144\n"
"GH v145\n"
"buffer_load_dwordx4 v8, s[36:39], 0 offen offset:0 lds\n"
"CH v158,v158,v159\n"
"PF v151,v150,v150,s56\n"
"PF v159,v158,v158,s56\n"
"GA v146\n"
"GA v147\n"
"PM v151,v151,v150\n"
"s_add_u32 m0, s34, 25888\n"
"PM v159,v159,v158\n"
"MB v[160:175],a[236:239],v[40:43]\n"
"GL v146\n"
"GH v147\n"
"buffer_load_dwordx4 v8, s[36:39], 0 offen offset:32 lds\n"
"EX v151,0\n"
"GA v148\n"
"GA v149\n"
"EX v159,0\n"
"s_add_u32 m0, s34, 45632\n"
"MB v[160:175],a[240:243],v[80:83]\n"
"GL v148\n"
"GH v149\n"
"buffer_load_dwordx4 v8, s[36:39], 0 offen offset:64 lds\n"
"EX v151,1\n"
"EX v159,1\n"
"GA v152\n"
"GA v153\n"
"MB v[160:175],a[244:247],v[88:91]\n"
"GL v152\n"
"GH v153\n"
"buffer_load_dwordx4 a[36:39], v8, s[36:39], 0 offen offset:96\n"
"PF v151,v151,s57,s57\n"
"PF v159,v159,s57,s57\n"
"WL 6\n"
"v_or_b32 v140, v140, v141\n"
"v_or_b32 v138, v138, v139\n"
"v_or_b32 v136, v136, v137\n"
"RC v151,0\n"
"PM v54,v140,v140\n"
"PM v53,v138,v138\n"
"PM v52,v136,v136\n"
"GA v154\n"
"GA v155\n"
"RC v159,0\n"
"MB v[160:175],a[248:251],v[112:115]\n"
"GL v154\n"
"GH v155\n"
"buffer_load_dwordx4 a[56:59], v8, s[36:39], 0 offen offset:128\n"
"RC v151,1\n"
"GA v156\n"
"GA v157\n"
"RC v159,1\n"
"MB v[160:175],a[252:255],v[120:123]\n"
"GL v156\n"
"GH v157\n"
"buffer_load_dwordx4 a[76:79], v8, s[36:39], 0 offen offset:160\n"
"PM v150,v150,v151\n"
"PM v158,v158,v159\n"
"PF v51,v150,v150,v51\n"
"PF v55,v158,v158,v55\n"
"WL 4\n"
"v_or_b32 v148, v148, v149\n"
"v_or_b32 v146, v146, v147\n"
"v_or_b32 v144, v144, v145\n"
"PF v50,v148,v148,v50\n"
"PF v49,v146,v146,v49\n"
"GA v160\n"
"GA v161\n"
"CH v166,v166,v167\n"
"MZ v[176:191],a[232:235],v[36:39]\n"
"GL v160\n"
"GH v161\n"
"LD a[232:235],v1,6144\n"
"CH v174,v174,v175\n"
"PF v167,v166,v166,s56\n"
"PF v175,v174,v174,s56\n"
"PF v48,v144,v144,v48\n"
"GA v162\n"
"GA v163\n"
"PM v167,v167,v166\n"
"PM v175,v175,v174\n"
"MB v[176:191],a[236:239],v[44:47]\n"
"GL v162\n"
"GH v163\n"
"LD a[236:239],v1,7168\n"
"EX v167,0\n"
"GA v164\n"
"GA v165\n"
"EX v175,0\n"
"MB v[176:191],a[240:243],v[84:87]\n"
"GL v164\n"
"GH v165\n"
"LD a[240:243],v1,8192\n"
"EX v167,1\n"
"EX v175,1\n"
"GA v168\n"
"GA v169\n"
"MB v[176:191],a[244:247],v[92:95]\n"
"GL v168\n"
"GH v169\n"
"LD a[244:247],v1,9216\n"
"PF v167,v167,s57,s57\n"
"PF v175,v175,s57,s57\n"
"WL 9\n"
"v_or_b32 v156, v156, v157\n"
"v_or_b32 v154, v154, v155\n"
"v_or_b32 v152, v152, v153\n"
"RC v167,0\n"
"PF v54,v156,v156,v54\n"
"PF v53,v154,v154,v53\n"
"PF v52,v152,v152,v52\n"
"GA v170\n"
"GA v171\n"
"RC v175,0\n"
"MB v[176:191],a[248:251],v[116:119]\n"
"GL v170\n"
"GH v171\n"
"LD a[248:251],v1,10240\n"
"RC v167,1\n"
"GA v172\n"
"GA v173\n"
"RC v175,1\n"
"MB v[176:191],a[252:255],v[124:127]\n"
"GL v172\n"
"GH v173\n"
"LD a[252:255],v1,11264\n"
"PM v166,v166,v167\n"
"PM v174,v174,v175\n"
"PF v51,v166,v166,v51\n"
"PF v55,v174,v174,v55\n"
"WL 7\n"
"v_or_b32 v164, v164, v165\n"
"v_or_b32 v162, v162, v163\n"
"v_or_b32 v160, v160, v161\n"
"PF v50,v164,v164,v50\n"
"PF v49,v162,v162,v49\n"
"GA v176\n"
"GA v177\n"
"CH v182,v182,v183\n"
"MZ v[128:143],a[232:235],v[16:19]\n"
"GL v176\n"
"GH v177\n"
"CH v190,v190,v191\n"
"PF v183,v182,v182,s56\n"
"PF v191,v190,v190,s56\n"
"PF v48,v160,v160,v48\n"
"GA v178\n"
"GA v179\n"
"PM v183,v183,v182\n"
"PM v191,v191,v190\n"
"MB v[128:143],a[236:239],v[24:27]\n"
"GL v178\n"
"GH v179\n"
"EX v183,0\n"
"GA v180\n"
"GA v181\n"
"EX v191,0\n"
"MB v[128:143],a[240:243],v[64:67]\n"
"GL v180\n"
"GH v181\n"
"EX v183,1\n"
"EX v191,1\n"
"GA v184\n"
"GA v185\n"
"WL 6\n"
"MB v[128:143],a[244:247],v[72:75]\n"
"GL v184\n"
"GH v185\n"
"PF v183,v183,s57,s57\n"
"PF v191,v191,s57,s57\n"
"v_or_b32 v172, v172, v173\n"
"v_or_b32 v170, v170, v171\n"
"v_or_b32 v168, v168, v169\n"
"RC v183,0\n"
"PF v54,v172,v172,v54\n"
"PF v53,v170,v170,v53\n"
"PF v52,v168,v168,v52\n"
"GA v186\n"
"GA v187\n"
"RC v191,0\n"
"MB v[128:143],a[248:251],v[96:99]\n"
"GL v186\n"
"GH v187\n"
"RC v183,1\n"
"GA v188\n"
"GA v189\n"
"RC v191,1\n"
"MB v[128:143],a[252:255],v[104:107]\n"
"GL v188\n"
"GH v189\n"
"PM v182,v182,v183\n"
"PM v190,v190,v191\n"
"PF v51,v182,v182,v51\n"
"PF v55,v190,v190,v55\n"
"AW a99,v182\n"
"AW a103,v190\n"
"WL 6\n"
"v_or_b32 v180, v180, v181\n"
"v_or_b32 v178, v178, v179\n"
"v_or_b32 v176, v176, v177\n"
"PF v50,v180,v180,v50\n"
"GA v128\n"
"GA v129\n"
"CH v134,v134,v135\n"
"MZ v[144:159],a[232:235],v[20:23]\n"
"GL v128\n"
"GH v129\n"
"CH v142,v142,v143\n"
"PF v135,v134,v134,s56\n"
"PF v143,v142,v142,s56\n"
"PF v49,v178,v178,v49\n"
"PF v48,v176,v176,v48\n"
"GA v130\n"
"GA v131\n"
"AW a98,v180\n"
"AW a97,v178\n"
"AW a96,v176\n"
"PM v135,v135,v134\n"
"v_mfma_f32_16x16x32_f16 v[48:51], v[48:51], v[56:59], 0\n"
"GL v130\n"
"GH v131\n"
"PM v143,v143,v142\n"
"MB v[144:159],a[236:239],v[28:31]\n"
"EX v135,0\n"
"GA v132\n"
"GA v133\n"
"EX v143,0\n"
"EX v135,1\n"
"EX v143,1\n"
"v_pk_add_f32 v[50:51], v[48:49], v[50:51]\n"
"MB v[144:159],a[240:243],v[68:71]\n"
"GL v132\n"
"GH v133\n"
"v_add_f32_e64 v51, v50, v51\n"
"GA v136\n"
"GA v137\n"
"PF v135,v135,s57,s57\n"
"MB v[144:159],a[244:247],v[76:79]\n"
"GL v136\n"
"GH v137\n"
"PF v143,v143,s57,s57\n"
"WL 6\n"
"v_or_b32 v188, v188, v189\n"
"v_or_b32 v186, v186, v187\n"
"v_or_b32 v184, v184, v185\n"
"RC v135,0\n"
"PF v54,v188,v188,v54\n"
"PF v53,v186,v186,v53\n"
"PF v52,v184,v184,v52\n"
"GA v138\n"
"GA v139\n"
"AW a102,v188\n"
"AW a101,v186\n"
"AW a100,v184\n"
"v_mfma_f32_16x16x32_f16 v[52:55], v[52:55], v[56:59], 0\n"
"GL v138\n"
"GH v139\n"
"RC v143,0\n"
"MB v[144:159],a[248:251],v[100:103]\n"
"RC v135,1\n"
"GA v140\n"
"GA v141\n"
"RC v143,1\n"
"PM v134,v134,v135\n"
"PM v142,v142,v143\n"
"PM v195,v134,v134\n"
"v_pk_add_f32 v[54:55], v[52:53], v[54:55]\n"
"v_add_f32_e64 v52, v54, v55\n"
"MB v[144:159],a[252:255],v[108:111]\n"
"GL v140\n"
"GH v141\n"
"v_permlane16_swap_b32 v51, v52\n"
"v_add_f32 v62, v51, v52\n"
"PM v199,v142,v142\n"
"WL 4\n"
"v_or_b32 v132, v132, v133\n"
"v_or_b32 v130, v130, v131\n"
"v_or_b32 v128, v128, v129\n"
"PM v194,v132,v132\n"
"PM v193,v130,v130\n"
"PM v192,v128,v128\n"
"GA v144\n"
"GA v145\n"
"CH v150,v150,v151\n"
"MZ v[160:175],a[232:235],v[32:35]\n"
"GL v144\n"
"GH v145\n"
"CH v158,v158,v159\n"
"PF v151,v150,v150,s56\n"
"PF v159,v158,v158,s56\n"
"GA v146\n"
"GA v147\n"
"PM v151,v151,v150\n"
"PM v159,v159,v158\n"
"MB v[160:175],a[236:239],v[40:43]\n"
"GL v146\n"
"GH v147\n"
"EX v151,0\n"
"GA v148\n"
"GA v149\n"
"EX v159,0\n"
"MB v[160:175],a[240:243],v[80:83]\n"
"GL v148\n"
"GH v149\n"
"EX v151,1\n"
"EX v159,1\n"
"GA v152\n"
"GA v153\n"
"MB v[160:175],a[244:247],v[88:91]\n"
"GL v152\n"
"GH v153\n"
"PF v151,v151,s57,s57\n"
"PF v159,v159,s57,s57\n"
"WL 8\n"
"v_or_b32 v140, v140, v141\n"
"v_or_b32 v138, v138, v139\n"
"v_or_b32 v136, v136, v137\n"
"RC v151,0\n"
"PM v198,v140,v140\n"
"PM v197,v138,v138\n"
"PM v196,v136,v136\n"
"GA v154\n"
"GA v155\n"
"RC v159,0\n"
"MB v[160:175],a[248:251],v[112:115]\n"
"GL v154\n"
"GH v155\n"
"RC v151,1\n"
"GA v156\n"
"GA v157\n"
"RC v159,1\n"
"MB v[160:175],a[252:255],v[120:123]\n"
"GL v156\n"
"GH v157\n"
"PM v150,v150,v151\n"
"PM v158,v158,v159\n"
"PF v195,v150,v150,v195\n"
"PF v199,v158,v158,v199\n"
"WL 6\n"
"v_or_b32 v148, v148, v149\n"
"v_or_b32 v146, v146, v147\n"
"v_or_b32 v144, v144, v145\n"
"PF v194,v148,v148,v194\n"
"PF v193,v146,v146,v193\n"
"PF v192,v144,v144,v192\n"
"GA v160\n"
"GA v161\n"
"CH v166,v166,v167\n"
"s_add_u32 m0, s34, 7168\n"
"MZ v[176:191],a[232:235],v[36:39]\n"
"GL v160\n"
"GH v161\n"
"LD a[232:235],v1,12288\n"
"buffer_load_dwordx4 v11, s[36:39], 0 offen offset:0 lds\n"
"CH v174,v174,v175\n"
"PF v167,v166,v166,s56\n"
"PF v175,v174,v174,s56\n"
"GA v162\n"
"GA v163\n"
"PM v167,v167,v166\n"
"s_add_u32 m0, s34, 26912\n"
"PM v175,v175,v174\n"
"MB v[176:191],a[236:239],v[44:47]\n"
"GL v162\n"
"GH v163\n"
"LD a[236:239],v1,13312\n"
"buffer_load_dwordx4 v11, s[36:39], 0 offen offset:32 lds\n"
"EX v167,0\n"
"GA v164\n"
"GA v165\n"
"EX v175,0\n"
"s_add_u32 m0, s34, 46656\n"
"MB v[176:191],a[240:243],v[84:87]\n"
"GL v164\n"
"GH v165\n"
"LD a[240:243],v1,14336\n"
"buffer_load_dwordx4 v11, s[36:39], 0 offen offset:64 lds\n"
"EX v167,1\n"
"EX v175,1\n"
"GA v168\n"
"GA v169\n"
"MB v[176:191],a[244:247],v[92:95]\n"
"GL v168\n"
"GH v169\n"
"LD a[244:247],v1,15360\n"
"buffer_load_dwordx4 a[40:43], v11, s[36:39], 0 offen offset:96\n"
"PF v167,v167,s57,s57\n"
"PF v175,v175,s57,s57\n"
"WL 9\n"
"v_or_b32 v156, v156, v157\n"
"v_or_b32 v154, v154, v155\n"
"v_or_b32 v152, v152, v153\n"
"RC v167,0\n"
"PF v198,v156,v156,v198\n"
"PF v197,v154,v154,v197\n"
"PF v196,v152,v152,v196\n"
"GA v170\n"
"GA v171\n"
"RC v175,0\n"
"MB v[176:191],a[248:251],v[116:119]\n"
"GL v170\n"
"GH v171\n"
"LD a[248:251],v1,16384\n"
"buffer_load_dwordx4 a[60:63], v11, s[36:39], 0 offen offset:128\n"
"RC v167,1\n"
"GA v172\n"
"GA v173\n"
"RC v175,1\n"
"MB v[176:191],a[252:255],v[124:127]\n"
"GL v172\n"
"GH v173\n"
"LD a[252:255],v1,17408\n"
"buffer_load_dwordx4 a[80:83], v11, s[36:39], 0 offen offset:160\n"
"PM v166,v166,v167\n"
"PM v174,v174,v175\n"
"PF v195,v166,v166,v195\n"
"PF v199,v174,v174,v199\n"
"WL 6\n"
"v_or_b32 v164, v164, v165\n"
"v_or_b32 v162, v162, v163\n"
"v_or_b32 v160, v160, v161\n"
"PF v194,v164,v164,v194\n"
"GA v176\n"
"GA v177\n"
"CH v182,v182,v183\n"
"MZ v[128:143],a[232:235],v[16:19]\n"
"GL v176\n"
"GH v177\n"
"CH v190,v190,v191\n"
"PF v183,v182,v182,s56\n"
"PF v191,v190,v190,s56\n"
"PF v193,v162,v162,v193\n"
"PF v192,v160,v160,v192\n"
"GA v178\n"
"GA v179\n"
"PM v183,v183,v182\n"
"PM v191,v191,v190\n"
"MB v[128:143],a[236:239],v[24:27]\n"
"GL v178\n"
"GH v179\n"
"EX v183,0\n"
"GA v180\n"
"GA v181\n"
"EX v191,0\n"
"MB v[128:143],a[240:243],v[64:67]\n"
"GL v180\n"
"GH v181\n"
"EX v183,1\n"
"EX v191,1\n"
"GA v184\n"
"GA v185\n"
"MB v[128:143],a[244:247],v[72:75]\n"
"GL v184\n"
"GH v185\n"
"PF v183,v183,s57,s57\n"
"PF v191,v191,s57,s57\n"
"WL 8\n"
"v_or_b32 v172, v172, v173\n"
"v_or_b32 v170, v170, v171\n"
"v_or_b32 v168, v168, v169\n"
"RC v183,0\n"
"PF v198,v172,v172,v198\n"
"PF v197,v170,v170,v197\n"
"PF v196,v168,v168,v196\n"
"GA v186\n"
"GA v187\n"
"RC v191,0\n"
"MB v[128:143],a[248:251],v[96:99]\n"
"GL v186\n"
"GH v187\n"
"RC v183,1\n"
"GA v188\n"
"GA v189\n"
"RC v191,1\n"
"MB v[128:143],a[252:255],v[104:107]\n"
"GL v188\n"
"GH v189\n"
"PM v182,v182,v183\n"
"PM v190,v190,v191\n"
"PF v195,v182,v182,v195\n"
"PF v199,v190,v190,v199\n"
"AW a107,v182\n"
"AW a111,v190\n"
"WL 5\n"
"v_or_b32 v180, v180, v181\n"
"v_or_b32 v178, v178, v179\n"
"v_or_b32 v176, v176, v177\n"
"PF v194,v180,v180,v194\n"
"GA v128\n"
"GA v129\n"
"CH v134,v134,v135\n"
"MZ v[144:159],a[232:235],v[20:23]\n"
"GL v128\n"
"GH v129\n"
"CH v142,v142,v143\n"
"PF v135,v134,v134,s56\n"
"PF v143,v142,v142,s56\n"
"PF v193,v178,v178,v193\n"
"PF v192,v176,v176,v192\n"
"GA v130\n"
"GA v131\n"
"AW a106,v180\n"
"AW a105,v178\n"
"AW a104,v176\n"
"PM v135,v135,v134\n"
"v_mfma_f32_16x16x32_f16 v[192:195], v[192:195], v[56:59], 0\n"
"GL v130\n"
"GH v131\n"
"PM v143,v143,v142\n"
"MB v[144:159],a[236:239],v[28:31]\n"
"EX v135,0\n"
"GA v132\n"
"GA v133\n"
"EX v143,0\n"
"EX v135,1\n"
"EX v143,1\n"
"v_pk_add_f32 v[194:195], v[192:193], v[194:195]\n"
"MB v[144:159],a[240:243],v[68:71]\n"
"GL v132\n"
"GH v133\n"
"v_add_f32_e64 v195, v194, v195\n"
"GA v136\n"
"GA v137\n"
"PF v135,v135,s57,s57\n"
"MB v[144:159],a[244:247],v[76:79]\n"
"GL v136\n"
"GH v137\n"
"PF v143,v143,s57,s57\n"
"WL 6\n"
"v_or_b32 v188, v188, v189\n"
"v_or_b32 v186, v186, v187\n"
"v_or_b32 v184, v184, v185\n"
"RC v135,0\n"
"PF v198,v188,v188,v198\n"
"PF v197,v186,v186,v197\n"
"PF v196,v184,v184,v196\n"
"GA v138\n"
"GA v139\n"
"AW a110,v188\n"
"AW a109,v186\n"
"AW a108,v184\n"
"v_mfma_f32_16x16x32_f16 v[196:199], v[196:199], v[56:59], 0\n"
"GL v138\n"
"GH v139\n"
"RC v143,0\n"
"MB v[144:159],a[248:251],v[100:103]\n"
"RC v135,1\n"
"GA v140\n"
"GA v141\n"
"RC v143,1\n"
"PM v134,v134,v135\n"
"PM v142,v142,v143\n"
"PM v51,v134,v134\n"
"v_pk_add_f32 v[198:199], v[196:197], v[198:199]\n"
"v_add_f32_e64 v196, v198, v199\n"
"MB v[144:159],a[252:255],v[108:111]\n"
"GL v140\n"
"GH v141\n"
"v_permlane16_swap_b32 v195, v196\n"
"v_add_f32 v196, v195, v196\n"
"PM v55,v142,v142\n"
"WL 4\n"
"v_or_b32 v132, v132, v133\n"
"v_permlane32_swap_b32 v62, v196\n"
"v_add_f32 v3, v62, v196\n"
"v_or_b32 v130, v130, v131\n"
"v_or_b32 v128, v128, v129\n"
"PM v50,v132,v132\n"
"PM v49,v130,v130\n"
"GA v144\n"
"GA v145\n"
"CH v150,v150,v151\n"
"MZ v[160:175],a[232:235],v[32:35]\n"
"GL v144\n"
"GH v145\n"
"CH v158,v158,v159\n"
"PF v151,v150,v150,s56\n"
"PF v159,v158,v158,s56\n"
"PM v48,v128,v128\n"
"GA v146\n"
"GA v147\n"
"PM v151,v151,v150\n"
"PM v159,v159,v158\n"
"MB v[160:175],a[236:239],v[40:43]\n"
"GL v146\n"
"GH v147\n"
"EX v151,0\n"
"GA v148\n"
"GA v149\n"
"EX v159,0\n"
"MB v[160:175],a[240:243],v[80:83]\n"
"GL v148\n"
"GH v149\n"
"EX v151,1\n"
"EX v159,1\n"
"GA v152\n"
"GA v153\n"
"MB v[160:175],a[244:247],v[88:91]\n"
"GL v152\n"
"GH v153\n"
"PF v151,v151,s57,s57\n"
"PF v159,v159,s57,s57\n"
"WL 8\n"
"v_or_b32 v140, v140, v141\n"
"v_or_b32 v138, v138, v139\n"
"v_or_b32 v136, v136, v137\n"
"RC v151,0\n"
"PM v54,v140,v140\n"
"PM v53,v138,v138\n"
"PM v52,v136,v136\n"
"GA v154\n"
"GA v155\n"
"RC v159,0\n"
"MB v[160:175],a[248:251],v[112:115]\n"
"GL v154\n"
"GH v155\n"
"RC v151,1\n"
"GA v156\n"
"GA v157\n"
"RC v159,1\n"
"MB v[160:175],a[252:255],v[120:123]\n"
"GL v156\n"
"GH v157\n"
"PM v150,v150,v151\n"
"PM v158,v158,v159\n"
"PF v51,v150,v150,v51\n"
"PF v55,v158,v158,v55\n"
"WL 6\n"
"v_or_b32 v148, v148, v149\n"
"v_or_b32 v146, v146, v147\n"
"v_or_b32 v144, v144, v145\n"
"PF v50,v148,v148,v50\n"
"PF v49,v146,v146,v49\n"
"PF v48,v144,v144,v48\n"
"GA v160\n"
"GA v161\n"
"CH v166,v166,v167\n"
"MZ v[176:191],a[232:235],v[36:39]\n"
"GL v160\n"
"GH v161\n"
"LD a[232:235],v1,18432\n"
"CH v174,v174,v175\n"
"PF v167,v166,v166,s56\n"
"PF v175,v174,v174,s56\n"
"GA v162\n"
"GA v163\n"
"PM v167,v167,v166\n"
"PM v175,v175,v174\n"
"MB v[176:191],a[236:239],v[44:47]\n"
"GL v162\n"
"GH v163\n"
"buffer_load_dword v227, v0, s[24:27], s28 offen\n"
"LD a[236:239],v1,19456\n"
"EX v167,0\n"
"GA v164\n"
"GA v165\n"
"EX v175,0\n"
"MB v[176:191],a[240:243],v[84:87]\n"
"GL v164\n"
"GH v165\n"
"LD a[240:243],v1,20480\n"
"EX v167,1\n"
"EX v175,1\n"
"GA v168\n"
"GA v169\n"
"MB v[176:191],a[244:247],v[92:95]\n"
"GL v168\n"
"GH v169\n"
"LD a[244:247],v1,21504\n"
"PF v167,v167,s57,s57\n"
"PF v175,v175,s57,s57\n"
"WL 9\n"
"v_or_b32 v156, v156, v157\n"
"v_or_b32 v154, v154, v155\n"
"v_or_b32 v152, v152, v153\n"
"RC v167,0\n"
"PF v54,v156,v156,v54\n"
"PF v53,v154,v154,v53\n"
"PF v52,v152,v152,v52\n"
"GA v170\n"
"GA v171\n"
"RC v175,0\n"
"MB v[176:191],a[248:251],v[116:119]\n"
"GL v170\n"
"GH v171\n"
"LD a[248:251],v1,22528\n"
"RC v167,1\n"
"GA v172\n"
"GA v173\n"
"RC v175,1\n"
"MB v[176:191],a[252:255],v[124:127]\n"
"GL v172\n"
"GH v173\n"
"LD a[252:255],v1,23552\n"
"PM v166,v166,v167\n"
"PM v174,v174,v175\n"
"PF v51,v166,v166,v51\n"
"PF v55,v174,v174,v55\n"
"WL 7\n"
"v_or_b32 v164, v164, v165\n"
"v_or_b32 v162, v162, v163\n"
"v_or_b32 v160, v160, v161\n"
"PF v50,v164,v164,v50\n"
"PF v49,v162,v162,v49\n"
"GA v176\n"
"GA v177\n"
"CH v182,v182,v183\n"
"MZ v[128:143],a[232:235],v[16:19]\n"
"GL v176\n"
"GH v177\n"
"CH v190,v190,v191\n"
"PF v183,v182,v182,s56\n"
"PF v191,v190,v190,s56\n"
"PF v48,v160,v160,v48\n"
"GA v178\n"
"GA v179\n"
"PM v183,v183,v182\n"
"PM v191,v191,v190\n"
"MB v[128:143],a[236:239],v[24:27]\n"
"GL v178\n"
"GH v179\n"
"EX v183,0\n"
"GA v180\n"
"GA v181\n"
"EX v191,0\n"
"MB v[128:143],a[240:243],v[64:67]\n"
"GL v180\n"
"GH v181\n"
"EX v183,1\n"
"EX v191,1\n"
"GA v184\n"
"GA v185\n"
"WL 6\n"
"MB v[128:143],a[244:247],v[72:75]\n"
"GL v184\n"
"GH v185\n"
"PF v183,v183,s57,s57\n"
"PF v191,v191,s57,s57\n"
"v_or_b32 v172, v172, v173\n"
"v_or_b32 v170, v170, v171\n"
"v_or_b32 v168, v168, v169\n"
"RC v183,0\n"
"PF v54,v172,v172,v54\n"
"PF v53,v170,v170,v53\n"
"PF v52,v168,v168,v52\n"
"GA v186\n"
"GA v187\n"
"RC v191,0\n"
"MB v[128:143],a[248:251],v[96:99]\n"
"GL v186\n"
"GH v187\n"
"RC v183,1\n"
"GA v188\n"
"GA v189\n"
"RC v191,1\n"
"MB v[128:143],a[252:255],v[104:107]\n"
"GL v188\n"
"GH v189\n"
"PM v182,v182,v183\n"
"PM v190,v190,v191\n"
"PF v51,v182,v182,v51\n"
"PF v55,v190,v190,v55\n"
"AW a115,v182\n"
"AW a119,v190\n"
"WL 6\n"
"v_or_b32 v180, v180, v181\n"
"v_or_b32 v178, v178, v179\n"
"v_or_b32 v176, v176, v177\n"
"PF v50,v180,v180,v50\n"
"GA v128\n"
"GA v129\n"
"CH v134,v134,v135\n"
"s_add_u32 m0, s34, 8192\n"
"MZ v[144:159],a[232:235],v[20:23]\n"
"GL v128\n"
"GH v129\n"
"buffer_load_dwordx4 v12, s[36:39], 0 offen offset:0 lds\n"
"CH v142,v142,v143\n"
"PF v135,v134,v134,s56\n"
"PF v143,v142,v142,s56\n"
"PF v49,v178,v178,v49\n"
"PF v48,v176,v176,v48\n"
"GA v130\n"
"GA v131\n"
"AW a114,v180\n"
"AW a113,v178\n"
"AW a112,v176\n"
"PM v135,v135,v134\n"
"s_add_u32 m0, s34, 27936\n"
"v_mfma_f32_16x16x32_f16 v[48:51], v[48:51], v[56:59], 0\n"
"GL v130\n"
"GH v131\n"
"PM v143,v143,v142\n"
"MB v[144:159],a[236:239],v[28:31]\n"
"buffer_load_dwordx4 v12, s[36:39], 0 offen offset:32 lds\n"
"EX v135,0\n"
"GA v132\n"
"GA v133\n"
"EX v143,0\n"
"s_add_u32 m0, s34, 47680\n"
"v_pk_add_f32 v[50:51], v[48:49], v[50:51]\n"
"MB v[144:159],a[240:243],v[68:71]\n"
"GL v132\n"
"GH v133\n"
"buffer_load_dwordx4 v12, s[36:39], 0 offen offset:64 lds\n"
"v_add_f32_e64 v51, v50, v51\n"
"EX v135,1\n"
"EX v143,1\n"
"s_waitcnt vmcnt(18)\n"
"v_readfirstlane_b32 s60, v210\n"
"v_and_b32 v209, 0xe4, v211\n"
"v_and_b32_e64 v210, 8, v211\n"
"v_lshl_or_b32 v209, v210, 1, v209\n"
"v_and_b32 v210, 16, v211\n"
"v_lshrrev_b32 v210, 1, v210\n"
"s_mul_i32 s60, s60, 0x9e3779b1\n"
"s_add_u32 s60, s60, 0x7f4a7c15\n"
"s_add_u32 s68, s68, 71680\n"
"v_add3_u32 v209, v209, v210, s68\n"
"s_add_u32 s69, s68, 1280\n"
"s_cmp_eq_u32 s58, 3\n"
"s_cselect_b32 s68, s68, s69\n"
"v_add_u32_e64 v211, s68, v211\n"
"GA v136\n"
"GA v137\n"
"MB v[144:159],a[244:247],v[76:79]\n"
"s_waitcnt vmcnt(17)\n"
"ds_write_b32 v209, v208\n"
"s_waitcnt vmcnt(16)\n"
"ds_write_b32 v211, v212\n"
"GL v136\n"
"GH v137\n"
"buffer_load_dwordx4 a[44:47], v12, s[36:39], 0 offen offset:96\n"
"PF v135,v135,s57,s57\n"
"PF v143,v143,s57,s57\n"
"WL 4\n"
"v_or_b32 v188, v188, v189\n"
"v_or_b32 v186, v186, v187\n"
"v_or_b32 v184, v184, v185\n"
"RC v135,0\n"
"PF v54,v188,v188,v54\n"
"PF v53,v186,v186,v53\n"
"PF v52,v184,v184,v52\n"
"GA v138\n"
"GA v139\n"
"AW a118,v188\n"
"AW a117,v186\n"
"AW a116,v184\n"
"v_mfma_f32_16x16x32_f16 v[52:55], v[52:55], v[56:59], 0\n"
"GL v138\n"
"GH v139\n"
"RC v143,0\n"
"MB v[144:159],a[248:251],v[100:103]\n"
"buffer_load_dwordx4 a[64:67], v12, s[36:39], 0 offen offset:128\n"
"RC v135,1\n"
"GA v140\n"
"GA v141\n"
"RC v143,1\n"
"v_pk_add_f32 v[54:55], v[52:53], v[54:55]\n"
"v_add_f32_e64 v52, v54, v55\n"
"MB v[144:159],a[252:255],v[108:111]\n"
"GL v140\n"
"GH v141\n"
"buffer_load_dwordx4 a[84:87], v12, s[36:39], 0 offen offset:160\n"
"v_permlane16_swap_b32 v51, v52\n"
"v_add_f32 v62, v51, v52\n"
"PM v134,v134,v135\n"
"PM v142,v142,v143\n"
"PM v195,v134,v134\n"
"PM v199,v142,v142\n"
"v_or_b32 v132, v132, v133\n"
"v_or_b32 v130, v130, v131\n"
"v_or_b32_e64 v128, v128, v129\n"
"GA v144\n"
"GA v145\n"
"CH v150,v150,v151\n"
"MZ v[160:175],a[232:235],v[32:35]\n"
"GL v144\n"
"GH v145\n"
"CH v158,v158,v159\n"
"PF v151,v150,v150,s56\n"
"PF v159,v158,v158,s56\n"
"PM v194,v132,v132\n"
"PM v193,v130,v130\n"
"PM v192,v128,v128\n"
"GA v146\n"
"GA v147\n"
"PM v151,v151,v150\n"
"PM v159,v159,v158\n"
"MB v[160:175],a[236:239],v[40:43]\n"
"GL v146\n"
"GH v147\n"
"EX v151,0\n"
"GA v148\n"
"GA v149\n"
"EX v159,0\n"
"MB v[160:175],a[240:243],v[80:83]\n"
"GL v148\n"
"GH v149\n"
"EX v151,1\n"
"EX v159,1\n"
"GA v152\n"
"GA v153\n"
"MB v[160:175],a[244:247],v[88:91]\n"
"GL v152\n"
"GH v153\n"
"PF v151,v151,s57,s57\n"
"PF v159,v159,s57,s57\n"
"WL 7\n"
"v_or_b32 v140, v140, v141\n"
"v_or_b32 v138, v138, v139\n"
"v_or_b32 v136, v136, v137\n"
"RC v151,0\n"
"PM v198,v140,v140\n"
"PM v197,v138,v138\n"
"PM v196,v136,v136\n"
"GA v154\n"
"GA v155\n"
"RC v159,0\n"
"MB v[160:175],a[248:251],v[112:115]\n"
"GL v154\n"
"GH v155\n"
"RC v151,1\n"
"GA v156\n"
"GA v157\n"
"RC v159,1\n"
"MB v[160:175],a[252:255],v[120:123]\n"
"GL v156\n"
"GH v157\n"
"PM v150,v150,v151\n"
"PM v158,v158,v159\n"
"PF v195,v150,v150,v195\n"
"PF v199,v158,v158,v199\n"
"WL 6\n"
"v_or_b32 v148, v148, v149\n"
"v_or_b32 v146, v146, v147\n"
"v_or_b32 v144, v144, v145\n"
"PF v194,v148,v148,v194\n"
"PF v193,v146,v146,v193\n"
"PF v192,v144,v144,v192\n"
"GA v160\n"
"GA v161\n"
"CH v166,v166,v167\n"
"MZ v[176:191],a[232:235],v[36:39]\n"
"GL v160\n"
"GH v161\n"
"LD a[232:235],v1,24576\n"
"CH v174,v174,v175\n"
"PF v167,v166,v166,s56\n"
"PF v175,v174,v174,s56\n"
"GA v162\n"
"GA v163\n"
"PM v167,v167,v166\n"
"PM v175,v175,v174\n"
"MB v[176:191],a[236:239],v[44:47]\n"
"GL v162\n"
"GH v163\n"
"LD a[236:239],v1,25600\n"
"EX v167,0\n"
"GA v164\n"
"GA v165\n"
"EX v175,0\n"
"MB v[176:191],a[240:243],v[84:87]\n"
"GL v164\n"
"GH v165\n"
"LD a[240:243],v1,26624\n"
"EX v167,1\n"
"EX v175,1\n"
"GA v168\n"
"GA v169\n"
"MB v[176:191],a[244:247],v[92:95]\n"
"GL v168\n"
"GH v169\n"
"LD a[244:247],v1,27648\n"
"PF v167,v167,s57,s57\n"
"PF v175,v175,s57,s57\n"
"WL 10\n"
"v_or_b32 v156, v156, v157\n"
"v_or_b32 v154, v154, v155\n"
"v_or_b32 v152, v152, v153\n"
"RC v167,0\n"
"PF v198,v156,v156,v198\n"
"PF v197,v154,v154,v197\n"
"PF v196,v152,v152,v196\n"
"GA v170\n"
"GA v171\n"
"RC v175,0\n"
"MB v[176:191],a[248:251],v[116:119]\n"
"GL v170\n"
"GH v171\n"
"LD a[248:251],v1,28672\n"
"RC v167,1\n"
"GA v172\n"
"GA v173\n"
"RC v175,1\n"
"MB v[176:191],a[252:255],v[124:127]\n"
"GL v172\n"
"GH v173\n"
"LD a[252:255],v1,29696\n"
"PM v166,v166,v167\n"
"PM v174,v174,v175\n"
"PF v195,v166,v166,v195\n"
"PF v199,v174,v174,v199\n"
"WL 7\n"
"v_or_b32 v164, v164, v165\n"
"v_or_b32 v162, v162, v163\n"
"v_or_b32 v160, v160, v161\n"
"PF v194,v164,v164,v194\n"
"PF v193,v162,v162,v193\n"
"GA v176\n"
"GA v177\n"
"CH v182,v182,v183\n"
"MZ v[128:143],a[232:235],v[16:19]\n"
"GL v176\n"
"GH v177\n"
"CH v190,v190,v191\n"
"PF v183,v182,v182,s56\n"
"PF v191,v190,v190,s56\n"
"PF v192,v160,v160,v192\n"
"GA v178\n"
"GA v179\n"
"PM v183,v183,v182\n"
"PM v191,v191,v190\n"
"MB v[128:143],a[236:239],v[24:27]\n"
"GL v178\n"
"GH v179\n"
"EX v183,0\n"
"GA v180\n"
"GA v181\n"
"EX v191,0\n"
"MB v[128:143],a[240:243],v[64:67]\n"
"GL v180\n"
"GH v181\n"
"EX v183,1\n"
"EX v191,1\n"
"GA v184\n"
"GA v185\n"
"WL 6\n"
"MB v[128:143],a[244:247],v[72:75]\n"
"GL v184\n"
"GH v185\n"
"PF v183,v183,s57,s57\n"
"PF v191,v191,s57,s57\n"
"v_or_b32 v172, v172, v173\n"
"v_or_b32 v170, v170, v171\n"
"v_or_b32 v168, v168, v169\n"
"RC v183,0\n"
"PF v198,v172,v172,v198\n"
"PF v197,v170,v170,v197\n"
"PF v196,v168,v168,v196\n"
"GA v186\n"
"GA v187\n"
"RC v191,0\n"
"MB v[128:143],a[248:251],v[96:99]\n"
"GL v186\n"
"GH v187\n"
"RC v183,1\n"
"GA v188\n"
"GA v189\n"
"RC v191,1\n"
"MB v[128:143],a[252:255],v[104:107]\n"
"GL v188\n"
"GH v189\n"
"PM v182,v182,v183\n"
"PM v190,v190,v191\n"
"PF v195,v182,v182,v195\n"
"PF v199,v190,v190,v199\n"
"AW a123,v182\n"
"AW a127,v190\n"
"WL 6\n"
"v_or_b32 v180, v180, v181\n"
"v_or_b32 v178, v178, v179\n"
"v_or_b32 v176, v176, v177\n"
"PF v194,v180,v180,v194\n"
"GA v128\n"
"GA v129\n"
"CH v134,v134,v135\n"
"MZ v[144:159],a[232:235],v[20:23]\n"
"GL v128\n"
"GH v129\n"
"CH v142,v142,v143\n"
"PF v135,v134,v134,s56\n"
"PF v143,v142,v142,s56\n"
"PF v193,v178,v178,v193\n"
"PF v192,v176,v176,v192\n"
"GA v130\n"
"GA v131\n"
"AW a122,v180\n"
"AW a121,v178\n"
"AW a120,v176\n"
"PM v135,v135,v134\n"
"v_mfma_f32_16x16x32_f16 v[192:195], v[192:195], v[56:59], 0\n"
"GL v130\n"
"GH v131\n"
"PM v143,v143,v142\n"
"MB v[144:159],a[236:239],v[28:31]\n"
"EX v135,0\n"
"GA v132\n"
"GA v133\n"
"EX v143,0\n"
"EX v135,1\n"
"EX v143,1\n"
"v_pk_add_f32 v[194:195], v[192:193], v[194:195]\n"
"MB v[144:159],a[240:243],v[68:71]\n"
"GL v132\n"
"GH v133\n"
"v_add_f32_e64 v195, v194, v195\n"
"GA v136\n"
"GA v137\n"
"PF v135,v135,s57,s57\n"
"MB v[144:159],a[244:247],v[76:79]\n"
"GL v136\n"
"GH v137\n"
"PF v143,v143,s57,s57\n"
"WL 6\n"
"v_or_b32 v188, v188, v189\n"
"v_or_b32 v186, v186, v187\n"
"v_or_b32 v184, v184, v185\n"
"RC v135,0\n"
"PF v198,v188,v188,v198\n"
"PF v197,v186,v186,v197\n"
"PF v196,v184,v184,v196\n"
"GA v138\n"
"GA v139\n"
"AW a126,v188\n"
"AW a125,v186\n"
"AW a124,v184\n"
"v_mfma_f32_16x16x32_f16 v[196:199], v[196:199], v[56:59], 0\n"
"GL v138\n"
"GH v139\n"
"RC v143,0\n"
"MB v[144:159],a[248:251],v[100:103]\n"
"RC v135,1\n"
"GA v140\n"
"GA v141\n"
"RC v143,1\n"
"PM v134,v134,v135\n"
"PM v142,v142,v143\n"
"PM v51,v134,v134\n"
"v_pk_add_f32 v[198:199], v[196:197], v[198:199]\n"
"v_add_f32_e64 v196, v198, v199\n"
"MB v[144:159],a[252:255],v[108:111]\n"
"GL v140\n"
"GH v141\n"
"v_permlane16_swap_b32 v195, v196\n"
"v_add_f32 v196, v195, v196\n"
"PM v55,v142,v142\n"
"WL 4\n"
"v_or_b32 v132, v132, v133\n"
"v_permlane32_swap_b32 v62, v196\n"
"v_add_f32 v4, v62, v196\n"
"v_or_b32 v130, v130, v131\n"
"v_or_b32 v128, v128, v129\n"
"PM v50,v132,v132\n"
"PM v49,v130,v130\n"
"GA v144\n"
"GA v145\n"
"CH v150,v150,v151\n"
"s_add_u32 m0, s34, 9216\n"
"MZ v[160:175],a[232:235],v[32:35]\n"
"GL v144\n"
"GH v145\n"
"buffer_load_dwordx4 v13, s[36:39], 0 offen offset:0 lds\n"
"CH v158,v158,v159\n"
"PF v151,v150,v150,s56\n"
"PF v159,v158,v158,s56\n"
"PM v48,v128,v128\n"
"GA v146\n"
"GA v147\n"
"PM v151,v151,v150\n"
"s_add_u32 m0, s34, 28960\n"
"PM v159,v159,v158\n"
"MB v[160:175],a[236:239],v[40:43]\n"
"GL v146\n"
"GH v147\n"
"buffer_load_dwordx4 v13, s[36:39], 0 offen offset:32 lds\n"
"EX v151,0\n"
"GA v148\n"
"GA v149\n"
"EX v159,0\n"
"s_add_u32 m0, s34, 48704\n"
"MB v[160:175],a[240:243],v[80:83]\n"
"GL v148\n"
"GH v149\n"
"buffer_load_dwordx4 v13, s[36:39], 0 offen offset:64 lds\n"
"EX v151,1\n"
"EX v159,1\n"
"GA v152\n"
"GA v153\n"
"MB v[160:175],a[244:247],v[88:91]\n"
"GL v152\n"
"GH v153\n"
"buffer_load_dwordx4 a[48:51], v13, s[36:39], 0 offen offset:96\n"
"PF v151,v151,s57,s57\n"
"PF v159,v159,s57,s57\n"
"WL 6\n"
"v_or_b32 v140, v140, v141\n"
"v_or_b32 v138, v138, v139\n"
"v_or_b32 v136, v136, v137\n"
"RC v151,0\n"
"PM v54,v140,v140\n"
"PM v53,v138,v138\n"
"PM v52,v136,v136\n"
"GA v154\n"
"GA v155\n"
"RC v159,0\n"
"MB v[160:175],a[248:251],v[112:115]\n"
"GL v154\n"
"GH v155\n"
"buffer_load_dwordx4 a[68:71], v13, s[36:39], 0 offen offset:128\n"
"RC v151,1\n"
"GA v156\n"
"GA v157\n"
"RC v159,1\n"
"MB v[160:175],a[252:255],v[120:123]\n"
"GL v156\n"
"GH v157\n"
"buffer_load_dwordx4 a[88:91], v13, s[36:39], 0 offen offset:160\n"
"PM v150,v150,v151\n"
"PM v158,v158,v159\n"
"PF v51,v150,v150,v51\n"
"PF v55,v158,v158,v55\n"
"WL 4\n"
"v_or_b32 v148, v148, v149\n"
"v_or_b32 v146, v146, v147\n"
"v_or_b32 v144, v144, v145\n"
"PF v50,v148,v148,v50\n"
"PF v49,v146,v146,v49\n"
"GA v160\n"
"GA v161\n"
"CH v166,v166,v167\n"
"MZ v[176:191],a[232:235],v[36:39]\n"
"GL v160\n"
"GH v161\n"
"LD a[232:235],v1,30720\n"
"CH v174,v174,v175\n"
"PF v167,v166,v166,s56\n"
"PF v175,v174,v174,s56\n"
"PF v48,v144,v144,v48\n"
"GA v162\n"
"GA v163\n"
"PM v167,v167,v166\n"
"PM v175,v175,v174\n"
"MB v[176:191],a[236:239],v[44:47]\n"
"GL v162\n"
"GH v163\n"
"LD a[236:239],v1,31744\n"
"EX v167,0\n"
"GA v164\n"
"GA v165\n"
"EX v175,0\n"
"MB v[176:191],a[240:243],v[84:87]\n"
"GL v164\n"
"GH v165\n"
"LD a[240:243],v1,32768\n"
"EX v167,1\n"
"EX v175,1\n"
"GA v168\n"
"GA v169\n"
"MB v[176:191],a[244:247],v[92:95]\n"
"GL v168\n"
"GH v169\n"
"LD a[244:247],v1,33792\n"
"PF v167,v167,s57,s57\n"
"PF v175,v175,s57,s57\n"
"WL 9\n"
"v_or_b32 v156, v156, v157\n"
"v_or_b32 v154, v154, v155\n"
"v_or_b32 v152, v152, v153\n"
"RC v167,0\n"
"PF v54,v156,v156,v54\n"
"PF v53,v154,v154,v53\n"
"PF v52,v152,v152,v52\n"
"GA v170\n"
"GA v171\n"
"RC v175,0\n"
"MB v[176:191],a[248:251],v[116:119]\n"
"GL v170\n"
"GH v171\n"
"LD a[248:251],v1,34816\n"
"RC v167,1\n"
"GA v172\n"
"GA v173\n"
"RC v175,1\n"
"MB v[176:191],a[252:255],v[124:127]\n"
"GL v172\n"
"GH v173\n"
"LD a[252:255],v1,35840\n"
"PM v166,v166,v167\n"
"PM v174,v174,v175\n"
"PF v51,v166,v166,v51\n"
"PF v55,v174,v174,v55\n"
"WL 7\n"
"v_or_b32 v164, v164, v165\n"
"v_or_b32 v162, v162, v163\n"
"v_or_b32 v160, v160, v161\n"
"PF v50,v164,v164,v50\n"
"PF v49,v162,v162,v49\n"
"GA v176\n"
"GA v177\n"
"CH v182,v182,v183\n"
"MZ v[128:143],a[232:235],v[16:19]\n"
"GL v176\n"
"GH v177\n"
"CH v190,v190,v191\n"
"PF v183,v182,v182,s56\n"
"PF v191,v190,v190,s56\n"
"PF v48,v160,v160,v48\n"
"GA v178\n"
"GA v179\n"
"PM v183,v183,v182\n"
"PM v191,v191,v190\n"
"MB v[128:143],a[236:239],v[24:27]\n"
"GL v178\n"
"GH v179\n"
"EX v183,0\n"
"GA v180\n"
"GA v181\n"
"EX v191,0\n"
"MB v[128:143],a[240:243],v[64:67]\n"
"GL v180\n"
"GH v181\n"
"EX v183,1\n"
"EX v191,1\n"
"GA v184\n"
"GA v185\n"
"WL 6\n"
"MB v[128:143],a[244:247],v[72:75]\n"
"GL v184\n"
"GH v185\n"
"PF v183,v183,s57,s57\n"
"PF v191,v191,s57,s57\n"
"v_or_b32 v172, v172, v173\n"
"v_or_b32 v170, v170, v171\n"
"v_or_b32 v168, v168, v169\n"
"RC v183,0\n"
"PF v54,v172,v172,v54\n"
"PF v53,v170,v170,v53\n"
"PF v52,v168,v168,v52\n"
"GA v186\n"
"GA v187\n"
"RC v191,0\n"
"MB v[128:143],a[248:251],v[96:99]\n"
"GL v186\n"
"GH v187\n"
"RC v183,1\n"
"GA v188\n"
"GA v189\n"
"RC v191,1\n"
"MB v[128:143],a[252:255],v[104:107]\n"
"GL v188\n"
"GH v189\n"
"PM v182,v182,v183\n"
"PM v190,v190,v191\n"
"PF v51,v182,v182,v51\n"
"PF v55,v190,v190,v55\n"
"AW a131,v182\n"
"AW a135,v190\n"
"WL 6\n"
"v_or_b32 v180, v180, v181\n"
"v_or_b32 v178, v178, v179\n"
"v_or_b32 v176, v176, v177\n"
"PF v50,v180,v180,v50\n"
"GA v128\n"
"GA v129\n"
"CH v134,v134,v135\n"
"MZ v[144:159],a[232:235],v[20:23]\n"
"GL v128\n"
"GH v129\n"
"CH v142,v142,v143\n"
"PF v135,v134,v134,s56\n"
"PF v143,v142,v142,s56\n"
"PF v49,v178,v178,v49\n"
"PF v48,v176,v176,v48\n"
"GA v130\n"
"GA v131\n"
"AW a130,v180\n"
"AW a129,v178\n"
"AW a128,v176\n"
"PM v135,v135,v134\n"
"v_mfma_f32_16x16x32_f16 v[48:51], v[48:51], v[56:59], 0\n"
"GL v130\n"
"GH v131\n"
"PM v143,v143,v142\n"
"MB v[144:159],a[236:239],v[28:31]\n"
"EX v135,0\n"
"GA v132\n"
"GA v133\n"
"EX v143,0\n"
"EX v135,1\n"
"EX v143,1\n"
"v_pk_add_f32 v[50:51], v[48:49], v[50:51]\n"
"MB v[144:159],a[240:243],v[68:71]\n"
"GL v132\n"
"GH v133\n"
"v_add_f32_e64 v51, v50, v51\n"
"GA v136\n"
"GA v137\n"
"PF v135,v135,s57,s57\n"
"MB v[144:159],a[244:247],v[76:79]\n"
"GL v136\n"
"GH v137\n"
"PF v143,v143,s57,s57\n"
"WL 6\n"
"v_or_b32 v188, v188, v189\n"
"v_or_b32 v186, v186, v187\n"
"v_or_b32 v184, v184, v185\n"
"RC v135,0\n"
"PF v54,v188,v188,v54\n"
"PF v53,v186,v186,v53\n"
"PF v52,v184,v184,v52\n"
"GA v138\n"
"GA v139\n"
"AW a134,v188\n"
"AW a133,v186\n"
"AW a132,v184\n"
"v_mfma_f32_16x16x32_f16 v[52:55], v[52:55], v[56:59], 0\n"
"GL v138\n"
"GH v139\n"
"RC v143,0\n"
"MB v[144:159],a[248:251],v[100:103]\n"
"RC v135,1\n"
"GA v140\n"
"GA v141\n"
"RC v143,1\n"
"PM v134,v134,v135\n"
"PM v142,v142,v143\n"
"PM v195,v134,v134\n"
"v_pk_add_f32 v[54:55], v[52:53], v[54:55]\n"
"v_add_f32_e64 v52, v54, v55\n"
"MB v[144:159],a[252:255],v[108:111]\n"
"GL v140\n"
"GH v141\n"
"v_permlane16_swap_b32 v51, v52\n"
"v_add_f32 v62, v51, v52\n"
"PM v199,v142,v142\n"
"WL 4\n"
"v_or_b32 v132, v132, v133\n"
"v_or_b32 v130, v130, v131\n"
"v_or_b32 v128, v128, v129\n"
"PM v194,v132,v132\n"
"PM v193,v130,v130\n"
"PM v192,v128,v128\n"
"GA v144\n"
"GA v145\n"
"CH v150,v150,v151\n"
"MZ v[160:175],a[232:235],v[32:35]\n"
"GL v144\n"
"GH v145\n"
"CH v158,v158,v159\n"
"PF v151,v150,v150,s56\n"
"PF v159,v158,v158,s56\n"
"GA v146\n"
"GA v147\n"
"PM v151,v151,v150\n"
"PM v159,v159,v158\n"
"MB v[160:175],a[236:239],v[40:43]\n"
"GL v146\n"
"GH v147\n"
"EX v151,0\n"
"GA v148\n"
"GA v149\n"
"EX v159,0\n"
"MB v[160:175],a[240:243],v[80:83]\n"
"GL v148\n"
"GH v149\n"
"EX v151,1\n"
"EX v159,1\n"
"GA v152\n"
"GA v153\n"
"MB v[160:175],a[244:247],v[88:91]\n"
"GL v152\n"
"GH v153\n"
"PF v151,v151,s57,s57\n"
"PF v159,v159,s57,s57\n"
"WL 8\n"
"v_or_b32 v140, v140, v141\n"
"v_or_b32 v138, v138, v139\n"
"v_or_b32 v136, v136, v137\n"
"RC v151,0\n"
"PM v198,v140,v140\n"
"PM v197,v138,v138\n"
"PM v196,v136,v136\n"
"GA v154\n"
"GA v155\n"
"RC v159,0\n"
"MB v[160:175],a[248:251],v[112:115]\n"
"GL v154\n"
"GH v155\n"
"RC v151,1\n"
"GA v156\n"
"GA v157\n"
"RC v159,1\n"
"MB v[160:175],a[252:255],v[120:123]\n"
"GL v156\n"
"GH v157\n"
"PM v150,v150,v151\n"
"PM v158,v158,v159\n"
"PF v195,v150,v150,v195\n"
"PF v199,v158,v158,v199\n"
"WL 6\n"
"v_or_b32 v148, v148, v149\n"
"v_or_b32 v146, v146, v147\n"
"v_or_b32 v144, v144, v145\n"
"PF v194,v148,v148,v194\n"
"PF v193,v146,v146,v193\n"
"PF v192,v144,v144,v192\n"
"GA v160\n"
"GA v161\n"
"CH v166,v166,v167\n"
"MZ v[176:191],a[232:235],v[36:39]\n"
"GL v160\n"
"GH v161\n"
"LD a[232:235],v1,36864\n"
"CH v174,v174,v175\n"
"PF v167,v166,v166,s56\n"
"PF v175,v174,v174,s56\n"
"GA v162\n"
"GA v163\n"
"PM v167,v167,v166\n"
"PM v175,v175,v174\n"
"MB v[176:191],a[236:239],v[44:47]\n"
"GL v162\n"
"GH v163\n"
"LD a[236:239],v1,37888\n"
"EX v167,0\n"
"GA v164\n"
"GA v165\n"
"EX v175,0\n"
"MB v[176:191],a[240:243],v[84:87]\n"
"GL v164\n"
"GH v165\n"
"LD a[240:243],v1,38912\n"
"EX v167,1\n"
"EX v175,1\n"
"GA v168\n"
"GA v169\n"
"MB v[176:191],a[244:247],v[92:95]\n"
"GL v168\n"
"GH v169\n"
"LD a[244:247],v1,39936\n"
"PF v167,v167,s57,s57\n"
"PF v175,v175,s57,s57\n"
"WL 10\n"
"v_or_b32 v156, v156, v157\n"
"v_or_b32 v154, v154, v155\n"
"v_or_b32 v152, v152, v153\n"
"RC v167,0\n"
"PF v198,v156,v156,v198\n"
"PF v197,v154,v154,v197\n"
"PF v196,v152,v152,v196\n"
"GA v170\n"
"GA v171\n"
"RC v175,0\n"
"MB v[176:191],a[248:251],v[116:119]\n"
"GL v170\n"
"GH v171\n"
"LD a[248:251],v1,40960\n"
"RC v167,1\n"
"GA v172\n"
"GA v173\n"
"RC v175,1\n"
"MB v[176:191],a[252:255],v[124:127]\n"
"GL v172\n"
"GH v173\n"
"LD a[252:255],v1,41984\n"
"PM v166,v166,v167\n"
"PM v174,v174,v175\n"
"PF v195,v166,v166,v195\n"
"PF v199,v174,v174,v199\n"
"WL 7\n"
"v_or_b32 v164, v164, v165\n"
"v_or_b32 v162, v162, v163\n"
"v_or_b32 v160, v160, v161\n"
"PF v194,v164,v164,v194\n"
"PF v193,v162,v162,v193\n"
"GA v176\n"
"GA v177\n"
"CH v182,v182,v183\n"
"s_add_u32 m0, s35, 6144\n"
"MZ v[128:143],a[232:235],v[16:19]\n"
"GL v176\n"
"GH v177\n"
"buffer_load_dwordx4 v15, s[36:39], 0 offen offset:0 lds\n"
"CH v190,v190,v191\n"
"PF v183,v182,v182,s56\n"
"PF v191,v190,v190,s56\n"
"PF v192,v160,v160,v192\n"
"GA v178\n"
"GA v179\n"
"PM v183,v183,v182\n"
"s_add_u32 m0, s35, 25888\n"
"PM v191,v191,v190\n"
"MB v[128:143],a[236:239],v[24:27]\n"
"GL v178\n"
"GH v179\n"
"buffer_load_dwordx4 v15, s[36:39], 0 offen offset:32 lds\n"
"EX v183,0\n"
"GA v180\n"
"GA v181\n"
"EX v191,0\n"
"s_add_u32 m0, s35, 45632\n"
"MB v[128:143],a[240:243],v[64:67]\n"
"GL v180\n"
"GH v181\n"
"buffer_load_dwordx4 v15, s[36:39], 0 offen offset:64 lds\n"
"EX v183,1\n"
"EX v191,1\n"
"GA v184\n"
"GA v185\n"
"WL 6\n"
"MB v[128:143],a[244:247],v[72:75]\n"
"GL v184\n"
"GH v185\n"
"buffer_load_dwordx4 a[52:55], v15, s[36:39], 0 offen offset:96\n"
"PF v183,v183,s57,s57\n"
"PF v191,v191,s57,s57\n"
"v_or_b32 v172, v172, v173\n"
"v_or_b32 v170, v170, v171\n"
"v_or_b32 v168, v168, v169\n"
"RC v183,0\n"
"PF v198,v172,v172,v198\n"
"PF v197,v170,v170,v197\n"
"PF v196,v168,v168,v196\n"
"GA v186\n"
"GA v187\n"
"RC v191,0\n"
"MB v[128:143],a[248:251],v[96:99]\n"
"GL v186\n"
"GH v187\n"
"buffer_load_dwordx4 a[72:75], v15, s[36:39], 0 offen offset:128\n"
"RC v183,1\n"
"GA v188\n"
"GA v189\n"
"RC v191,1\n"
"MB v[128:143],a[252:255],v[104:107]\n"
"GL v188\n"
"GH v189\n"
"buffer_load_dwordx4 a[92:95], v15, s[36:39], 0 offen offset:160\n"
"PM v182,v182,v183\n"
"PM v190,v190,v191\n"
"PF v195,v182,v182,v195\n"
"PF v199,v190,v190,v199\n"
"AW a139,v182\n"
"AW a143,v190\n"
"WL 4\n"
"v_or_b32 v180, v180, v181\n"
"v_or_b32 v178, v178, v179\n"
"v_or_b32 v176, v176, v177\n"
"GA v128\n"
"GA v129\n"
"CH v134,v134,v135\n"
"MZ v[144:159],a[232:235],v[20:23]\n"
"GL v128\n"
"GH v129\n"
"CH v142,v142,v143\n"
"PF v135,v134,v134,s56\n"
"PF v143,v142,v142,s56\n"
"PF v194,v180,v180,v194\n"
"PF v193,v178,v178,v193\n"
"PF v192,v176,v176,v192\n"
"GA v130\n"
"GA v131\n"
"AW a138,v180\n"
"AW a137,v178\n"
"AW a136,v176\n"
"PM v135,v135,v134\n"
"v_mfma_f32_16x16x32_f16 v[192:195], v[192:195], v[56:59], 0\n"
"GL v130\n"
"GH v131\n"
"PM v143,v143,v142\n"
"MB v[144:159],a[236:239],v[28:31]\n"
"EX v135,0\n"
"GA v132\n"
"GA v133\n"
"EX v143,0\n"
"EX v135,1\n"
"EX v143,1\n"
"v_pk_add_f32 v[194:195], v[192:193], v[194:195]\n"
"MB v[144:159],a[240:243],v[68:71]\n"
"GL v132\n"
"GH v133\n"
"v_add_f32_e64 v195, v194, v195\n"
"GA v136\n"
"GA v137\n"
"PF v135,v135,s57,s57\n"
"MB v[144:159],a[244:247],v[76:79]\n"
"GL v136\n"
"GH v137\n"
"PF v143,v143,s57,s57\n"
"WL 6\n"
"v_or_b32 v188, v188, v189\n"
"v_or_b32 v186, v186, v187\n"
"v_or_b32 v184, v184, v185\n"
"RC v135,0\n"
"PF v198,v188,v188,v198\n"
"PF v197,v186,v186,v197\n"
"PF v196,v184,v184,v196\n"
"GA v138\n"
"GA v139\n"
"AW a142,v188\n"
"AW a141,v186\n"
"AW a140,v184\n"
"v_mfma_f32_16x16x32_f16 v[196:199], v[196:199], v[56:59], 0\n"
"GL v138\n"
"GH v139\n"
"RC v143,0\n"
"MB v[144:159],a[248:251],v[100:103]\n"
"RC v135,1\n"
"GA v140\n"
"GA v141\n"
"RC v143,1\n"
"PM v134,v134,v135\n"
"PM v142,v142,v143\n"
"PM v51,v134,v134\n"
"v_pk_add_f32 v[198:199], v[196:197], v[198:199]\n"
"v_add_f32_e64 v196, v198, v199\n"
"MB v[144:159],a[252:255],v[108:111]\n"
"GL v140\n"
"GH v141\n"
"v_permlane16_swap_b32 v195, v196\n"
"v_add_f32 v196, v195, v196\n"
"PM v55,v142,v142\n"
"WL 4\n"
"v_or_b32 v132, v132, v133\n"
"v_permlane32_swap_b32 v62, v196\n"
"v_add_f32 v5, v62, v196\n"
"v_or_b32 v130, v130, v131\n"
"v_or_b32 v128, v128, v129\n"
"PM v50,v132,v132\n"
"PM v49,v130,v130\n"
"GA v144\n"
"GA v145\n"
"CH v150,v150,v151\n"
"MZ v[160:175],a[232:235],v[32:35]\n"
"GL v144\n"
"GH v145\n"
"CH v158,v158,v159\n"
"PF v151,v150,v150,s56\n"
"PF v159,v158,v158,s56\n"
"PM v48,v128,v128\n"
"GA v146\n"
"GA v147\n"
"PM v151,v151,v150\n"
"PM v159,v159,v158\n"
"MB v[160:175],a[236:239],v[40:43]\n"
"GL v146\n"
"GH v147\n"
"buffer_load_dword v228, v0, s[24:27], s28 offen\n"
"EX v151,0\n"
"GA v148\n"
"GA v149\n"
"EX v159,0\n"
"MB v[160:175],a[240:243],v[80:83]\n"
"GL v148\n"
"GH v149\n"
"EX v151,1\n"
"EX v159,1\n"
"GA v152\n"
"GA v153\n"
"MB v[160:175],a[244:247],v[88:91]\n"
"GL v152\n"
"GH v153\n"
"PF v151,v151,s57,s57\n"
"PF v159,v159,s57,s57\n"
"WL 8\n"
"v_or_b32 v140, v140, v141\n"
"v_or_b32 v138, v138, v139\n"
"v_or_b32 v136, v136, v137\n"
"RC v151,0\n"
"PM v54,v140,v140\n"
"PM v53,v138,v138\n"
"PM v52,v136,v136\n"
"GA v154\n"
"GA v155\n"
"RC v159,0\n"
"MB v[160:175],a[248:251],v[112:115]\n"
"GL v154\n"
"GH v155\n"
"RC v151,1\n"
"GA v156\n"
"GA v157\n"
"RC v159,1\n"
"MB v[160:175],a[252:255],v[120:123]\n"
"GL v156\n"
"GH v157\n"
"PM v150,v150,v151\n"
"PM v158,v158,v159\n"
"PF v51,v150,v150,v51\n"
"PF v55,v158,v158,v55\n"
"WL 6\n"
"v_or_b32 v148, v148, v149\n"
"v_or_b32 v146, v146, v147\n"
"v_or_b32 v144, v144, v145\n"
"PF v50,v148,v148,v50\n"
"PF v49,v146,v146,v49\n"
"PF v48,v144,v144,v48\n"
"GA v160\n"
"GA v161\n"
"CH v166,v166,v167\n"
"MZ v[176:191],a[232:235],v[36:39]\n"
"GL v160\n"
"GH v161\n"
"LD a[232:235],v1,43008\n"
"CH v174,v174,v175\n"
"PF v167,v166,v166,s56\n"
"PF v175,v174,v174,s56\n"
"GA v162\n"
"GA v163\n"
"PM v167,v167,v166\n"
"PM v175,v175,v174\n"
"MB v[176:191],a[236:239],v[44:47]\n"
"GL v162\n"
"GH v163\n"
"LD a[236:239],v1,44032\n"
"EX v167,0\n"
"GA v164\n"
"GA v165\n"
"EX v175,0\n"
"MB v[176:191],a[240:243],v[84:87]\n"
"GL v164\n"
"GH v165\n"
"LD a[240:243],v1,45056\n"
"EX v167,1\n"
"EX v175,1\n"
"GA v168\n"
"GA v169\n"
"MB v[176:191],a[244:247],v[92:95]\n"
"GL v168\n"
"GH v169\n"
"LD a[244:247],v1,46080\n"
"PF v167,v167,s57,s57\n"
"PF v175,v175,s57,s57\n"
"WL 10\n"
"v_or_b32 v156, v156, v157\n"
"v_or_b32 v154, v154, v155\n"
"v_or_b32 v152, v152, v153\n"
"RC v167,0\n"
"PF v54,v156,v156,v54\n"
"PF v53,v154,v154,v53\n"
"PF v52,v152,v152,v52\n"
"GA v170\n"
"GA v171\n"
"RC v175,0\n"
"MB v[176:191],a[248:251],v[116:119]\n"
"GL v170\n"
"GH v171\n"
"LD a[248:251],v1,47104\n"
"RC v167,1\n"
"GA v172\n"
"GA v173\n"
"RC v175,1\n"
"MB v[176:191],a[252:255],v[124:127]\n"
"GL v172\n"
"GH v173\n"
"LD a[252:255],v1,48128\n"
"PM v166,v166,v167\n"
"PM v174,v174,v175\n"
"PF v51,v166,v166,v51\n"
"PF v55,v174,v174,v55\n"
"WL 7\n"
"v_or_b32 v164, v164, v165\n"
"v_or_b32 v162, v162, v163\n"
"v_or_b32 v160, v160, v161\n"
"PF v50,v164,v164,v50\n"
"PF v49,v162,v162,v49\n"
"GA v176\n"
"GA v177\n"
"CH v182,v182,v183\n"
"MZ v[128:143],a[232:235],v[16:19]\n"
"GL v176\n"
"GH v177\n"
"CH v190,v190,v191\n"
"PF v183,v182,v182,s56\n"
"PF v191,v190,v190,s56\n"
"PF v48,v160,v160,v48\n"
"GA v178\n"
"GA v179\n"
"PM v183,v183,v182\n"
"PM v191,v191,v190\n"
"MB v[128:143],a[236:239],v[24:27]\n"
"GL v178\n"
"GH v179\n"
"EX v183,0\n"
"GA v180\n"
"GA v181\n"
"EX v191,0\n"
"MB v[128:143],a[240:243],v[64:67]\n"
"GL v180\n"
"GH v181\n"
"EX v183,1\n"
"EX v191,1\n"
"GA v184\n"
"GA v185\n"
"WL 6\n"
"MB v[128:143],a[244:247],v[72:75]\n"
"GL v184\n"
"GH v185\n"
"PF v183,v183,s57,s57\n"
"PF v191,v191,s57,s57\n"
"v_or_b32 v172, v172, v173\n"
"v_or_b32 v170, v170, v171\n"
"v_or_b32 v168, v168, v169\n"
"RC v183,0\n"
"PF v54,v172,v172,v54\n"
"PF v53,v170,v170,v53\n"
"PF v52,v168,v168,v52\n"
"GA v186\n"
"GA v187\n"
"RC v191,0\n"
"MB v[128:143],a[248:251],v[96:99]\n"
"GL v186\n"
"GH v187\n"
"RC v183,1\n"
"GA v188\n"
"GA v189\n"
"RC v191,1\n"
"MB v[128:143],a[252:255],v[104:107]\n"
"GL v188\n"
"GH v189\n"
"PM v182,v182,v183\n"
"PM v190,v190,v191\n"
"PF v51,v182,v182,v51\n"
"PF v55,v190,v190,v55\n"
"AW a147,v182\n"
"AW a151,v190\n"
"WL 6\n"
"v_or_b32 v180, v180, v181\n"
"v_or_b32 v178, v178, v179\n"
"v_or_b32 v176, v176, v177\n"
"PF v50,v180,v180,v50\n"
"GA v128\n"
"GA v129\n"
"CH v134,v134,v135\n"
"MZ v[144:159],a[232:235],v[20:23]\n"
"GL v128\n"
"GH v129\n"
"CH v142,v142,v143\n"
"PF v135,v134,v134,s56\n"
"PF v143,v142,v142,s56\n"
"PF v49,v178,v178,v49\n"
"PF v48,v176,v176,v48\n"
"GA v130\n"
"GA v131\n"
"AW a146,v180\n"
"AW a145,v178\n"
"AW a144,v176\n"
"PM v135,v135,v134\n"
"v_mfma_f32_16x16x32_f16 v[48:51], v[48:51], v[56:59], 0\n"
"GL v130\n"
"GH v131\n"
"PM v143,v143,v142\n"
"MB v[144:159],a[236:239],v[28:31]\n"
"EX v135,0\n"
"GA v132\n"
"GA v133\n"
"EX v143,0\n"
"EX v135,1\n"
"EX v143,1\n"
"v_pk_add_f32 v[50:51], v[48:49], v[50:51]\n"
"MB v[144:159],a[240:243],v[68:71]\n"
"GL v132\n"
"GH v133\n"
"v_add_f32_e64 v51, v50, v51\n"
"GA v136\n"
"GA v137\n"
"PF v135,v135,s57,s57\n"
"MB v[144:159],a[244:247],v[76:79]\n"
"GL v136\n"
"GH v137\n"
"PF v143,v143,s57,s57\n"
"WL 6\n"
"v_or_b32 v188, v188, v189\n"
"v_or_b32 v186, v186, v187\n"
"v_or_b32 v184, v184, v185\n"
"RC v135,0\n"
"PF v54,v188,v188,v54\n"
"PF v53,v186,v186,v53\n"
"PF v52,v184,v184,v52\n"
"GA v138\n"
"GA v139\n"
"AW a150,v188\n"
"AW a149,v186\n"
"AW a148,v184\n"
"v_mfma_f32_16x16x32_f16 v[52:55], v[52:55], v[56:59], 0\n"
"GL v138\n"
"GH v139\n"
"RC v143,0\n"
"MB v[144:159],a[248:251],v[100:103]\n"
"RC v135,1\n"
"GA v140\n"
"GA v141\n"
"RC v143,1\n"
"PM v134,v134,v135\n"
"PM v142,v142,v143\n"
"PM v195,v134,v134\n"
"v_pk_add_f32 v[54:55], v[52:53], v[54:55]\n"
"v_add_f32_e64 v52, v54, v55\n"
"MB v[144:159],a[252:255],v[108:111]\n"
"GL v140\n"
"GH v141\n"
"v_permlane16_swap_b32 v51, v52\n"
"v_add_f32 v62, v51, v52\n"
"PM v199,v142,v142\n"
"WL 4\n"
"v_or_b32 v132, v132, v133\n"
"v_or_b32 v130, v130, v131\n"
"v_or_b32 v128, v128, v129\n"
"PM v194,v132,v132\n"
"PM v193,v130,v130\n"
"PM v192,v128,v128\n"
"GA v144\n"
"GA v145\n"
"CH v150,v150,v151\n"
"MZ v[160:175],a[232:235],v[32:35]\n"
"GL v144\n"
"GH v145\n"
"CH v158,v158,v159\n"
"PF v151,v150,v150,s56\n"
"PF v159,v158,v158,s56\n"
"GA v146\n"
"GA v147\n"
"PM v151,v151,v150\n"
"PM v159,v159,v158\n"
"MB v[160:175],a[236:239],v[40:43]\n"
"GL v146\n"
"GH v147\n"
"EX v151,0\n"
"GA v148\n"
"GA v149\n"
"EX v159,0\n"
"MB v[160:175],a[240:243],v[80:83]\n"
"GL v148\n"
"GH v149\n"
"EX v151,1\n"
"EX v159,1\n"
"GA v152\n"
"GA v153\n"
"MB v[160:175],a[244:247],v[88:91]\n"
"GL v152\n"
"GH v153\n"
"PF v151,v151,s57,s57\n"
"PF v159,v159,s57,s57\n"
"WL 8\n"
"v_or_b32 v140, v140, v141\n"
"v_or_b32 v138, v138, v139\n"
"v_or_b32 v136, v136, v137\n"
"RC v151,0\n"
"PM v198,v140,v140\n"
"PM v197,v138,v138\n"
"PM v196,v136,v136\n"
"GA v154\n"
"GA v155\n"
"RC v159,0\n"
"MB v[160:175],a[248:251],v[112:115]\n"
"GL v154\n"
"GH v155\n"
"RC v151,1\n"
"GA v156\n"
"GA v157\n"
"RC v159,1\n"
"MB v[160:175],a[252:255],v[120:123]\n"
"GL v156\n"
"GH v157\n"
"PM v150,v150,v151\n"
"PM v158,v158,v159\n"
"PF v195,v150,v150,v195\n"
"PF v199,v158,v158,v199\n"
"WL 6\n"
"v_or_b32 v148, v148, v149\n"
"v_or_b32 v146, v146, v147\n"
"v_or_b32 v144, v144, v145\n"
"PF v194,v148,v148,v194\n"
"PF v193,v146,v146,v193\n"
"PF v192,v144,v144,v192\n"
"GA v160\n"
"GA v161\n"
"CH v166,v166,v167\n"
"MZ v[176:191],a[232:235],v[36:39]\n"
"GL v160\n"
"GH v161\n"
"LD a[232:235],v1,49152\n"
"CH v174,v174,v175\n"
"PF v167,v166,v166,s56\n"
"PF v175,v174,v174,s56\n"
"GA v162\n"
"GA v163\n"
"PM v167,v167,v166\n"
"PM v175,v175,v174\n"
"MB v[176:191],a[236:239],v[44:47]\n"
"GL v162\n"
"GH v163\n"
"LD a[236:239],v1,50176\n"
"EX v167,0\n"
"GA v164\n"
"GA v165\n"
"EX v175,0\n"
"MB v[176:191],a[240:243],v[84:87]\n"
"GL v164\n"
"GH v165\n"
"LD a[240:243],v1,51200\n"
"EX v167,1\n"
"EX v175,1\n"
"GA v168\n"
"GA v169\n"
"MB v[176:191],a[244:247],v[92:95]\n"
"GL v168\n"
"GH v169\n"
"LD a[244:247],v1,52224\n"
"PF v167,v167,s57,s57\n"
"PF v175,v175,s57,s57\n"
"WL 10\n"
"v_or_b32 v156, v156, v157\n"
"v_or_b32 v154, v154, v155\n"
"v_or_b32 v152, v152, v153\n"
"RC v167,0\n"
"PF v198,v156,v156,v198\n"
"PF v197,v154,v154,v197\n"
"PF v196,v152,v152,v196\n"
"GA v170\n"
"GA v171\n"
"RC v175,0\n"
"MB v[176:191],a[248:251],v[116:119]\n"
"GL v170\n"
"GH v171\n"
"LD a[248:251],v1,53248\n"
"RC v167,1\n"
"GA v172\n"
"GA v173\n"
"RC v175,1\n"
"MB v[176:191],a[252:255],v[124:127]\n"
"GL v172\n"
"GH v173\n"
"LD a[252:255],v1,54272\n"
"PM v166,v166,v167\n"
"PM v174,v174,v175\n"
"PF v195,v166,v166,v195\n"
"PF v199,v174,v174,v199\n"
"WL 7\n"
"v_or_b32 v164, v164, v165\n"
"v_or_b32 v162, v162, v163\n"
"v_or_b32 v160, v160, v161\n"
"PF v194,v164,v164,v194\n"
"PF v193,v162,v162,v193\n"
"GA v176\n"
"GA v177\n"
"CH v182,v182,v183\n"
"MZ v[128:143],a[232:235],v[16:19]\n"
"GL v176\n"
"GH v177\n"
"CH v190,v190,v191\n"
"PF v183,v182,v182,s56\n"
"PF v191,v190,v190,s56\n"
"PF v192,v160,v160,v192\n"
"GA v178\n"
"GA v179\n"
"PM v183,v183,v182\n"
"PM v191,v191,v190\n"
"MB v[128:143],a[236:239],v[24:27]\n"
"GL v178\n"
"GH v179\n"
"EX v183,0\n"
"GA v180\n"
"GA v181\n"
"EX v191,0\n"
"MB v[128:143],a[240:243],v[64:67]\n"
"GL v180\n"
"GH v181\n"
"EX v183,1\n"
"EX v191,1\n"
"GA v184\n"
"GA v185\n"
"WL 6\n"
"MB v[128:143],a[244:247],v[72:75]\n"
"GL v184\n"
"GH v185\n"
"PF v183,v183,s57,s57\n"
"PF v191,v191,s57,s57\n"
"v_or_b32 v172, v172, v173\n"
"v_or_b32 v170, v170, v171\n"
"v_or_b32 v168, v168, v169\n"
"RC v183,0\n"
"PF v198,v172,v172,v198\n"
"PF v197,v170,v170,v197\n"
"PF v196,v168,v168,v196\n"
"GA v186\n"
"GA v187\n"
"RC v191,0\n"
"MB v[128:143],a[248:251],v[96:99]\n"
"GL v186\n"
"GH v187\n"
"RC v183,1\n"
"GA v188\n"
"GA v189\n"
"RC v191,1\n"
"MB v[128:143],a[252:255],v[104:107]\n"
"GL v188\n"
"GH v189\n"
"PM v182,v182,v183\n"
"s_add_u32 s29, s31, 294912\n"
"PM v190,v190,v191\n"
"PF v195,v182,v182,v195\n"
"PF v199,v190,v190,v199\n"
"AW a155,v182\n"
"AW a159,v190\n"
"WL 5\n"
"v_or_b32 v180, v180, v181\n"
"v_or_b32 v178, v178, v179\n"
"v_or_b32 v176, v176, v177\n"
"GA v128\n"
"GA v129\n"
"CH v134,v134,v135\n"
"MZ v[144:159],a[232:235],v[20:23]\n"
"buffer_store_dwordx4 v[16:19], v14, s[40:43], s29 offen offset:0 sc1\n"
"GL v128\n"
"GH v129\n"
"CH v142,v142,v143\n"
"PF v135,v134,v134,s56\n"
"PF v143,v142,v142,s56\n"
"PF v194,v180,v180,v194\n"
"PF v193,v178,v178,v193\n"
"PF v192,v176,v176,v192\n"
"GA v130\n"
"GA v131\n"
"AW a154,v180\n"
"AW a153,v178\n"
"AW a152,v176\n"
"PM v135,v135,v134\n"
"v_mfma_f32_16x16x32_f16 v[192:195], v[192:195], v[56:59], 0\n"
"GL v130\n"
"GH v131\n"
"PM v143,v143,v142\n"
"MB v[144:159],a[236:239],v[28:31]\n"
"EX v135,0\n"
"GA v132\n"
"GA v133\n"
"EX v143,0\n"
"EX v135,1\n"
"EX v143,1\n"
"v_pk_add_f32 v[194:195], v[192:193], v[194:195]\n"
"MB v[144:159],a[240:243],v[68:71]\n"
"GL v132\n"
"GH v133\n"
"v_add_f32_e64 v195, v194, v195\n"
"GA v136\n"
"GA v137\n"
"PF v135,v135,s57,s57\n"
"MB v[144:159],a[244:247],v[76:79]\n"
"GL v136\n"
"GH v137\n"
"PF v143,v143,s57,s57\n"
"WL 6\n"
"v_or_b32 v188, v188, v189\n"
"v_or_b32 v186, v186, v187\n"
"v_or_b32 v184, v184, v185\n"
"RC v135,0\n"
"PF v198,v188,v188,v198\n"
"PF v197,v186,v186,v197\n"
"PF v196,v184,v184,v196\n"
"GA v138\n"
"GA v139\n"
"AW a158,v188\n"
"AW a157,v186\n"
"AW a156,v184\n"
"v_mfma_f32_16x16x32_f16 v[196:199], v[196:199], v[56:59], 0\n"
"GL v138\n"
"GH v139\n"
"RC v143,0\n"
"MB v[144:159],a[248:251],v[100:103]\n"
"RC v135,1\n"
"GA v140\n"
"GA v141\n"
"RC v143,1\n"
"PM v134,v134,v135\n"
"PM v142,v142,v143\n"
"PM v51,v134,v134\n"
"v_pk_add_f32 v[198:199], v[196:197], v[198:199]\n"
"v_add_f32_e64 v196, v198, v199\n"
"MB v[144:159],a[252:255],v[108:111]\n"
"GL v140\n"
"GH v141\n"
"buffer_store_dwordx4 v[24:27], v14, s[40:43], s29 offen offset:256 sc1\n"
"v_permlane16_swap_b32 v195, v196\n"
"v_add_f32 v196, v195, v196\n"
"PM v55,v142,v142\n"
"WL 4\n"
"v_or_b32 v132, v132, v133\n"
"v_permlane32_swap_b32 v62, v196\n"
"v_add_f32 v6, v62, v196\n"
"v_or_b32 v130, v130, v131\n"
"v_or_b32 v128, v128, v129\n"
"PM v50,v132,v132\n"
"GA v144\n"
"GA v145\n"
"CH v150,v150,v151\n"
"MZ v[160:175],a[232:235],v[32:35]\n"
"GL v144\n"
"GH v145\n"
"CH v158,v158,v159\n"
"PF v151,v150,v150,s56\n"
"PF v159,v158,v158,s56\n"
"PM v49,v130,v130\n"
"PM v48,v128,v128\n"
"GA v146\n"
"GA v147\n"
"PM v151,v151,v150\n"
"PM v159,v159,v158\n"
"MB v[160:175],a[236:239],v[40:43]\n"
"GL v146\n"
"GH v147\n"
"EX v151,0\n"
"GA v148\n"
"GA v149\n"
"EX v159,0\n"
"MB v[160:175],a[240:243],v[80:83]\n"
"GL v148\n"
"GH v149\n"
"EX v151,1\n"
"EX v159,1\n"
"GA v152\n"
"GA v153\n"
"MB v[160:175],a[244:247],v[88:91]\n"
"GL v152\n"
"GH v153\n"
"PF v151,v151,s57,s57\n"
"PF v159,v159,s57,s57\n"
"WL 8\n"
"v_or_b32 v140, v140, v141\n"
"v_or_b32 v138, v138, v139\n"
"v_or_b32 v136, v136, v137\n"
"RC v151,0\n"
"PM v54,v140,v140\n"
"PM v53,v138,v138\n"
"PM v52,v136,v136\n"
"GA v154\n"
"GA v155\n"
"s_add_u32 s29, s31, 296960\n"
"RC v159,0\n"
"MB v[160:175],a[248:251],v[112:115]\n"
"GL v154\n"
"GH v155\n"
"buffer_store_dwordx4 v[64:67], v14, s[40:43], s29 offen offset:0 sc1\n"
"RC v151,1\n"
"GA v156\n"
"GA v157\n"
"RC v159,1\n"
"MB v[160:175],a[252:255],v[120:123]\n"
"GL v156\n"
"GH v157\n"
"PM v150,v150,v151\n"
"PM v158,v158,v159\n"
"PF v51,v150,v150,v51\n"
"PF v55,v158,v158,v55\n"
"WL 5\n"
"v_or_b32 v148, v148, v149\n"
"v_or_b32 v146, v146, v147\n"
"v_or_b32 v144, v144, v145\n"
"PF v50,v148,v148,v50\n"
"PF v49,v146,v146,v49\n"
"PF v48,v144,v144,v48\n"
"GA v160\n"
"GA v161\n"
"CH v166,v166,v167\n"
"MZ v[176:191],a[232:235],v[36:39]\n"
"GL v160\n"
"GH v161\n"
"LD a[232:235],v1,55296\n"
"CH v174,v174,v175\n"
"PF v167,v166,v166,s56\n"
"PF v175,v174,v174,s56\n"
"GA v162\n"
"GA v163\n"
"PM v167,v167,v166\n"
"PM v175,v175,v174\n"
"MB v[176:191],a[236:239],v[44:47]\n"
"GL v162\n"
"GH v163\n"
"LD a[236:239],v1,56320\n"
"EX v167,0\n"
"GA v164\n"
"GA v165\n"
"EX v175,0\n"
"MB v[176:191],a[240:243],v[84:87]\n"
"GL v164\n"
"GH v165\n"
"LD a[240:243],v1,57344\n"
"EX v167,1\n"
"EX v175,1\n"
"GA v168\n"
"GA v169\n"
"MB v[176:191],a[244:247],v[92:95]\n"
"GL v168\n"
"GH v169\n"
"buffer_store_dwordx4 v[72:75], v14, s[40:43], s29 offen offset:256 sc1\n"
"LD a[244:247],v1,58368\n"
"PF v167,v167,s57,s57\n"
"PF v175,v175,s57,s57\n"
"WL 9\n"
"v_or_b32 v156, v156, v157\n"
"v_or_b32 v154, v154, v155\n"
"v_or_b32 v152, v152, v153\n"
"RC v167,0\n"
"PF v54,v156,v156,v54\n"
"PF v53,v154,v154,v53\n"
"PF v52,v152,v152,v52\n"
"GA v170\n"
"GA v171\n"
"RC v175,0\n"
"MB v[176:191],a[248:251],v[116:119]\n"
"GL v170\n"
"GH v171\n"
"LD a[248:251],v1,59392\n"
"RC v167,1\n"
"GA v172\n"
"GA v173\n"
"RC v175,1\n"
"MB v[176:191],a[252:255],v[124:127]\n"
"GL v172\n"
"GH v173\n"
"LD a[252:255],v1,60416\n"
"PM v166,v166,v167\n"
"PM v174,v174,v175\n"
"PF v51,v166,v166,v51\n"
"PF v55,v174,v174,v55\n"
"WL 7\n"
"v_or_b32 v164, v164, v165\n"
"v_or_b32 v162, v162, v163\n"
"v_or_b32 v160, v160, v161\n"
"PF v50,v164,v164,v50\n"
"PF v49,v162,v162,v49\n"
"GA v176\n"
"GA v177\n"
"CH v182,v182,v183\n"
"MZ v[128:143],a[232:235],v[16:19]\n"
"GL v176\n"
"GH v177\n"
"CH v190,v190,v191\n"
"PF v183,v182,v182,s56\n"
"PF v191,v190,v190,s56\n"
"PF v48,v160,v160,v48\n"
"GA v178\n"
"GA v179\n"
"PM v183,v183,v182\n"
"PM v191,v191,v190\n"
"MB v[128:143],a[236:239],v[24:27]\n"
"GL v178\n"
"GH v179\n"
"EX v183,0\n"
"GA v180\n"
"GA v181\n"
"EX v191,0\n"
"s_add_u32 s29, s31, 299008\n"
"MB v[128:143],a[240:243],v[64:67]\n"
"GL v180\n"
"GH v181\n"
"buffer_store_dwordx4 v[96:99], v14, s[40:43], s29 offen offset:0 sc1\n"
"EX v183,1\n"
"EX v191,1\n"
"GA v184\n"
"GA v185\n"
"WL 6\n"
"MB v[128:143],a[244:247],v[72:75]\n"
"GL v184\n"
"GH v185\n"
"PF v183,v183,s57,s57\n"
"PF v191,v191,s57,s57\n"
"v_or_b32 v172, v172, v173\n"
"v_or_b32 v170, v170, v171\n"
"v_or_b32 v168, v168, v169\n"
"RC v183,0\n"
"PF v54,v172,v172,v54\n"
"PF v53,v170,v170,v53\n"
"PF v52,v168,v168,v52\n"
"GA v186\n"
"GA v187\n"
"RC v191,0\n"
"MB v[128:143],a[248:251],v[96:99]\n"
"GL v186\n"
"GH v187\n"
"RC v183,1\n"
"GA v188\n"
"GA v189\n"
"RC v191,1\n"
"MB v[128:143],a[252:255],v[104:107]\n"
"GL v188\n"
"GH v189\n"
"PM v182,v182,v183\n"
"PM v190,v190,v191\n"
"PF v51,v182,v182,v51\n"
"PF v55,v190,v190,v55\n"
"AW a163,v182\n"
"AW a167,v190\n"
"WL 6\n"
"v_or_b32 v180, v180, v181\n"
"v_or_b32 v178, v178, v179\n"
"v_or_b32 v176, v176, v177\n"
"PF v50,v180,v180,v50\n"
"GA v128\n"
"GA v129\n"
"CH v134,v134,v135\n"
"MZ v[144:159],a[232:235],v[20:23]\n"
"GL v128\n"
"GH v129\n"
"CH v142,v142,v143\n"
"PF v135,v134,v134,s56\n"
"PF v143,v142,v142,s56\n"
"PF v49,v178,v178,v49\n"
"PF v48,v176,v176,v48\n"
"GA v130\n"
"GA v131\n"
"AW a162,v180\n"
"AW a161,v178\n"
"AW a160,v176\n"
"PM v135,v135,v134\n"
"v_mfma_f32_16x16x32_f16 v[48:51], v[48:51], v[56:59], 0\n"
"GL v130\n"
"GH v131\n"
"PM v143,v143,v142\n"
"MB v[144:159],a[236:239],v[28:31]\n"
"EX v135,0\n"
"GA v132\n"
"GA v133\n"
"EX v143,0\n"
"EX v135,1\n"
"EX v143,1\n"
"v_pk_add_f32 v[50:51], v[48:49], v[50:51]\n"
"MB v[144:159],a[240:243],v[68:71]\n"
"buffer_store_dwordx4 v[104:107], v14, s[40:43], s29 offen offset:256 sc1\n"
"GL v132\n"
"GH v133\n"
"v_add_f32_e64 v51, v50, v51\n"
"GA v136\n"
"GA v137\n"
"MB v[144:159],a[244:247],v[76:79]\n"
"GL v136\n"
"GH v137\n"
"PF v135,v135,s57,s57\n"
"PF v143,v143,s57,s57\n"
"WL 6\n"
"v_or_b32 v188, v188, v189\n"
"v_or_b32 v186, v186, v187\n"
"v_or_b32 v184, v184, v185\n"
"RC v135,0\n"
"PF v54,v188,v188,v54\n"
"PF v53,v186,v186,v53\n"
"PF v52,v184,v184,v52\n"
"GA v138\n"
"GA v139\n"
"AW a166,v188\n"
"AW a165,v186\n"
"AW a164,v184\n"
"v_mfma_f32_16x16x32_f16 v[52:55], v[52:55], v[56:59], 0\n"
"GL v138\n"
"GH v139\n"
"RC v143,0\n"
"MB v[144:159],a[248:251],v[100:103]\n"
"RC v135,1\n"
"GA v140\n"
"GA v141\n"
"RC v143,1\n"
"PM v134,v134,v135\n"
"PM v142,v142,v143\n"
"PM v195,v134,v134\n"
"v_pk_add_f32 v[54:55], v[52:53], v[54:55]\n"
"v_add_f32_e64 v52, v54, v55\n"
"MB v[144:159],a[252:255],v[108:111]\n"
"GL v140\n"
"GH v141\n"
"v_permlane16_swap_b32 v51, v52\n"
"v_add_f32 v62, v51, v52\n"
"PM v199,v142,v142\n"
"WL 4\n"
"v_or_b32 v132, v132, v133\n"
"v_or_b32_e64 v130, v130, v131\n"
"v_or_b32 v128, v128, v129\n"
"PM v194,v132,v132\n"
"PM v193,v130,v130\n"
"PM v192,v128,v128\n"
"GA v144\n"
"GA v145\n"
"CH v150,v150,v151\n"
"MZ v[160:175],a[232:235],v[32:35]\n"
"s_add_u32 s29, s31, 0\n"
"GL v144\n"
"GH v145\n"
"CH v158,v158,v159\n"
"PF v151,v150,v150,s56\n"
"PF v159,v158,v158,s56\n"
"GA v146\n"
"GA v147\n"
"PM v151,v151,v150\n"
"PM v159,v159,v158\n"
"MB v[160:175],a[236:239],v[40:43]\n"
"buffer_store_dwordx4 v[20:23], v14, s[40:43], s29 offen offset:0 sc1\n"
"GL v146\n"
"GH v147\n"
"EX v151,0\n"
"GA v148\n"
"GA v149\n"
"EX v159,0\n"
"MB v[160:175],a[240:243],v[80:83]\n"
"GL v148\n"
"GH v149\n"
"EX v151,1\n"
"EX v159,1\n"
"GA v152\n"
"GA v153\n"
"MB v[160:175],a[244:247],v[88:91]\n"
"GL v152\n"
"GH v153\n"
"PF v151,v151,s57,s57\n"
"PF v159,v159,s57,s57\n"
"WL 8\n"
"v_or_b32 v140, v140, v141\n"
"v_or_b32 v138, v138, v139\n"
"v_or_b32 v136, v136, v137\n"
"RC v151,0\n"
"PM v198,v140,v140\n"
"PM v197,v138,v138\n"
"PM v196,v136,v136\n"
"GA v154\n"
"GA v155\n"
"RC v159,0\n"
"MB v[160:175],a[248:251],v[112:115]\n"
"GL v154\n"
"GH v155\n"
"RC v151,1\n"
"GA v156\n"
"GA v157\n"
"RC v159,1\n"
"MB v[160:175],a[252:255],v[120:123]\n"
"GL v156\n"
"GH v157\n"
"PM v150,v150,v151\n"
"PM v158,v158,v159\n"
"PF v195,v150,v150,v195\n"
"PF v199,v158,v158,v199\n"
"WL 6\n"
"v_or_b32 v148, v148, v149\n"
"v_or_b32 v146, v146, v147\n"
"v_or_b32 v144, v144, v145\n"
"PF v194,v148,v148,v194\n"
"PF v193,v146,v146,v193\n"
"PF v192,v144,v144,v192\n"
"GA v160\n"
"GA v161\n"
"CH v166,v166,v167\n"
"MZ v[176:191],a[232:235],v[36:39]\n"
"buffer_store_dwordx4 v[28:31], v14, s[40:43], s29 offen offset:256 sc1\n"
"GL v160\n"
"GH v161\n"
"LD a[232:235],v1,61440\n"
"CH v174,v174,v175\n"
"PF v167,v166,v166,s56\n"
"PF v175,v174,v174,s56\n"
"GA v162\n"
"GA v163\n"
"PM v167,v167,v166\n"
"PM v175,v175,v174\n"
"MB v[176:191],a[236:239],v[44:47]\n"
"GL v162\n"
"GH v163\n"
"LD a[236:239],v1,62464\n"
"EX v167,0\n"
"GA v164\n"
"GA v165\n"
"EX v175,0\n"
"MB v[176:191],a[240:243],v[84:87]\n"
"GL v164\n"
"GH v165\n"
"LD a[240:243],v1,63488\n"
"EX v167,1\n"
"EX v175,1\n"
"GA v168\n"
"GA v169\n"
"MB v[176:191],a[244:247],v[92:95]\n"
"GL v168\n"
"GH v169\n"
"LD a[244:247],v1,64512\n"
"PF v167,v167,s57,s57\n"
"PF v175,v175,s57,s57\n"
"WL 10\n"
"v_or_b32 v156, v156, v157\n"
"v_or_b32 v154, v154, v155\n"
"v_or_b32 v152, v152, v153\n"
"RC v167,0\n"
"PF v198,v156,v156,v198\n"
"PF v197,v154,v154,v197\n"
"PF v196,v152,v152,v196\n"
"GA v170\n"
"GA v171\n"
"RC v175,0\n"
"MB v[176:191],a[248:251],v[116:119]\n"
"GL v170\n"
"GH v171\n"
"LD a[248:251],v2,0\n"
"s_add_u32 s29, s31, 2048\n"
"RC v167,1\n"
"GA v172\n"
"GA v173\n"
"RC v175,1\n"
"MB v[176:191],a[252:255],v[124:127]\n"
"buffer_store_dwordx4 v[68:71], v14, s[40:43], s29 offen offset:0 sc1\n"
"GL v172\n"
"GH v173\n"
"LD a[252:255],v2,1024\n"
"PM v166,v166,v167\n"
"PM v174,v174,v175\n"
"PF v195,v166,v166,v195\n"
"PF v199,v174,v174,v199\n"
"WL 6\n"
"v_or_b32 v164, v164, v165\n"
"v_or_b32 v162, v162, v163\n"
"v_or_b32 v160, v160, v161\n"
"PF v194,v164,v164,v194\n"
"GA v176\n"
"GA v177\n"
"CH v182,v182,v183\n"
"MZ v[128:143],a[232:235],v[16:19]\n"
"GL v176\n"
"GH v177\n"
"CH v190,v190,v191\n"
"PF v183,v182,v182,s56\n"
"PF v191,v190,v190,s56\n"
"PF v193,v162,v162,v193\n"
"PF v192,v160,v160,v192\n"
"GA v178\n"
"GA v179\n"
"PM v183,v183,v182\n"
"PM v191,v191,v190\n"
"MB v[128:143],a[236:239],v[24:27]\n"
"GL v178\n"
"GH v179\n"
"EX v183,0\n"
"GA v180\n"
"GA v181\n"
"EX v191,0\n"
"MB v[128:143],a[240:243],v[64:67]\n"
"GL v180\n"
"GH v181\n"
"EX v183,1\n"
"EX v191,1\n"
"GA v184\n"
"GA v185\n"
"MB v[128:143],a[244:247],v[72:75]\n"
"GL v184\n"
"GH v185\n"
"PF v183,v183,s57,s57\n"
"PF v191,v191,s57,s57\n"
"WL 8\n"
"v_or_b32 v172, v172, v173\n"
"v_or_b32 v170, v170, v171\n"
"v_or_b32 v168, v168, v169\n"
"RC v183,0\n"
"PF v198,v172,v172,v198\n"
"PF v197,v170,v170,v197\n"
"PF v196,v168,v168,v196\n"
"GA v186\n"
"GA v187\n"
"RC v191,0\n"
"MB v[128:143],a[248:251],v[96:99]\n"
"buffer_store_dwordx4 v[76:79], v14, s[40:43], s29 offen offset:256 sc1\n"
"GL v186\n"
"GH v187\n"
"RC v183,1\n"
"GA v188\n"
"GA v189\n"
"RC v191,1\n"
"MB v[128:143],a[252:255],v[104:107]\n"
"GL v188\n"
"GH v189\n"
"PM v182,v182,v183\n"
"PM v190,v190,v191\n"
"PF v195,v182,v182,v195\n"
"PF v199,v190,v190,v199\n"
"AW a171,v182\n"
"AW a175,v190\n"
"WL 4\n"
"v_or_b32 v180, v180, v181\n"
"v_or_b32 v178, v178, v179\n"
"v_or_b32 v176, v176, v177\n"
"PF v194,v180,v180,v194\n"
"GA v128\n"
"GA v129\n"
"CH v134,v134,v135\n"
"MZ v[144:159],a[232:235],v[20:23]\n"
"GL v128\n"
"GH v129\n"
"CH v142,v142,v143\n"
"PF v135,v134,v134,s56\n"
"PF v143,v142,v142,s56\n"
"PF v193,v178,v178,v193\n"
"PF v192,v176,v176,v192\n"
"GA v130\n"
"GA v131\n"
"AW a170,v180\n"
"AW a169,v178\n"
"AW a168,v176\n"
"PM v135,v135,v134\n"
"v_mfma_f32_16x16x32_f16 v[192:195], v[192:195], v[56:59], 0\n"
"GL v130\n"
"GH v131\n"
"buffer_load_dword v229, v0, s[24:27], s28 offen\n"
"PM v143,v143,v142\n"
"MB v[144:159],a[236:239],v[28:31]\n"
"EX v135,0\n"
"GA v132\n"
"GA v133\n"
"EX v143,0\n"
"EX v135,1\n"
"EX v143,1\n"
"v_pk_add_f32 v[194:195], v[192:193], v[194:195]\n"
"MB v[144:159],a[240:243],v[68:71]\n"
"GL v132\n"
"GH v133\n"
"v_add_f32_e64 v195, v194, v195\n"
"s_add_u32 s29, s31, 4096\n"
"GA v136\n"
"GA v137\n"
"MB v[144:159],a[244:247],v[76:79]\n"
"buffer_store_dwordx4 v[100:103], v14, s[40:43], s29 offen offset:0 sc1\n"
"GL v136\n"
"GH v137\n"
"PF v135,v135,s57,s57\n"
"PF v143,v143,s57,s57\n"
"WL 6\n"
"v_or_b32 v188, v188, v189\n"
"v_or_b32 v186, v186, v187\n"
"v_or_b32 v184, v184, v185\n"
"RC v135,0\n"
"PF v198,v188,v188,v198\n"
"PF v197,v186,v186,v197\n"
"PF v196,v184,v184,v196\n"
"GA v138\n"
"GA v139\n"
"AW a174,v188\n"
"AW a173,v186\n"
"AW a172,v184\n"
"v_mfma_f32_16x16x32_f16 v[196:199], v[196:199], v[56:59], 0\n"
"GL v138\n"
"GH v139\n"
"RC v143,0\n"
"MB v[144:159],a[248:251],v[100:103]\n"
"RC v135,1\n"
"GA v140\n"
"GA v141\n"
"RC v143,1\n"
"PM v134,v134,v135\n"
"PM v142,v142,v143\n"
"PM v51,v134,v134\n"
"v_pk_add_f32 v[198:199], v[196:197], v[198:199]\n"
"v_add_f32_e64 v196, v198, v199\n"
"MB v[144:159],a[252:255],v[108:111]\n"
"GL v140\n"
"GH v141\n"
"v_permlane16_swap_b32 v195, v196\n"
"v_add_f32 v196, v195, v196\n"
"PM v55,v142,v142\n"
"WL 4\n"
"v_or_b32 v132, v132, v133\n"
"v_permlane32_swap_b32 v62, v196\n"
"v_add_f32 v7, v62, v196\n"
"v_or_b32 v130, v130, v131\n"
"v_or_b32 v128, v128, v129\n"
"PM v50,v132,v132\n"
"PM v49,v130,v130\n"
"GA v144\n"
"GA v145\n"
"CH v150,v150,v151\n"
"MZ v[160:175],a[232:235],v[32:35]\n"
"GL v144\n"
"GH v145\n"
"CH v158,v158,v159\n"
"PF v151,v150,v150,s56\n"
"PF v159,v158,v158,s56\n"
"PM v48,v128,v128\n"
"GA v146\n"
"GA v147\n"
"PM v151,v151,v150\n"
"PM v159,v159,v158\n"
"MB v[160:175],a[236:239],v[40:43]\n"
"GL v146\n"
"GH v147\n"
"EX v151,0\n"
"GA v148\n"
"GA v149\n"
"EX v159,0\n"
"MB v[160:175],a[240:243],v[80:83]\n"
"GL v148\n"
"GH v149\n"
"buffer_store_dwordx4 v[108:111], v14, s[40:43], s29 offen offset:256 sc1\n"
"EX v151,1\n"
"EX v159,1\n"
"GA v152\n"
"GA v153\n"
"MB v[160:175],a[244:247],v[88:91]\n"
"GL v152\n"
"GH v153\n"
"PF v151,v151,s57,s57\n"
"PF v159,v159,s57,s57\n"
"WL 8\n"
"v_or_b32 v140, v140, v141\n"
"v_or_b32 v138, v138, v139\n"
"v_or_b32 v136, v136, v137\n"
"RC v151,0\n"
"PM v54,v140,v140\n"
"PM v53,v138,v138\n"
"PM v52,v136,v136\n"
"GA v154\n"
"GA v155\n"
"RC v159,0\n"
"MB v[160:175],a[248:251],v[112:115]\n"
"GL v154\n"
"GH v155\n"
"RC v151,1\n"
"GA v156\n"
"GA v157\n"
"RC v159,1\n"
"MB v[160:175],a[252:255],v[120:123]\n"
"GL v156\n"
"GH v157\n"
"PM v150,v150,v151\n"
"PM v158,v158,v159\n"
"PF v51,v150,v150,v51\n"
"PF v55,v158,v158,v55\n"
"WL 6\n"
"v_or_b32 v148, v148, v149\n"
"v_or_b32 v146, v146, v147\n"
"v_or_b32 v144, v144, v145\n"
"PF v50,v148,v148,v50\n"
"PF v49,v146,v146,v49\n"
"PF v48,v144,v144,v48\n"
"GA v160\n"
"GA v161\n"
"CH v166,v166,v167\n"
"MZ v[176:191],a[232:235],v[36:39]\n"
"GL v160\n"
"GH v161\n"
"LD a[232:235],v2,2048\n"
"CH v174,v174,v175\n"
"PF v167,v166,v166,s56\n"
"PF v175,v174,v174,s56\n"
"s_add_u32 s29, s31, 98304\n"
"GA v162\n"
"GA v163\n"
"PM v167,v167,v166\n"
"PM v175,v175,v174\n"
"MB v[176:191],a[236:239],v[44:47]\n"
"GL v162\n"
"GH v163\n"
"buffer_store_dwordx4 v[32:35], v14, s[40:43], s29 offen offset:0 sc1\n"
"LD a[236:239],v2,3072\n"
"EX v167,0\n"
"GA v164\n"
"GA v165\n"
"EX v175,0\n"
"MB v[176:191],a[240:243],v[84:87]\n"
"GL v164\n"
"GH v165\n"
"LD a[240:243],v2,4096\n"
"EX v167,1\n"
"EX v175,1\n"
"GA v168\n"
"GA v169\n"
"MB v[176:191],a[244:247],v[92:95]\n"
"GL v168\n"
"GH v169\n"
"LD a[244:247],v2,5120\n"
"PF v167,v167,s57,s57\n"
"PF v175,v175,s57,s57\n"
"WL 9\n"
"v_or_b32 v156, v156, v157\n"
"v_or_b32 v154, v154, v155\n"
"v_or_b32 v152, v152, v153\n"
"RC v167,0\n"
"PF v54,v156,v156,v54\n"
"PF v53,v154,v154,v53\n"
"PF v52,v152,v152,v52\n"
"GA v170\n"
"GA v171\n"
"RC v175,0\n"
"MB v[176:191],a[248:251],v[116:119]\n"
"GL v170\n"
"GH v171\n"
"LD a[248:251],v2,6144\n"
"RC v167,1\n"
"GA v172\n"
"GA v173\n"
"RC v175,1\n"
"MB v[176:191],a[252:255],v[124:127]\n"
"GL v172\n"
"GH v173\n"
"LD a[252:255],v2,7168\n"
"PM v166,v166,v167\n"
"PM v174,v174,v175\n"
"PF v51,v166,v166,v51\n"
"PF v55,v174,v174,v55\n"
"WL 7\n"
"v_or_b32 v164, v164, v165\n"
"v_or_b32 v162, v162, v163\n"
"v_or_b32 v160, v160, v161\n"
"PF v50,v164,v164,v50\n"
"PF v49,v162,v162,v49\n"
"GA v176\n"
"GA v177\n"
"CH v182,v182,v183\n"
"MZ v[128:143],a[232:235],v[16:19]\n"
"GL v176\n"
"GH v177\n"
"buffer_store_dwordx4 v[40:43], v14, s[40:43], s29 offen offset:256 sc1\n"
"CH v190,v190,v191\n"
"PF v183,v182,v182,s56\n"
"PF v191,v190,v190,s56\n"
"PF v48,v160,v160,v48\n"
"GA v178\n"
"GA v179\n"
"PM v183,v183,v182\n"
"PM v191,v191,v190\n"
"MB v[128:143],a[236:239],v[24:27]\n"
"GL v178\n"
"GH v179\n"
"EX v183,0\n"
"GA v180\n"
"GA v181\n"
"EX v191,0\n"
"MB v[128:143],a[240:243],v[64:67]\n"
"GL v180\n"
"GH v181\n"
"EX v183,1\n"
"EX v191,1\n"
"GA v184\n"
"GA v185\n"
"WL 6\n"
"MB v[128:143],a[244:247],v[72:75]\n"
"GL v184\n"
"GH v185\n"
"PF v183,v183,s57,s57\n"
"PF v191,v191,s57,s57\n"
"v_or_b32 v172, v172, v173\n"
"v_or_b32 v170, v170, v171\n"
"v_or_b32 v168, v168, v169\n"
"RC v183,0\n"
"PF v54,v172,v172,v54\n"
"PF v53,v170,v170,v53\n"
"PF v52,v168,v168,v52\n"
"GA v186\n"
"GA v187\n"
"RC v191,0\n"
"MB v[128:143],a[248:251],v[96:99]\n"
"GL v186\n"
"GH v187\n"
"RC v183,1\n"
"GA v188\n"
"GA v189\n"
"RC v191,1\n"
"s_add_u32 s29, s31, 100352\n"
"MB v[128:143],a[252:255],v[104:107]\n"
"GL v188\n"
"GH v189\n"
"buffer_store_dwordx4 v[80:83], v14, s[40:43], s29 offen offset:0 sc1\n"
"PM v182,v182,v183\n"
"PM v190,v190,v191\n"
"PF v51,v182,v182,v51\n"
"PF v55,v190,v190,v55\n"
"AW a179,v182\n"
"AW a183,v190\n"
"WL 4\n"
"v_or_b32 v180, v180, v181\n"
"v_or_b32 v178, v178, v179\n"
"v_or_b32 v176, v176, v177\n"
"GA v128\n"
"GA v129\n"
"CH v134,v134,v135\n"
"MZ v[144:159],a[232:235],v[20:23]\n"
"GL v128\n"
"GH v129\n"
"CH v142,v142,v143\n"
"PF v135,v134,v134,s56\n"
"PF v143,v142,v142,s56\n"
"PF v50,v180,v180,v50\n"
"PF v49,v178,v178,v49\n"
"PF v48,v176,v176,v48\n"
"GA v130\n"
"GA v131\n"
"AW a178,v180\n"
"AW a177,v178\n"
"AW a176,v176\n"
"PM v135,v135,v134\n"
"v_mfma_f32_16x16x32_f16 v[48:51], v[48:51], v[56:59], 0\n"
"GL v130\n"
"GH v131\n"
"PM v143,v143,v142\n"
"MB v[144:159],a[236:239],v[28:31]\n"
"EX v135,0\n"
"GA v132\n"
"GA v133\n"
"EX v143,0\n"
"EX v135,1\n"
"EX v143,1\n"
"v_pk_add_f32 v[50:51], v[48:49], v[50:51]\n"
"MB v[144:159],a[240:243],v[68:71]\n"
"GL v132\n"
"GH v133\n"
"v_add_f32_e64 v51, v50, v51\n"
"GA v136\n"
"GA v137\n"
"PF v135,v135,s57,s57\n"
"MB v[144:159],a[244:247],v[76:79]\n"
"GL v136\n"
"GH v137\n"
"PF v143,v143,s57,s57\n"
"WL 6\n"
"v_or_b32 v188, v188, v189\n"
"v_or_b32 v186, v186, v187\n"
"v_or_b32 v184, v184, v185\n"
"RC v135,0\n"
"PF v54,v188,v188,v54\n"
"PF v53,v186,v186,v53\n"
"PF v52,v184,v184,v52\n"
"GA v138\n"
"GA v139\n"
"AW a182,v188\n"
"AW a181,v186\n"
"AW a180,v184\n"
"v_mfma_f32_16x16x32_f16 v[52:55], v[52:55], v[56:59], 0\n"
"GL v138\n"
"GH v139\n"
"RC v143,0\n"
"MB v[144:159],a[248:251],v[100:103]\n"
"RC v135,1\n"
"GA v140\n"
"GA v141\n"
"RC v143,1\n"
"PM v134,v134,v135\n"
"PM v142,v142,v143\n"
"PM v195,v134,v134\n"
"v_pk_add_f32 v[54:55], v[52:53], v[54:55]\n"
"v_add_f32_e64 v52, v54, v55\n"
"MB v[144:159],a[252:255],v[108:111]\n"
"buffer_store_dwordx4 v[88:91], v14, s[40:43], s29 offen offset:256 sc1\n"
"GL v140\n"
"GH v141\n"
"v_permlane16_swap_b32 v51, v52\n"
"v_add_f32 v62, v51, v52\n"
"PM v199,v142,v142\n"
"WL 4\n"
"v_or_b32 v132, v132, v133\n"
"v_or_b32 v130, v130, v131\n"
"v_or_b32 v128, v128, v129\n"
"PM v194,v132,v132\n"
"PM v193,v130,v130\n"
"PM v192,v128,v128\n"
"GA v144\n"
"GA v145\n"
"CH v150,v150,v151\n"
"MZ v[160:175],a[232:235],v[32:35]\n"
"GL v144\n"
"GH v145\n"
"CH v158,v158,v159\n"
"PF v151,v150,v150,s56\n"
"PF v159,v158,v158,s56\n"
"GA v146\n"
"GA v147\n"
"PM v151,v151,v150\n"
"PM v159,v159,v158\n"
"MB v[160:175],a[236:239],v[40:43]\n"
"GL v146\n"
"GH v147\n"
"EX v151,0\n"
"GA v148\n"
"GA v149\n"
"EX v159,0\n"
"MB v[160:175],a[240:243],v[80:83]\n"
"GL v148\n"
"GH v149\n"
"EX v151,1\n"
"EX v159,1\n"
"GA v152\n"
"GA v153\n"
"MB v[160:175],a[244:247],v[88:91]\n"
"GL v152\n"
"GH v153\n"
"PF v151,v151,s57,s57\n"
"s_add_u32 s29, s31, 102400\n"
"PF v159,v159,s57,s57\n"
"WL 8\n"
"v_or_b32 v140, v140, v141\n"
"v_or_b32 v138, v138, v139\n"
"v_or_b32 v136, v136, v137\n"
"RC v151,0\n"
"PM v198,v140,v140\n"
"PM v197,v138,v138\n"
"PM v196,v136,v136\n"
"GA v154\n"
"GA v155\n"
"RC v159,0\n"
"MB v[160:175],a[248:251],v[112:115]\n"
"buffer_store_dwordx4 v[112:115], v14, s[40:43], s29 offen offset:0 sc1\n"
"GL v154\n"
"GH v155\n"
"RC v151,1\n"
"GA v156\n"
"GA v157\n"
"RC v159,1\n"
"MB v[160:175],a[252:255],v[120:123]\n"
"GL v156\n"
"GH v157\n"
"PM v150,v150,v151\n"
"PM v158,v158,v159\n"
"PF v195,v150,v150,v195\n"
"PF v199,v158,v158,v199\n"
"WL 5\n"
"v_or_b32 v148, v148, v149\n"
"v_or_b32 v146, v146, v147\n"
"v_or_b32 v144, v144, v145\n"
"PF v194,v148,v148,v194\n"
"PF v193,v146,v146,v193\n"
"PF v192,v144,v144,v192\n"
"GA v160\n"
"GA v161\n"
"CH v166,v166,v167\n"
"MZ v[176:191],a[232:235],v[36:39]\n"
"GL v160\n"
"GH v161\n"
"CH v174,v174,v175\n"
"PF v167,v166,v166,s56\n"
"PF v175,v174,v174,s56\n"
"GA v162\n"
"GA v163\n"
"PM v167,v167,v166\n"
"PM v175,v175,v174\n"
"MB v[176:191],a[236:239],v[44:47]\n"
"GL v162\n"
"GH v163\n"
"EX v167,0\n"
"GA v164\n"
"GA v165\n"
"EX v175,0\n"
"MB v[176:191],a[240:243],v[84:87]\n"
"GL v164\n"
"GH v165\n"
"EX v167,1\n"
"EX v175,1\n"
"GA v168\n"
"GA v169\n"
"MB v[176:191],a[244:247],v[92:95]\n"
"buffer_store_dwordx4 v[120:123], v14, s[40:43], s29 offen offset:256 sc1\n"
"GL v168\n"
"GH v169\n"
"PF v167,v167,s57,s57\n"
"PF v175,v175,s57,s57\n"
"WL 8\n"
"v_or_b32 v156, v156, v157\n"
"v_or_b32 v154, v154, v155\n"
"v_or_b32 v152, v152, v153\n"
"RC v167,0\n"
"PF v198,v156,v156,v198\n"
"PF v197,v154,v154,v197\n"
"PF v196,v152,v152,v196\n"
"GA v170\n"
"GA v171\n"
"RC v175,0\n"
"MB v[176:191],a[248:251],v[116:119]\n"
"GL v170\n"
"GH v171\n"
"RC v167,1\n"
"GA v172\n"
"GA v173\n"
"RC v175,1\n"
"MB v[176:191],a[252:255],v[124:127]\n"
"GL v172\n"
"GH v173\n"
"PM v166,v166,v167\n"
"PM v174,v174,v175\n"
"PF v195,v166,v166,v195\n"
"PF v199,v174,v174,v199\n"
"WL 6\n"
"v_or_b32 v164, v164, v165\n"
"v_or_b32 v162, v162, v163\n"
"v_or_b32 v160, v160, v161\n"
"PF v194,v164,v164,v194\n"
"PF v193,v162,v162,v193\n"
"PF v192,v160,v160,v192\n"
"GA v176\n"
"GA v177\n"
"GL v176\n"
"GH v177\n"
"CH v182,v182,v183\n"
"CH v190,v190,v191\n"
"PF v183,v182,v182,s56\n"
"PF v191,v190,v190,s56\n"
"GA v178\n"
"GA v179\n"
"GL v178\n"
"GH v179\n"
"PM v183,v183,v182\n"
"PM v191,v191,v190\n"
"EX v183,0\n"
"GA v180\n"
"GA v181\n"
"GL v180\n"
"GH v181\n"
"EX v191,0\n"
"EX v183,1\n"
"EX v191,1\n"
"GA v184\n"
"GA v185\n"
"GL v184\n"
"GH v185\n"
"PF v183,v183,s57,s57\n"
"PF v191,v191,s57,s57\n"
"WL 8\n"
"v_or_b32 v172, v172, v173\n"
"v_or_b32 v170, v170, v171\n"
"v_or_b32 v168, v168, v169\n"
"RC v183,0\n"
"PF v198,v172,v172,v198\n"
"PF v197,v170,v170,v197\n"
"PF v196,v168,v168,v196\n"
"GA v186\n"
"GA v187\n"
"GL v186\n"
"GH v187\n"
"RC v191,0\n"
"RC v183,1\n"
"GA v188\n"
"GA v189\n"
"GL v188\n"
"GH v189\n"
"RC v191,1\n"
"PM v182,v182,v183\n"
"PM v190,v190,v191\n"
"PF v195,v182,v182,v195\n"
"PF v199,v190,v190,v199\n"
"AW a187,v182\n"
"AW a191,v190\n"
"WL 6\n"
"v_or_b32 v180, v180, v181\n"
"v_or_b32 v178, v178, v179\n"
"v_or_b32 v176, v176, v177\n"
"PF v194,v180,v180,v194\n"
"PF v193,v178,v178,v193\n"
"PF v192,v176,v176,v192\n"
"AW a186,v180\n"
"AW a185,v178\n"
"AW a184,v176\n"
"v_mfma_f32_16x16x32_f16 v[192:195], v[192:195], v[56:59], 0\n"
"WL 0\n"
"v_or_b32 v188, v188, v189\n"
"v_or_b32 v186, v186, v187\n"
"v_or_b32 v184, v184, v185\n"
"PF v198,v188,v188,v198\n"
"PF v197,v186,v186,v197\n"
"PF v196,v184,v184,v196\n"
"AW a190,v188\n"
"AW a189,v186\n"
"AW a188,v184\n"
"v_mfma_f32_16x16x32_f16 v[196:199], v[196:199], v[56:59], 0\n"
"v_pk_add_f32 v[194:195], v[192:193], v[194:195]\n"
"v_add_f32 v195, v194, v195\n"
"s_nop 5\n"
"v_pk_add_f32 v[198:199], v[196:197], v[198:199]\n"
"v_add_f32 v196, v198, v199\n"
"s_nop 1\n"
"v_permlane16_swap_b32 v195, v196\n"
"v_add_f32 v196, v195, v196\n"
"s_nop 1\n"
"v_permlane32_swap_b32 v62, v196\n"
"v_add_f32_e64 v63, v62, v196\n"
"AW a208,v63\n"
"AW a209,v56\n"
"AW a210,v57\n"
"AW a211,v58\n"
"AW a212,v59\n"
"AR v26,a192\n"
"AR v27,a193\n"
"AR v28,a194\n"
"AR v29,a195\n"
"AR v30,a196\n"
"AR v31,a197\n"
"AR v32,a198\n"
"AR v33,a199\n"
"AR v34,a200\n"
"AR v35,a201\n"
"AR v36,a202\n"
"AR v37,a203\n"
"AR v38,a204\n"
"AR v39,a205\n"
"AR v40,a206\n"
"AR v41,a207\n"
"v_add_u32 v25, 6144, v0\n"
"v_add_u32 v24, s34, v25\n"
"v_add_u32 v25, s35, v25\n"
"s_waitcnt vmcnt(23)\n"
"s_barrier\n"
"LD a[0:3],v30,2176\n"
"LD a[8:11],v40,0\n"
"LD a[12:15],v26,0\n"
"LD a[16:19],v28,0\n"
"ds_read_b64 a[20:21], v32 offset:0\n"
"ds_read_b64 a[22:23], v33 offset:0\n"
"LD a[24:27],v30,0\n"
"LD a[28:31],v26,1088\n"
"ds_read_b64 a[32:33], v34 offset:0\n"
"ds_read_b64 a[34:35], v35 offset:0\n"
"LD a[216:219],v28,1088\n"
"LD a[220:223],v30,1088\n"
"ds_read_b64 a[224:225], v36 offset:0\n"
"ds_read_b64 a[226:227], v37 offset:0\n"
"WL 12\n"
"s_nop 0\n"
"MZ v[64:79],a[8:11],a[0:3]\n"
"LD a[228:231],v26,2176\n"
"LD a[232:235],v28,2176\n"
"ds_read_b64 a[236:237], v38 offset:0\n"
"ds_read_b64 a[238:239], v39 offset:0\n"
"WL 12\n"
"v_smfmac_f32_32x32x32_bf16 v[64:79], a[20:23], a[12:19], v41\n"
"LD a[240:243],v31,2176\n"
"LD a[248:251],v40,128\n"
"WL 10\n"
"v_smfmac_f32_32x32x32_bf16 v[64:79], a[32:35], a[24:31], v41\n"
"LD a[0:3],v27,0\n"
"LD a[4:7],v29,0\n"
"ds_read_b64 a[8:9], v32 offset:64\n"
"ds_read_b64 a[10:11], v33 offset:64\n"
"WL 10\n"
"v_smfmac_f32_32x32x32_bf16 v[64:79], a[224:227], a[216:223], v41\n"
"LD a[12:15],v31,0\n"
"LD a[16:19],v27,1088\n"
"ds_read_b64 a[20:21], v34 offset:64\n"
"ds_read_b64 a[22:23], v35 offset:64\n"
"WL 10\n"
"v_smfmac_f32_32x32x32_bf16 v[64:79], a[236:239], a[228:235], v41\n"
"LD a[24:27],v29,1088\n"
"LD a[28:31],v31,1088\n"
"ds_read_b64 a[32:33], v36 offset:64\n"
"ds_read_b64 a[34:35], v37 offset:64\n"
"WL 12\n"
"MZ v[80:95],a[248:251],a[240:243]\n"
"LD a[216:219],v27,2176\n"
"LD a[220:223],v29,2176\n"
"ds_read_b64 a[224:225], v38 offset:64\n"
"ds_read_b64 a[226:227], v39 offset:64\n"
"s_nop 1\n"
"v_pk_mul_f32 v[50:51], v[64:65], v[64:65]\n"
"v_pk_mul_f32 v[54:55], v[68:69], v[68:69]\n"
"v_pk_mul_f32 v[58:59], v[72:73], v[72:73]\n"
"v_pk_mul_f32 v[62:63], v[76:77], v[76:77]\n"
"v_pk_add_f32 v[48:49], v[64:65], v[66:67]\n"
"v_pk_fma_f32 v[50:51], v[66:67], v[66:67], v[50:51]\n"
"v_pk_add_f32 v[52:53], v[68:69], v[70:71]\n"
"v_pk_fma_f32 v[54:55], v[70:71], v[70:71], v[54:55]\n"
"WL 12\n"
"v_smfmac_f32_32x32x32_bf16 v[80:95], a[8:11], a[0:7], v41\n"
"LD a[228:231],v30,21952\n"
"LD a[236:239],v40,256\n"
"WL 10\n"
"v_smfmac_f32_32x32x32_bf16 v[80:95], a[20:23], a[12:19], v41\n"
"LD a[240:243],v26,19776\n"
"LD a[244:247],v28,19776\n"
"ds_read_b64 a[248:249], v32 offset:128\n"
"ds_read_b64 a[250:251], v33 offset:128\n"
"v_pk_add_f32 v[56:57], v[72:73], v[74:75]\n"
"v_pk_fma_f32 v[58:59], v[74:75], v[74:75], v[58:59]\n"
"v_pk_add_f32 v[60:61], v[76:77], v[78:79]\n"
"v_pk_fma_f32 v[62:63], v[78:79], v[78:79], v[62:63]\n"
"WL 10\n"
"v_smfmac_f32_32x32x32_bf16 v[80:95], a[32:35], a[24:31], v41\n"
"LD a[0:3],v30,19776\n"
"LD a[4:7],v26,20864\n"
"ds_read_b64 a[8:9], v34 offset:128\n"
"ds_read_b64 a[10:11], v35 offset:128\n"
"WL 10\n"
"v_smfmac_f32_32x32x32_bf16 v[80:95], a[224:227], a[216:223], v41\n"
"LD a[12:15],v28,20864\n"
"LD a[16:19],v30,20864\n"
"ds_read_b64 a[20:21], v36 offset:128\n"
"ds_read_b64 a[22:23], v37 offset:128\n"
"s_barrier\n"
"ds_write_b128 v24, a[36:39] offset:0\n"
"s_nop 5\n"
"v_pk_add_f32 v[48:49], v[48:49], v[80:81]\n"
"v_pk_fma_f32 v[50:51], v[80:81], v[80:81], v[50:51]\n"
"v_pk_add_f32 v[52:53], v[52:53], v[84:85]\n"
"v_pk_fma_f32 v[54:55], v[84:85], v[84:85], v[54:55]\n"
"ds_write_b128 v24, a[40:43] offset:1024\n"
"v_pk_add_f32 v[56:57], v[56:57], v[88:89]\n"
"v_pk_fma_f32 v[58:59], v[88:89], v[88:89], v[58:59]\n"
"v_pk_add_f32 v[60:61], v[60:61], v[92:93]\n"
"v_pk_fma_f32 v[62:63], v[92:93], v[92:93], v[62:63]\n"
"ds_write_b128 v24, a[44:47] offset:2048\n"
"v_pk_add_f32 v[48:49], v[48:49], v[82:83]\n"
"v_pk_fma_f32 v[50:51], v[82:83], v[82:83], v[50:51]\n"
"v_pk_add_f32 v[52:53], v[52:53], v[86:87]\n"
"v_pk_fma_f32 v[54:55], v[86:87], v[86:87], v[54:55]\n"
"ds_write_b128 v24, a[48:51] offset:3072\n"
"v_pk_add_f32 v[56:57], v[56:57], v[90:91]\n"
"v_pk_fma_f32 v[58:59], v[90:91], v[90:91], v[58:59]\n"
"v_pk_add_f32 v[60:61], v[60:61], v[94:95]\n"
"v_pk_fma_f32 v[62:63], v[94:95], v[94:95], v[62:63]\n"
"s_waitcnt vmcnt(22)\n"
"ds_write_b128 v25, a[52:55] offset:0\n"
"WL 9\n"
"MZ v[96:111],a[236:239],a[228:231]\n"
"LD a[24:27],v26,21952\n"
"LD a[28:31],v28,21952\n"
"ds_read_b64 a[32:33], v38 offset:128\n"
"ds_read_b64 a[34:35], v39 offset:128\n"
"v_smfmac_f32_32x32x32_bf16 v[96:111], a[248:251], a[240:247], v41\n"
"LD a[216:219],v31,21952\n"
"LD a[224:227],v40,384\n"
"v_smfmac_f32_32x32x32_bf16 v[96:111], a[8:11], a[0:7], v41\n"
"LD a[228:231],v27,19776\n"
"LD a[232:235],v29,19776\n"
"ds_read_b64 a[236:237], v32 offset:192\n"
"ds_read_b64 a[238:239], v33 offset:192\n"
"WL 13\n"
"v_smfmac_f32_32x32x32_bf16 v[96:111], a[20:23], a[12:19], v41\n"
"LD a[240:243],v31,19776\n"
"LD a[244:247],v27,20864\n"
"ds_read_b64 a[248:249], v34 offset:192\n"
"ds_read_b64 a[250:251], v35 offset:192\n"
"WL 10\n"
"v_smfmac_f32_32x32x32_bf16 v[96:111], a[32:35], a[24:31], v41\n"
"LD a[0:3],v29,20864\n"
"LD a[4:7],v31,20864\n"
"ds_read_b64 a[8:9], v36 offset:192\n"
"ds_read_b64 a[10:11], v37 offset:192\n"
"WL 12\n"
"MZ v[112:127],a[224:227],a[216:219]\n"
"LD a[12:15],v27,21952\n"
"LD a[16:19],v29,21952\n"
"ds_read_b64 a[20:21], v38 offset:192\n"
"ds_read_b64 a[22:23], v39 offset:192\n"
"s_nop 1\n"
"v_pk_add_f32 v[48:49], v[48:49], v[96:97]\n"
"v_pk_fma_f32 v[50:51], v[96:97], v[96:97], v[50:51]\n"
"v_pk_add_f32 v[52:53], v[52:53], v[100:101]\n"
"v_pk_fma_f32 v[54:55], v[100:101], v[100:101], v[54:55]\n"
"v_pk_add_f32 v[56:57], v[56:57], v[104:105]\n"
"v_pk_fma_f32 v[58:59], v[104:105], v[104:105], v[58:59]\n"
"v_pk_add_f32 v[60:61], v[60:61], v[108:109]\n"
"v_pk_fma_f32 v[62:63], v[108:109], v[108:109], v[62:63]\n"
"WL 12\n"
"v_smfmac_f32_32x32x32_bf16 v[112:127], a[236:239], a[228:235], v41\n"
"LD a[24:27],v30,41728\n"
"LD a[32:35],v40,512\n"
"WL 10\n"
"v_smfmac_f32_32x32x32_bf16 v[112:127], a[248:251], a[240:247], v41\n"
"LD a[216:219],v26,39552\n"
"LD a[220:223],v28,39552\n"
"ds_read_b64 a[224:225], v32 offset:256\n"
"ds_read_b64 a[226:227], v33 offset:256\n"
"v_pk_add_f32 v[48:49], v[48:49], v[98:99]\n"
"v_pk_fma_f32 v[50:51], v[98:99], v[98:99], v[50:51]\n"
"v_pk_add_f32 v[52:53], v[52:53], v[102:103]\n"
"v_pk_fma_f32 v[54:55], v[102:103], v[102:103], v[54:55]\n"
"v_pk_add_f32 v[56:57], v[56:57], v[106:107]\n"
"v_pk_fma_f32 v[58:59], v[106:107], v[106:107], v[58:59]\n"
"v_pk_add_f32 v[60:61], v[60:61], v[110:111]\n"
"v_pk_fma_f32 v[62:63], v[110:111], v[110:111], v[62:63]\n"
"WL 9\n"
"v_smfmac_f32_32x32x32_bf16 v[112:127], a[8:11], a[0:7], v41\n"
"LD a[228:231],v30,39552\n"
"LD a[232:235],v26,40640\n"
"ds_read_b64 a[236:237], v34 offset:256\n"
"ds_read_b64 a[238:239], v35 offset:256\n"
"WL 10\n"
"v_smfmac_f32_32x32x32_bf16 v[112:127], a[20:23], a[12:19], v41\n"
"LD a[240:243],v28,40640\n"
"LD a[244:247],v30,40640\n"
"ds_read_b64 a[248:249], v36 offset:256\n"
"ds_read_b64 a[250:251], v37 offset:256\n"
"WL 12\n"
"MZ v[128:143],a[32:35],a[24:27]\n"
"LD a[0:3],v26,41728\n"
"LD a[4:7],v28,41728\n"
"ds_read_b64 a[8:9], v38 offset:256\n"
"ds_read_b64 a[10:11], v39 offset:256\n"
"s_nop 1\n"
"v_pk_add_f32 v[48:49], v[48:49], v[112:113]\n"
"v_pk_fma_f32 v[50:51], v[112:113], v[112:113], v[50:51]\n"
"v_pk_add_f32 v[52:53], v[52:53], v[116:117]\n"
"v_pk_fma_f32 v[54:55], v[116:117], v[116:117], v[54:55]\n"
"v_pk_add_f32 v[56:57], v[56:57], v[120:121]\n"
"v_pk_fma_f32 v[58:59], v[120:121], v[120:121], v[58:59]\n"
"v_pk_add_f32 v[60:61], v[60:61], v[124:125]\n"
"v_pk_fma_f32 v[62:63], v[124:125], v[124:125], v[62:63]\n"
"WL 12\n"
"v_smfmac_f32_32x32x32_bf16 v[128:143], a[224:227], a[216:223], v41\n"
"LD a[12:15],v31,41728\n"
"LD a[20:23],v40,640\n"
"WL 10\n"
"v_smfmac_f32_32x32x32_bf16 v[128:143], a[236:239], a[228:235], v41\n"
"LD a[24:27],v27,39552\n"
"LD a[28:31],v29,39552\n"
"ds_read_b64 a[32:33], v32 offset:320\n"
"ds_read_b64 a[34:35], v33 offset:320\n"
"v_pk_add_f32 v[48:49], v[48:49], v[114:115]\n"
"v_pk_fma_f32 v[50:51], v[114:115], v[114:115], v[50:51]\n"
"v_pk_add_f32 v[52:53], v[52:53], v[118:119]\n"
"v_pk_fma_f32 v[54:55], v[118:119], v[118:119], v[54:55]\n"
"v_pk_add_f32 v[56:57], v[56:57], v[122:123]\n"
"v_pk_fma_f32 v[58:59], v[122:123], v[122:123], v[58:59]\n"
"v_pk_add_f32 v[60:61], v[60:61], v[126:127]\n"
"v_pk_fma_f32 v[62:63], v[126:127], v[126:127], v[62:63]\n"
"WL 9\n"
"v_smfmac_f32_32x32x32_bf16 v[128:143], a[248:251], a[240:247], v41\n"
"LD a[216:219],v31,39552\n"
"LD a[220:223],v27,40640\n"
"ds_read_b64 a[224:225], v34 offset:320\n"
"ds_read_b64 a[226:227], v35 offset:320\n"
"WL 10\n"
"v_smfmac_f32_32x32x32_bf16 v[128:143], a[8:11], a[0:7], v41\n"
"LD a[228:231],v29,40640\n"
"LD a[232:235],v31,40640\n"
"ds_read_b64 a[236:237], v36 offset:320\n"
"ds_read_b64 a[238:239], v37 offset:320\n"
"WL 12\n"
"MZ v[144:159],a[20:23],a[12:15]\n"
"LD a[240:243],v27,41728\n"
"LD a[244:247],v29,41728\n"
"ds_read_b64 a[248:249], v38 offset:320\n"
"ds_read_b64 a[250:251], v39 offset:320\n"
"s_barrier\n"
"WL 12\n"
"v_smfmac_f32_32x32x32_bf16 v[144:159], a[32:35], a[24:31], v41\n"
"LD a[0:3],v30,2176\n"
"LD a[8:11],v40,768\n"
"WL 10\n"
"v_smfmac_f32_32x32x32_bf16 v[144:159], a[224:227], a[216:223], v41\n"
"LD a[12:15],v26,0\n"
"LD a[16:19],v28,0\n"
"ds_read_b64 a[20:21], v32 offset:384\n"
"ds_read_b64 a[22:23], v33 offset:384\n"
"ds_write_b128 v24, a[56:59] offset:19776\n"
"v_pk_add_f32 v[48:49], v[48:49], v[128:129]\n"
"v_pk_fma_f32 v[50:51], v[128:129], v[128:129], v[50:51]\n"
"v_pk_add_f32 v[52:53], v[52:53], v[132:133]\n"
"v_pk_fma_f32 v[54:55], v[132:133], v[132:133], v[54:55]\n"
"ds_write_b128 v24, a[60:63] offset:20800\n"
"v_pk_add_f32 v[56:57], v[56:57], v[136:137]\n"
"v_pk_fma_f32 v[58:59], v[136:137], v[136:137], v[58:59]\n"
"v_pk_add_f32 v[60:61], v[60:61], v[140:141]\n"
"v_pk_fma_f32 v[62:63], v[140:141], v[140:141], v[62:63]\n"
"ds_write_b128 v24, a[64:67] offset:21824\n"
"v_pk_add_f32 v[48:49], v[48:49], v[130:131]\n"
"v_pk_fma_f32 v[50:51], v[130:131], v[130:131], v[50:51]\n"
"v_pk_add_f32 v[52:53], v[52:53], v[134:135]\n"
"v_pk_fma_f32 v[54:55], v[134:135], v[134:135], v[54:55]\n"
"ds_write_b128 v24, a[68:71] offset:22848\n"
"v_pk_add_f32 v[56:57], v[56:57], v[138:139]\n"
"v_pk_fma_f32 v[58:59], v[138:139], v[138:139], v[58:59]\n"
"v_pk_add_f32 v[60:61], v[60:61], v[142:143]\n"
"v_pk_fma_f32 v[62:63], v[142:143], v[142:143], v[62:63]\n"
"s_waitcnt vmcnt(21)\n"
"ds_write_b128 v25, a[72:75] offset:19776\n"
"WL 11\n"
"v_smfmac_f32_32x32x32_bf16 v[144:159], a[236:239], a[228:235], v41\n"
"LD a[24:27],v30,0\n"
"LD a[28:31],v26,1088\n"
"ds_read_b64 a[32:33], v34 offset:384\n"
"ds_read_b64 a[34:35], v35 offset:384\n"
"v_smfmac_f32_32x32x32_bf16 v[144:159], a[248:251], a[240:247], v41\n"
"LD a[216:219],v28,1088\n"
"LD a[220:223],v30,1088\n"
"ds_read_b64 a[224:225], v36 offset:384\n"
"ds_read_b64 a[226:227], v37 offset:384\n"
"WL 12\n"
"MZ v[160:175],a[8:11],a[0:3]\n"
"LD a[228:231],v26,2176\n"
"LD a[232:235],v28,2176\n"
"ds_read_b64 a[236:237], v38 offset:384\n"
"ds_read_b64 a[238:239], v39 offset:384\n"
"s_nop 1\n"
"v_pk_add_f32 v[48:49], v[48:49], v[144:145]\n"
"v_pk_fma_f32 v[50:51], v[144:145], v[144:145], v[50:51]\n"
"v_pk_add_f32 v[52:53], v[52:53], v[148:149]\n"
"v_pk_fma_f32 v[54:55], v[148:149], v[148:149], v[54:55]\n"
"v_pk_add_f32 v[56:57], v[56:57], v[152:153]\n"
"v_pk_fma_f32 v[58:59], v[152:153], v[152:153], v[58:59]\n"
"v_pk_add_f32 v[60:61], v[60:61], v[156:157]\n"
"v_pk_fma_f32 v[62:63], v[156:157], v[156:157], v[62:63]\n"
"v_smfmac_f32_32x32x32_bf16 v[160:175], a[20:23], a[12:19], v41\n"
"LD a[240:243],v31,2176\n"
"LD a[248:251],v40,896\n"
"WL 10\n"
"s_nop 0\n"
"v_smfmac_f32_32x32x32_bf16 v[160:175], a[32:35], a[24:31], v41\n"
"LD a[0:3],v27,0\n"
"LD a[4:7],v29,0\n"
"ds_read_b64 a[8:9], v32 offset:448\n"
"ds_read_b64 a[10:11], v33 offset:448\n"
"v_pk_add_f32 v[48:49], v[48:49], v[146:147]\n"
"v_pk_fma_f32 v[50:51], v[146:147], v[146:147], v[50:51]\n"
"v_pk_add_f32 v[52:53], v[52:53], v[150:151]\n"
"v_pk_fma_f32 v[54:55], v[150:151], v[150:151], v[54:55]\n"
"v_pk_add_f32 v[56:57], v[56:57], v[154:155]\n"
"v_pk_fma_f32 v[58:59], v[154:155], v[154:155], v[58:59]\n"
"v_pk_add_f32 v[60:61], v[60:61], v[158:159]\n"
"v_pk_fma_f32 v[62:63], v[158:159], v[158:159], v[62:63]\n"
"WL 10\n"
"v_smfmac_f32_32x32x32_bf16 v[160:175], a[224:227], a[216:223], v41\n"
"LD a[12:15],v31,0\n"
"LD a[16:19],v27,1088\n"
"ds_read_b64 a[20:21], v34 offset:448\n"
"ds_read_b64 a[22:23], v35 offset:448\n"
"WL 10\n"
"v_smfmac_f32_32x32x32_bf16 v[160:175], a[236:239], a[228:235], v41\n"
"LD a[24:27],v29,1088\n"
"LD a[28:31],v31,1088\n"
"ds_read_b64 a[32:33], v36 offset:448\n"
"ds_read_b64 a[34:35], v37 offset:448\n"
"WL 12\n"
"MZ v[176:191],a[248:251],a[240:243]\n"
"LD a[216:219],v27,2176\n"
"LD a[220:223],v29,2176\n"
"ds_read_b64 a[224:225], v38 offset:448\n"
"ds_read_b64 a[226:227], v39 offset:448\n"
"s_barrier\n"
"WL 12\n"
"v_smfmac_f32_32x32x32_bf16 v[176:191], a[8:11], a[0:7], v41\n"
"LD a[228:231],v30,21952\n"
"LD a[236:239],v40,1024\n"
"WL 10\n"
"v_smfmac_f32_32x32x32_bf16 v[176:191], a[20:23], a[12:19], v41\n"
"LD a[240:243],v26,19776\n"
"LD a[244:247],v28,19776\n"
"ds_read_b64 a[248:249], v32 offset:512\n"
"ds_read_b64 a[250:251], v33 offset:512\n"
"ds_write_b128 v24, a[76:79] offset:39552\n"
"v_pk_add_f32 v[48:49], v[48:49], v[160:161]\n"
"v_pk_fma_f32 v[50:51], v[160:161], v[160:161], v[50:51]\n"
"v_pk_add_f32 v[52:53], v[52:53], v[164:165]\n"
"v_pk_fma_f32 v[54:55], v[164:165], v[164:165], v[54:55]\n"
"ds_write_b128 v24, a[80:83] offset:40576\n"
"v_pk_add_f32 v[56:57], v[56:57], v[168:169]\n"
"v_pk_fma_f32 v[58:59], v[168:169], v[168:169], v[58:59]\n"
"v_pk_add_f32 v[60:61], v[60:61], v[172:173]\n"
"v_pk_fma_f32 v[62:63], v[172:173], v[172:173], v[62:63]\n"
"ds_write_b128 v24, a[84:87] offset:41600\n"
"v_pk_add_f32 v[48:49], v[48:49], v[162:163]\n"
"v_pk_fma_f32 v[50:51], v[162:163], v[162:163], v[50:51]\n"
"v_pk_add_f32 v[52:53], v[52:53], v[166:167]\n"
"v_pk_fma_f32 v[54:55], v[166:167], v[166:167], v[54:55]\n"
"ds_write_b128 v24, a[88:91] offset:42624\n"
"v_pk_add_f32 v[56:57], v[56:57], v[170:171]\n"
"v_pk_fma_f32 v[58:59], v[170:171], v[170:171], v[58:59]\n"
"v_pk_add_f32 v[60:61], v[60:61], v[174:175]\n"
"v_pk_fma_f32 v[62:63], v[174:175], v[174:175], v[62:63]\n"
"s_waitcnt vmcnt(20)\n"
"ds_write_b128 v25, a[92:95] offset:39552\n"
"WL 11\n"
"v_smfmac_f32_32x32x32_bf16 v[176:191], a[32:35], a[24:31], v41\n"
"LD a[0:3],v30,19776\n"
"LD a[4:7],v26,20864\n"
"ds_read_b64 a[8:9], v34 offset:512\n"
"ds_read_b64 a[10:11], v35 offset:512\n"
"v_smfmac_f32_32x32x32_bf16 v[176:191], a[224:227], a[216:223], v41\n"
"LD a[12:15],v28,20864\n"
"LD a[16:19],v30,20864\n"
"ds_read_b64 a[20:21], v36 offset:512\n"
"ds_read_b64 a[22:23], v37 offset:512\n"
"WL 12\n"
"MZ v[192:207],a[236:239],a[228:231]\n"
"LD a[24:27],v26,21952\n"
"LD a[28:31],v28,21952\n"
"ds_read_b64 a[32:33], v38 offset:512\n"
"ds_read_b64 a[34:35], v39 offset:512\n"
"s_nop 1\n"
"v_pk_add_f32 v[48:49], v[48:49], v[176:177]\n"
"v_pk_fma_f32 v[50:51], v[176:177], v[176:177], v[50:51]\n"
"v_pk_add_f32 v[52:53], v[52:53], v[180:181]\n"
"v_pk_fma_f32 v[54:55], v[180:181], v[180:181], v[54:55]\n"
"v_pk_add_f32 v[56:57], v[56:57], v[184:185]\n"
"v_pk_fma_f32 v[58:59], v[184:185], v[184:185], v[58:59]\n"
"v_pk_add_f32 v[60:61], v[60:61], v[188:189]\n"
"v_pk_fma_f32 v[62:63], v[188:189], v[188:189], v[62:63]\n"
"v_smfmac_f32_32x32x32_bf16 v[192:207], a[248:251], a[240:247], v41\n"
"LD a[216:219],v31,21952\n"
"LD a[224:227],v40,1152\n"
"WL 10\n"
"s_nop 0\n"
"v_smfmac_f32_32x32x32_bf16 v[192:207], a[8:11], a[0:7], v41\n"
"LD a[228:231],v27,19776\n"
"LD a[232:235],v29,19776\n"
"ds_read_b64 a[236:237], v32 offset:576\n"
"ds_read_b64 a[238:239], v33 offset:576\n"
"v_pk_add_f32 v[48:49], v[48:49], v[178:179]\n"
"v_pk_fma_f32 v[50:51], v[178:179], v[178:179], v[50:51]\n"
"v_pk_add_f32 v[52:53], v[52:53], v[182:183]\n"
"v_pk_fma_f32 v[54:55], v[182:183], v[182:183], v[54:55]\n"
"v_pk_add_f32 v[56:57], v[56:57], v[186:187]\n"
"v_pk_fma_f32 v[58:59], v[186:187], v[186:187], v[58:59]\n"
"v_pk_add_f32 v[60:61], v[60:61], v[190:191]\n"
"v_pk_fma_f32 v[62:63], v[190:191], v[190:191], v[62:63]\n"
"WL 10\n"
"v_smfmac_f32_32x32x32_bf16 v[192:207], a[20:23], a[12:19], v41\n"
"LD a[240:243],v31,19776\n"
"LD a[244:247],v27,20864\n"
"ds_read_b64 a[248:249], v34 offset:576\n"
"ds_read_b64 a[250:251], v35 offset:576\n"
"WL 10\n"
"v_smfmac_f32_32x32x32_bf16 v[192:207], a[32:35], a[24:31], v41\n"
"LD a[0:3],v29,20864\n"
"LD a[4:7],v31,20864\n"
"ds_read_b64 a[8:9], v36 offset:576\n"
"ds_read_b64 a[10:11], v37 offset:576\n"
"WL 12\n"
"MZ v[208:223],a[224:227],a[216:219]\n"
"LD a[12:15],v27,21952\n"
"LD a[16:19],v29,21952\n"
"ds_read_b64 a[20:21], v38 offset:576\n"
"ds_read_b64 a[22:23], v39 offset:576\n"
"s_nop 1\n"
"v_pk_add_f32 v[48:49], v[48:49], v[192:193]\n"
"v_pk_fma_f32 v[50:51], v[192:193], v[192:193], v[50:51]\n"
"v_pk_add_f32 v[52:53], v[52:53], v[196:197]\n"
"v_pk_fma_f32 v[54:55], v[196:197], v[196:197], v[54:55]\n"
"v_pk_add_f32 v[56:57], v[56:57], v[200:201]\n"
"v_pk_fma_f32 v[58:59], v[200:201], v[200:201], v[58:59]\n"
"v_pk_add_f32 v[60:61], v[60:61], v[204:205]\n"
"v_pk_fma_f32 v[62:63], v[204:205], v[204:205], v[62:63]\n"
"s_barrier\n"
"WL 12\n"
"v_smfmac_f32_32x32x32_bf16 v[208:223], a[236:239], a[228:235], v41\n"
"LD a[24:27],v30,41728\n"
"LD a[32:35],v40,1280\n"
"WL 10\n"
"s_nop 0\n"
"v_smfmac_f32_32x32x32_bf16 v[208:223], a[248:251], a[240:247], v41\n"
"LD a[216:219],v26,39552\n"
"LD a[220:223],v28,39552\n"
"ds_read_b64 a[224:225], v32 offset:640\n"
"ds_read_b64 a[226:227], v33 offset:640\n"
"v_pk_add_f32 v[48:49], v[48:49], v[194:195]\n"
"v_pk_fma_f32 v[50:51], v[194:195], v[194:195], v[50:51]\n"
"v_pk_add_f32 v[52:53], v[52:53], v[198:199]\n"
"v_pk_fma_f32 v[54:55], v[198:199], v[198:199], v[54:55]\n"
"v_pk_add_f32 v[56:57], v[56:57], v[202:203]\n"
"v_pk_fma_f32 v[58:59], v[202:203], v[202:203], v[58:59]\n"
"v_pk_add_f32 v[60:61], v[60:61], v[206:207]\n"
"v_pk_fma_f32 v[62:63], v[206:207], v[206:207], v[62:63]\n"
"WL 8\n"
"v_smfmac_f32_32x32x32_bf16 v[208:223], a[8:11], a[0:7], v41\n"
"LD a[228:231],v30,39552\n"
"LD a[232:235],v26,40640\n"
"ds_read_b64 a[236:237], v34 offset:640\n"
"ds_read_b64 a[238:239], v35 offset:640\n"
"WL 10\n"
"v_smfmac_f32_32x32x32_bf16 v[208:223], a[20:23], a[12:19], v41\n"
"LD a[240:243],v28,40640\n"
"LD a[244:247],v30,40640\n"
"ds_read_b64 a[248:249], v36 offset:640\n"
"ds_read_b64 a[250:251], v37 offset:640\n"
"s_waitcnt vmcnt(8) lgkmcnt(12)\n"
"MZ v[224:239],a[32:35],a[24:27]\n"
"LD a[0:3],v26,41728\n"
"LD a[4:7],v28,41728\n"
"ds_read_b64 a[8:9], v38 offset:640\n"
"ds_read_b64 a[10:11], v39 offset:640\n"
"s_nop 1\n"
"v_pk_add_f32 v[48:49], v[48:49], v[208:209]\n"
"v_pk_fma_f32 v[50:51], v[208:209], v[208:209], v[50:51]\n"
"v_pk_add_f32 v[52:53], v[52:53], v[212:213]\n"
"v_pk_fma_f32 v[54:55], v[212:213], v[212:213], v[54:55]\n"
"v_pk_add_f32 v[56:57], v[56:57], v[216:217]\n"
"v_pk_fma_f32 v[58:59], v[216:217], v[216:217], v[58:59]\n"
"v_pk_add_f32 v[60:61], v[60:61], v[220:221]\n"
"v_pk_fma_f32 v[62:63], v[220:221], v[220:221], v[62:63]\n"
"WL 12\n"
"v_smfmac_f32_32x32x32_bf16 v[224:239], a[224:227], a[216:223], v41\n"
"LD a[12:15],v31,41728\n"
"LD a[20:23],v40,1408\n"
"WL 10\n"
"v_smfmac_f32_32x32x32_bf16 v[224:239], a[236:239], a[228:235], v41\n"
"LD a[24:27],v27,39552\n"
"LD a[28:31],v29,39552\n"
"ds_read_b64 a[32:33], v32 offset:704\n"
"ds_read_b64 a[34:35], v33 offset:704\n"
"v_pk_add_f32 v[48:49], v[48:49], v[210:211]\n"
"v_pk_fma_f32 v[50:51], v[210:211], v[210:211], v[50:51]\n"
"v_pk_add_f32 v[52:53], v[52:53], v[214:215]\n"
"v_pk_fma_f32 v[54:55], v[214:215], v[214:215], v[54:55]\n"
"v_pk_add_f32 v[56:57], v[56:57], v[218:219]\n"
"v_pk_fma_f32 v[58:59], v[218:219], v[218:219], v[58:59]\n"
"v_pk_add_f32 v[60:61], v[60:61], v[222:223]\n"
"v_pk_fma_f32 v[62:63], v[222:223], v[222:223], v[62:63]\n"
"WL 9\n"
"v_smfmac_f32_32x32x32_bf16 v[224:239], a[248:251], a[240:247], v41\n"
"LD a[216:219],v31,39552\n"
"LD a[220:223],v27,40640\n"
"ds_read_b64 a[224:225], v34 offset:704\n"
"ds_read_b64 a[226:227], v35 offset:704\n"
"WL 10\n"
"v_smfmac_f32_32x32x32_bf16 v[224:239], a[8:11], a[0:7], v41\n"
"LD a[228:231],v29,40640\n"
"LD a[232:235],v31,40640\n"
"ds_read_b64 a[236:237], v36 offset:704\n"
"ds_read_b64 a[238:239], v37 offset:704\n"
"WL 12\n"
"MZ v[240:255],a[20:23],a[12:15]\n"
"LD a[240:243],v27,41728\n"
"LD a[244:247],v29,41728\n"
"ds_read_b64 a[248:249], v38 offset:704\n"
"ds_read_b64 a[250:251], v39 offset:704\n"
"s_nop 1\n"
"v_pk_add_f32 v[48:49], v[48:49], v[224:225]\n"
"v_pk_fma_f32 v[50:51], v[224:225], v[224:225], v[50:51]\n"
"v_pk_add_f32 v[52:53], v[52:53], v[228:229]\n"
"v_pk_fma_f32 v[54:55], v[228:229], v[228:229], v[54:55]\n"
"v_pk_add_f32 v[56:57], v[56:57], v[232:233]\n"
"v_pk_fma_f32 v[58:59], v[232:233], v[232:233], v[58:59]\n"
"v_pk_add_f32 v[60:61], v[60:61], v[236:237]\n"
"v_pk_fma_f32 v[62:63], v[236:237], v[236:237], v[62:63]\n"
"WL 12\n"
"v_smfmac_f32_32x32x32_bf16 v[240:255], a[32:35], a[24:31], v41\n"
"WL 8\n"
"v_smfmac_f32_32x32x32_bf16 v[240:255], a[224:227], a[216:223], v41\n"
"v_pk_add_f32 v[48:49], v[48:49], v[226:227]\n"
"v_pk_fma_f32 v[50:51], v[226:227], v[226:227], v[50:51]\n"
"v_pk_add_f32 v[52:53], v[52:53], v[230:231]\n"
"v_pk_fma_f32 v[54:55], v[230:231], v[230:231], v[54:55]\n"
"v_pk_add_f32 v[56:57], v[56:57], v[234:235]\n"
"v_pk_fma_f32 v[58:59], v[234:235], v[234:235], v[58:59]\n"
"v_pk_add_f32 v[60:61], v[60:61], v[238:239]\n"
"v_pk_fma_f32 v[62:63], v[238:239], v[238:239], v[62:63]\n"
"WL 4\n"
"s_nop 0\n"
"v_smfmac_f32_32x32x32_bf16 v[240:255], a[236:239], a[228:235], v41\n"
"WL 0\n"
"v_smfmac_f32_32x32x32_bf16 v[240:255], a[248:251], a[240:247], v41\n"
"s_nop 11\n"
"v_pk_add_f32 v[48:49], v[48:49], v[240:241]\n"
"v_pk_fma_f32 v[50:51], v[240:241], v[240:241], v[50:51]\n"
"v_pk_add_f32 v[52:53], v[52:53], v[244:245]\n"
"v_pk_fma_f32 v[54:55], v[244:245], v[244:245], v[54:55]\n"
"v_pk_add_f32 v[56:57], v[56:57], v[248:249]\n"
"v_pk_fma_f32 v[58:59], v[248:249], v[248:249], v[58:59]\n"
"v_pk_add_f32 v[60:61], v[60:61], v[252:253]\n"
"v_pk_fma_f32 v[62:63], v[252:253], v[252:253], v[62:63]\n"
"v_pk_add_f32 v[48:49], v[48:49], v[242:243]\n"
"v_pk_fma_f32 v[50:51], v[242:243], v[242:243], v[50:51]\n"
"v_pk_add_f32 v[52:53], v[52:53], v[246:247]\n"
"v_pk_fma_f32 v[54:55], v[246:247], v[246:247], v[54:55]\n"
"v_pk_add_f32 v[56:57], v[56:57], v[250:251]\n"
"v_pk_fma_f32 v[58:59], v[250:251], v[250:251], v[58:59]\n"
"v_pk_add_f32 v[60:61], v[60:61], v[254:255]\n"
"v_pk_fma_f32 v[62:63], v[254:255], v[254:255], v[62:63]\n"
"v_add_f32 v48, v48, v49\n"
"v_add_f32 v52, v52, v53\n"
"v_add_f32 v56, v56, v57\n"
"v_add_f32 v60, v60, v61\n"
"v_add_f32 v50, v50, v51\n"
"v_add_f32 v54, v54, v55\n"
"v_add_f32 v58, v58, v59\n"
"v_add_f32 v62, v62, v63\n"
"v_permlane32_swap_b32 v48, v50\n"
"v_permlane32_swap_b32 v52, v54\n"
"v_permlane32_swap_b32 v56, v58\n"
"v_permlane32_swap_b32 v60, v62\n"
"v_add_f32 v48, v48, v50\n"
"v_add_f32 v52, v52, v54\n"
"v_add_f32 v56, v56, v58\n"
"v_add_f32 v60, v60, v62\n"
"v_mul_f32 v48, 0x3c2aaaab, v48\n"
"v_mul_f32 v52, 0x3c2aaaab, v52\n"
"v_mul_f32 v56, 0x3c2aaaab, v56\n"
"v_mul_f32 v60, 0x3c2aaaab, v60\n"
"v_mov_b32 v50, v48\n"
"v_mov_b32 v54, v52\n"
"v_mov_b32 v58, v56\n"
"v_mov_b32 v62, v60\n"
"v_permlane32_swap_b32 v48, v50\n"
"v_permlane32_swap_b32 v52, v54\n"
"v_permlane32_swap_b32 v56, v58\n"
"v_permlane32_swap_b32 v60, v62\n"
"v_fma_f32 v50, -v48, v48, v50\n"
"v_fma_f32 v54, -v52, v52, v54\n"
"v_fma_f32 v58, -v56, v56, v58\n"
"v_fma_f32 v62, -v60, v60, v62\n"
"v_max_f32 v50, 0, v50\n"
"v_max_f32 v54, 0, v54\n"
"v_max_f32 v58, 0, v58\n"
"v_max_f32 v62, 0, v62\n"
"v_add_f32 v50, 0x3727c5ac, v50\n"
"v_add_f32 v54, 0x3727c5ac, v54\n"
"v_add_f32 v58, 0x3727c5ac, v58\n"
"v_add_f32 v62, 0x3727c5ac, v62\n"
"v_rsq_f32 v50, v50\n"
"v_rsq_f32 v54, v54\n"
"v_rsq_f32 v58, v58\n"
"v_rsq_f32 v62, v62\n"
"v_mul_f32 v50, s21, v50\n"
"v_mul_f32 v54, s21, v54\n"
"v_mul_f32 v58, s21, v58\n"
"v_mul_f32 v62, s21, v62\n"
"v_mul_f32 v51, v48, v50\n"
"v_mul_f32 v55, v52, v54\n"
"v_mul_f32 v59, v56, v58\n"
"v_mul_f32 v63, v60, v62\n"
"LD a[232:235],v1,0\n"
"LD a[236:239],v1,1024\n"
"LD a[240:243],v1,2048\n"
"LD a[244:247],v1,3072\n"
"LD a[248:251],v1,4096\n"
"LD a[252:255],v1,5120\n"
"NR v[64:65],v[50:51]\n"
"NR v[66:67],v[50:51]\n"
"NR v[80:81],v[50:51]\n"
"NR v[82:83],v[50:51]\n"
"NR v[68:69],v[54:55]\n"
"NR v[70:71],v[54:55]\n"
"NR v[84:85],v[54:55]\n"
"NR v[86:87],v[54:55]\n"
"NR v[72:73],v[58:59]\n"
"NR v[74:75],v[58:59]\n"
"NR v[88:89],v[58:59]\n"
"NR v[90:91],v[58:59]\n"
"NR v[76:77],v[62:63]\n"
"NR v[78:79],v[62:63]\n"
"NR v[92:93],v[62:63]\n"
"NR v[94:95],v[62:63]\n"
"CB v16,v64,v65\n"
"CB v17,v66,v67\n"
"CB v18,v80,v81\n"
"CB v19,v82,v83\n"
"CB v20,v68,v69\n"
"CB v21,v70,v71\n"
"CB v22,v84,v85\n"
"CB v23,v86,v87\n"
"CB v32,v72,v73\n"
"CB v33,v74,v75\n"
"CB v34,v88,v89\n"
"CB v35,v90,v91\n"
"CB v36,v76,v77\n"
"CB v37,v78,v79\n"
"CB v38,v92,v93\n"
"CB v39,v94,v95\n"
"NR v[96:97],v[50:51]\n"
"NR v[98:99],v[50:51]\n"
"NR v[112:113],v[50:51]\n"
"NR v[114:115],v[50:51]\n"
"NR v[100:101],v[54:55]\n"
"NR v[102:103],v[54:55]\n"
"NR v[116:117],v[54:55]\n"
"NR v[118:119],v[54:55]\n"
"NR v[104:105],v[58:59]\n"
"NR v[106:107],v[58:59]\n"
"NR v[120:121],v[58:59]\n"
"NR v[122:123],v[58:59]\n"
"NR v[108:109],v[62:63]\n"
"NR v[110:111],v[62:63]\n"
"NR v[124:125],v[62:63]\n"
"NR v[126:127],v[62:63]\n"
"CB v24,v96,v97\n"
"CB v25,v98,v99\n"
"CB v26,v112,v113\n"
"CB v27,v114,v115\n"
"CB v28,v100,v101\n"
"CB v29,v102,v103\n"
"CB v30,v116,v117\n"
"CB v31,v118,v119\n"
"CB v40,v104,v105\n"
"CB v41,v106,v107\n"
"CB v42,v120,v121\n"
"CB v43,v122,v123\n"
"CB v44,v108,v109\n"
"CB v45,v110,v111\n"
"CB v46,v124,v125\n"
"CB v47,v126,v127\n"
"NR v[128:129],v[50:51]\n"
"NR v[130:131],v[50:51]\n"
"NR v[144:145],v[50:51]\n"
"NR v[146:147],v[50:51]\n"
"NR v[132:133],v[54:55]\n"
"NR v[134:135],v[54:55]\n"
"NR v[148:149],v[54:55]\n"
"NR v[150:151],v[54:55]\n"
"NR v[136:137],v[58:59]\n"
"NR v[138:139],v[58:59]\n"
"NR v[152:153],v[58:59]\n"
"NR v[154:155],v[58:59]\n"
"NR v[140:141],v[62:63]\n"
"NR v[142:143],v[62:63]\n"
"NR v[156:157],v[62:63]\n"
"NR v[158:159],v[62:63]\n"
"CB v64,v128,v129\n"
"CB v65,v130,v131\n"
"CB v66,v144,v145\n"
"CB v67,v146,v147\n"
"CB v68,v132,v133\n"
"CB v69,v134,v135\n"
"CB v70,v148,v149\n"
"CB v71,v150,v151\n"
"CB v80,v136,v137\n"
"CB v81,v138,v139\n"
"CB v82,v152,v153\n"
"CB v83,v154,v155\n"
"CB v84,v140,v141\n"
"CB v85,v142,v143\n"
"CB v86,v156,v157\n"
"CB v87,v158,v159\n"
"NR v[160:161],v[50:51]\n"
"NR v[162:163],v[50:51]\n"
"NR v[176:177],v[50:51]\n"
"NR v[178:179],v[50:51]\n"
"NR v[164:165],v[54:55]\n"
"NR v[166:167],v[54:55]\n"
"NR v[180:181],v[54:55]\n"
"NR v[182:183],v[54:55]\n"
"NR v[168:169],v[58:59]\n"
"NR v[170:171],v[58:59]\n"
"NR v[184:185],v[58:59]\n"
"NR v[186:187],v[58:59]\n"
"NR v[172:173],v[62:63]\n"
"NR v[174:175],v[62:63]\n"
"NR v[188:189],v[62:63]\n"
"NR v[190:191],v[62:63]\n"
"CB v72,v160,v161\n"
"WL 0\n"
"s_nop 0\n"
"MZ v[128:143],a[232:235],v[16:19]\n"
"CB v73,v162,v163\n"
"CB v74,v176,v177\n"
"CB v75,v178,v179\n"
"CB v76,v164,v165\n"
"CB v77,v166,v167\n"
"CB v78,v180,v181\n"
"CB v79,v182,v183\n"
"CB v88,v168,v169\n"
"CB v89,v170,v171\n"
"CB v90,v184,v185\n"
"CB v91,v186,v187\n"
"CB v92,v172,v173\n"
"MB v[128:143],a[236:239],v[24:27]\n"
"CB v93,v174,v175\n"
"CB v94,v188,v189\n"
"CB v95,v190,v191\n"
"NR v[192:193],v[50:51]\n"
"NR v[194:195],v[50:51]\n"
"NR v[208:209],v[50:51]\n"
"NR v[210:211],v[50:51]\n"
"NR v[196:197],v[54:55]\n"
"NR v[198:199],v[54:55]\n"
"NR v[212:213],v[54:55]\n"
"NR v[214:215],v[54:55]\n"
"NR v[200:201],v[58:59]\n"
"NR v[202:203],v[58:59]\n"
"NR v[216:217],v[58:59]\n"
"NR v[218:219],v[58:59]\n"
"NR v[204:205],v[62:63]\n"
"NR v[206:207],v[62:63]\n"
"NR v[220:221],v[62:63]\n"
"NR v[222:223],v[62:63]\n"
"CB v96,v192,v193\n"
"MB v[128:143],a[240:243],v[64:67]\n"
"CB v97,v194,v195\n"
"CB v98,v208,v209\n"
"CB v99,v210,v211\n"
"CB v100,v196,v197\n"
"CB v101,v198,v199\n"
"CB v102,v212,v213\n"
"CB v103,v214,v215\n"
"CB v112,v200,v201\n"
"CB v113,v202,v203\n"
"CB v114,v216,v217\n"
"CB v115,v218,v219\n"
"CB v116,v204,v205\n"
"MB v[128:143],a[244:247],v[72:75]\n"
"CB v117,v206,v207\n"
"CB v118,v220,v221\n"
"CB v119,v222,v223\n"
"NR v[224:225],v[50:51]\n"
"NR v[226:227],v[50:51]\n"
"NR v[240:241],v[50:51]\n"
"NR v[242:243],v[50:51]\n"
"NR v[228:229],v[54:55]\n"
"NR v[230:231],v[54:55]\n"
"NR v[244:245],v[54:55]\n"
"NR v[246:247],v[54:55]\n"
"NR v[232:233],v[58:59]\n"
"NR v[234:235],v[58:59]\n"
"NR v[248:249],v[58:59]\n"
"NR v[250:251],v[58:59]\n"
"NR v[236:237],v[62:63]\n"
"NR v[238:239],v[62:63]\n"
"NR v[252:253],v[62:63]\n"
"NR v[254:255],v[62:63]\n"
"CB v104,v224,v225\n"
"MB v[128:143],a[248:251],v[96:99]\n"
"CB v105,v226,v227\n"
"CB v106,v240,v241\n"
"CB v107,v242,v243\n"
"CB v108,v228,v229\n"
"CB v109,v230,v231\n"
"CB v110,v244,v245\n"
"CB v111,v246,v247\n"
"CB v120,v232,v233\n"
"CB v121,v234,v235\n"
"CB v122,v248,v249\n"
"CB v123,v250,v251\n"
"CB v124,v236,v237\n"
"MB v[128:143],a[252:255],v[104:107]\n"
"CB v125,v238,v239\n"
"CB v126,v252,v253\n"
"CB v127,v254,v255\n"
"AR v63,a208\n"
"AR v56,a209\n"
"AR v57,a210\n"
"AR v58,a211\n"
"AR v59,a212\n"
"v_mov_b32 v61, v14\n"
"s_nop 2\n"
"GA v128\n"
"GA v129\n"
"CH v134,v134,v135\n"
"CH v142,v142,v143\n"
"MZ v[144:159],a[232:235],v[20:23]\n"
"GL v128\n"
"GH v129\n"
"PF v135,v134,v134,s56\n"
"PF v143,v142,v142,s56\n"
"GA v130\n"
"GA v131\n"
"PM v135,v135,v134\n"
"PM v143,v143,v142\n"
"MB v[144:159],a[236:239],v[28:31]\n"
"GL v130\n"
"GH v131\n"
"EX v135,0\n"
"GA v132\n"
"GA v133\n"
"EX v143,0\n"
"MB v[144:159],a[240:243],v[68:71]\n"
"GL v132\n"
"GH v133\n"
"EX v135,1\n"
"EX v143,1\n"
"GA v136\n"
"GA v137\n"
"PF v135,v135,s57,s57\n"
"MB v[144:159],a[244:247],v[76:79]\n"
"GL v136\n"
"GH v137\n"
"PF v143,v143,s57,s57\n"
"RC v135,0\n"
"GA v138\n"
"GA v139\n"
"RC v143,0\n"
"MB v[144:159],a[248:251],v[100:103]\n"
"GL v138\n"
"GH v139\n"
"RC v135,1\n"
"GA v140\n"
"GA v141\n"
"RC v143,1\n"
"MB v[144:159],a[252:255],v[108:111]\n"
"GL v140\n"
"GH v141\n"
"PM v134,v134,v135\n"
"PM v142,v142,v143\n"
"PM v51,v134,v134\n"
"PM v55,v142,v142\n"
"AW a3,v134\n"
"AW a7,v142\n"
"WL 6\n"
"v_or_b32 v132, v132, v133\n"
"v_or_b32 v130, v130, v131\n"
"v_or_b32 v128, v128, v129\n"
"PM v50,v132,v132\n"
"GA v144\n"
"GA v145\n"
"CH v150,v150,v151\n"
"CH v158,v158,v159\n"
"MZ v[224:239],a[232:235],v[32:35]\n"
"GL v144\n"
"GH v145\n"
"PF v151,v150,v150,s56\n"
"PF v159,v158,v158,s56\n"
"PM v49,v130,v130\n"
"PM v48,v128,v128\n"
"GA v146\n"
"GA v147\n"
"AW a2,v132\n"
"AW a1,v130\n"
"AW a0,v128\n"
"PM v151,v151,v150\n"
"PM v159,v159,v158\n"
"MB v[224:239],a[236:239],v[40:43]\n"
"GL v146\n"
"GH v147\n"
"EX v151,0\n"
"GA v148\n"
"GA v149\n"
"EX v159,0\n"
"MB v[224:239],a[240:243],v[80:83]\n"
"GL v148\n"
"GH v149\n"
"EX v151,1\n"
"EX v159,1\n"
"GA v152\n"
"GA v153\n"
"PF v151,v151,s57,s57\n"
"MB v[224:239],a[244:247],v[88:91]\n"
"GL v152\n"
"GH v153\n"
"PF v159,v159,s57,s57\n"
"WL 6\n"
"v_or_b32 v140, v140, v141\n"
"v_or_b32 v138, v138, v139\n"
"v_or_b32 v136, v136, v137\n"
"RC v151,0\n"
"PM v54,v140,v140\n"
"PM v53,v138,v138\n"
"PM v52,v136,v136\n"
"GA v154\n"
"GA v155\n"
"AW a6,v140\n"
"AW a5,v138\n"
"AW a4,v136\n"
"RC v159,0\n"
"MB v[224:239],a[248:251],v[112:115]\n"
"GL v154\n"
"GH v155\n"
"RC v151,1\n"
"GA v156\n"
"GA v157\n"
"RC v159,1\n"
"MB v[224:239],a[252:255],v[120:123]\n"
"GL v156\n"
"GH v157\n"
"PM v150,v150,v151\n"
"PM v158,v158,v159\n"
"PF v51,v150,v150,v51\n"
"PF v55,v158,v158,v55\n"
"AW a11,v150\n"
"AW a15,v158\n"
"WL 4\n"
"v_or_b32 v148, v148, v149\n"
"v_or_b32 v146, v146, v147\n"
"v_or_b32 v144, v144, v145\n"
"PF v50,v148,v148,v50\n"
"GA v224\n"
"GA v225\n"
"CH v230,v230,v231\n"
"CH v238,v238,v239\n"
"MZ v[240:255],a[232:235],v[36:39]\n"
"GL v224\n"
"GH v225\n"
"LD a[232:235],v1,6144\n"
"PF v231,v230,v230,s56\n"
"PF v239,v238,v238,s56\n"
"PF v49,v146,v146,v49\n"
"PF v48,v144,v144,v48\n"
"GA v226\n"
"GA v227\n"
"AW a10,v148\n"
"AW a9,v146\n"
"AW a8,v144\n"
"PM v231,v231,v230\n"
"PM v239,v239,v238\n"
"MB v[240:255],a[236:239],v[44:47]\n"
"GL v226\n"
"GH v227\n"
"LD a[236:239],v1,7168\n"
"EX v231,0\n"
"GA v228\n"
"GA v229\n"
"EX v239,0\n"
"MB v[240:255],a[240:243],v[84:87]\n"
"GL v228\n"
"GH v229\n"
"LD a[240:243],v1,8192\n"
"EX v231,1\n"
"EX v239,1\n"
"GA v232\n"
"GA v233\n"
"PF v231,v231,s57,s57\n"
"MB v[240:255],a[244:247],v[92:95]\n"
"GL v232\n"
"GH v233\n"
"LD a[244:247],v1,9216\n"
"PF v239,v239,s57,s57\n"
"WL 9\n"
"v_or_b32 v156, v156, v157\n"
"v_or_b32 v154, v154, v155\n"
"v_or_b32 v152, v152, v153\n"
"RC v231,0\n"
"PF v54,v156,v156,v54\n"
"PF v53,v154,v154,v53\n"
"PF v52,v152,v152,v52\n"
"GA v234\n"
"GA v235\n"
"AW a14,v156\n"
"AW a13,v154\n"
"AW a12,v152\n"
"RC v239,0\n"
"MB v[240:255],a[248:251],v[116:119]\n"
"GL v234\n"
"GH v235\n"
"LD a[248:251],v1,10240\n"
"RC v231,1\n"
"GA v236\n"
"GA v237\n"
"RC v239,1\n"
"MB v[240:255],a[252:255],v[124:127]\n"
"GL v236\n"
"GH v237\n"
"LD a[252:255],v1,11264\n"
"PM v230,v230,v231\n"
"PM v238,v238,v239\n"
"PF v51,v230,v230,v51\n"
"PF v55,v238,v238,v55\n"
"v_permlane16_swap_b32_e64 v230, v238\n"
"AW a195,v230\n"
"AW a199,v238\n"
"WL 6\n"
"v_or_b32 v228, v228, v229\n"
"v_or_b32_e64 v226, v226, v227\n"
"GA v240\n"
"GA v241\n"
"CH v246,v246,v247\n"
"CH v254,v254,v255\n"
"MZ v[128:143],a[232:235],v[16:19]\n"
"GL v240\n"
"GH v241\n"
"PF v247,v246,v246,s56\n"
"v_or_b32_e64 v224, v224, v225\n"
"PF v255,v254,v254,s56\n"
"PF v50,v228,v228,v50\n"
"PF v49,v226,v226,v49\n"
"PF v48,v224,v224,v48\n"
"GA v242\n"
"GA v243\n"
"PM v247,v247,v246\n"
"PM v255,v255,v254\n"
"MB v[128:143],a[236:239],v[24:27]\n"
"GL v242\n"
"GH v243\n"
"EX v247,0\n"
"GA v244\n"
"GA v245\n"
"EX v255,0\n"
"MB v[128:143],a[240:243],v[64:67]\n"
"GL v244\n"
"GH v245\n"
"EX v247,1\n"
"EX v255,1\n"
"s_barrier\n"
"s_mov_b64 s[52:53], s[10:11]\n"
"s_mov_b32 s54, 73728\n"
"s_mov_b32 s55, 0x20000\n"
"v_lshrrev_b32_e64 v60, 6, v0\n"
"v_mul_u32_u24 v60, 192, v60\n"
"v_and_or_b32 v60, v0, 48, v60\n"
"s_mul_i32 s62, s58, 18432\n"
"s_mul_i32 s63, s58, 18432\n"
"s_add_u32 s63, s63, 8192\n"
"s_cmp_eq_u32 s58, 3\n"
"s_cselect_b32 s64, 81920, 0\n"
"s_add_u32 s64, s64, s63\n"
"GA v248\n"
"GA v249\n"
"PF v247,v247,s57,s57\n"
"MB v[128:143],a[244:247],v[72:75]\n"
"GL v248\n"
"GH v249\n"
"PF v255,v255,s57,s57\n"
"WL 5\n"
"v_or_b32 v236, v236, v237\n"
"v_or_b32 v234, v234, v235\n"
"v_or_b32 v232, v232, v233\n"
"RC v247,0\n"
"PF v54,v236,v236,v54\n"
"PF v53,v234,v234,v53\n"
"PF v52,v232,v232,v52\n"
"GA v250\n"
"GA v251\n"
"v_permlane16_swap_b32 v228, v236\n"
"v_permlane16_swap_b32 v226, v234\n"
"v_permlane16_swap_b32_e64 v224, v232\n"
"AW a194,v228\n"
"AW a198,v236\n"
"AW a193,v226\n"
"AW a197,v234\n"
"AW a192,v224\n"
"AW a196,v232\n"
"RC v255,0\n"
"MB v[128:143],a[248:251],v[96:99]\n"
"GL v250\n"
"GH v251\n"
"RC v247,1\n"
"GA v252\n"
"GA v253\n"
"RC v255,1\n"
"MB v[128:143],a[252:255],v[104:107]\n"
"GL v252\n"
"GH v253\n"
"s_add_u32 m0, s63, 0\n"
"s_add_u32 s65, s62, 0\n"
"PM v246,v246,v247\n"
"PM v254,v254,v255\n"
"PF v51,v246,v246,v51\n"
"PF v55,v254,v254,v55\n"
"v_permlane16_swap_b32_e64 v246, v254\n"
"AW a203,v246\n"
"AW a207,v254\n"
"WL 4\n"
"v_or_b32 v244, v244, v245\n"
"GA v128\n"
"GA v129\n"
"CH v134,v134,v135\n"
"CH v142,v142,v143\n"
"MZ v[144:159],a[232:235],v[20:23]\n"
"buffer_load_dwordx4 v60, s[52:55], s65 offen offset:0 lds\n"
"GL v128\n"
"GH v129\n"
"PF v135,v134,v134,s56\n"
"v_or_b32 v242, v242, v243\n"
"v_or_b32 v240, v240, v241\n"
"PF v143,v142,v142,s56\n"
"PF v50,v244,v244,v50\n"
"PF v49,v242,v242,v49\n"
"PF v48,v240,v240,v48\n"
"GA v130\n"
"GA v131\n"
"PM v135,v135,v134\n"
"v_mfma_f32_16x16x32_f16 v[48:51], v[48:51], v[56:59], 0\n"
"GL v130\n"
"GH v131\n"
"PM v143,v143,v142\n"
"MB v[144:159],a[236:239],v[28:31]\n"
"EX v135,0\n"
"GA v132\n"
"GA v133\n"
"EX v143,0\n"
"EX v135,1\n"
"EX v143,1\n"
"v_pk_add_f32 v[50:51], v[48:49], v[50:51]\n"
"MB v[144:159],a[240:243],v[68:71]\n"
"GL v132\n"
"GH v133\n"
"v_add_f32_e64 v51, v50, v51\n"
"GA v136\n"
"GA v137\n"
"s_add_u32 m0, s63, 4294965248\n"
"PF v135,v135,s57,s57\n"
"MB v[144:159],a[244:247],v[76:79]\n"
"GL v136\n"
"GH v137\n"
"PF v143,v143,s57,s57\n"
"WL 6\n"
"v_or_b32 v252, v252, v253\n"
"v_or_b32 v250, v250, v251\n"
"v_or_b32 v248, v248, v249\n"
"RC v135,0\n"
"PF v54,v252,v252,v54\n"
"PF v53,v250,v250,v53\n"
"PF v52,v248,v248,v52\n"
"GA v138\n"
"GA v139\n"
"v_permlane16_swap_b32 v244, v252\n"
"v_permlane16_swap_b32 v242, v250\n"
"v_permlane16_swap_b32_e64 v240, v248\n"
"AW a202,v244\n"
"AW a206,v252\n"
"AW a201,v242\n"
"AW a205,v250\n"
"AW a200,v240\n"
"AW a204,v248\n"
"v_mfma_f32_16x16x32_f16 v[52:55], v[52:55], v[56:59], 0\n"
"buffer_load_dwordx4 v60, s[52:55], s65 offen offset:3072 lds\n"
"GL v138\n"
"GH v139\n"
"RC v143,0\n"
"MB v[144:159],a[248:251],v[100:103]\n"
"RC v135,1\n"
"GA v140\n"
"GA v141\n"
"RC v143,1\n"
"PM v134,v134,v135\n"
"PM v142,v142,v143\n"
"PM v211,v134,v134\n"
"v_pk_add_f32 v[54:55], v[52:53], v[54:55]\n"
"v_add_f32_e64 v52, v54, v55\n"
"MB v[144:159],a[252:255],v[108:111]\n"
"GL v140\n"
"GH v141\n"
"v_permlane16_swap_b32 v51, v52\n"
"v_add_f32 v62, v51, v52\n"
"PM v215,v142,v142\n"
"AW a19,v134\n"
"AW a23,v142\n"
"WL 4\n"
"v_or_b32 v132, v132, v133\n"
"v_or_b32 v130, v130, v131\n"
"v_or_b32 v128, v128, v129\n"
"PM v210,v132,v132\n"
"PM v209,v130,v130\n"
"GA v144\n"
"GA v145\n"
"CH v150,v150,v151\n"
"CH v158,v158,v159\n"
"MZ v[224:239],a[232:235],v[32:35]\n"
"GL v144\n"
"GH v145\n"
"PF v151,v150,v150,s56\n"
"PF v159,v158,v158,s56\n"
"PM v208,v128,v128\n"
"GA v146\n"
"GA v147\n"
"s_add_u32 m0, s64, 1984\n"
"AW a18,v132\n"
"AW a17,v130\n"
"AW a16,v128\n"
"PM v151,v151,v150\n"
"PM v159,v159,v158\n"
"MB v[224:239],a[236:239],v[40:43]\n"
"GL v146\n"
"GH v147\n"
"buffer_load_dwordx4 v60, s[52:55], s65 offen offset:64 lds\n"
"EX v151,0\n"
"GA v148\n"
"GA v149\n"
"EX v159,0\n"
"MB v[224:239],a[240:243],v[80:83]\n"
"GL v148\n"
"GH v149\n"
"EX v151,1\n"
"EX v159,1\n"
"GA v152\n"
"GA v153\n"
"PF v151,v151,s57,s57\n"
"MB v[224:239],a[244:247],v[88:91]\n"
"GL v152\n"
"GH v153\n"
"PF v159,v159,s57,s57\n"
"WL 6\n"
"v_or_b32 v140, v140, v141\n"
"v_or_b32 v138, v138, v139\n"
"v_or_b32 v136, v136, v137\n"
"RC v151,0\n"
"PM v214,v140,v140\n"
"PM v213,v138,v138\n"
"PM v212,v136,v136\n"
"GA v154\n"
"GA v155\n"
"AW a22,v140\n"
"AW a21,v138\n"
"AW a20,v136\n"
"RC v159,0\n"
"MB v[224:239],a[248:251],v[112:115]\n"
"GL v154\n"
"GH v155\n"
"RC v151,1\n"
"s_add_u32 m0, s64, 4294967232\n"
"GA v156\n"
"GA v157\n"
"RC v159,1\n"
"MB v[224:239],a[252:255],v[120:123]\n"
"GL v156\n"
"GH v157\n"
"buffer_load_dwordx4 v60, s[52:55], s65 offen offset:3136 lds\n"
"PM v150,v150,v151\n"
"PM v158,v158,v159\n"
"PF v211,v150,v150,v211\n"
"PF v215,v158,v158,v215\n"
"AW a27,v150\n"
"AW a31,v158\n"
"WL 4\n"
"v_or_b32 v148, v148, v149\n"
"v_or_b32 v146, v146, v147\n"
"v_or_b32 v144, v144, v145\n"
"GA v224\n"
"GA v225\n"
"CH v230,v230,v231\n"
"CH v238,v238,v239\n"
"MZ v[240:255],a[232:235],v[36:39]\n"
"GL v224\n"
"GH v225\n"
"LD a[232:235],v1,12288\n"
"PF v231,v230,v230,s56\n"
"PF v239,v238,v238,s56\n"
"PF v210,v148,v148,v210\n"
"PF v209,v146,v146,v209\n"
"PF v208,v144,v144,v208\n"
"GA v226\n"
"GA v227\n"
"AW a26,v148\n"
"AW a25,v146\n"
"AW a24,v144\n"
"PM v231,v231,v230\n"
"PM v239,v239,v238\n"
"MB v[240:255],a[236:239],v[44:47]\n"
"GL v226\n"
"GH v227\n"
"LD a[236:239],v1,13312\n"
"EX v231,0\n"
"GA v228\n"
"GA v229\n"
"EX v239,0\n"
"MB v[240:255],a[240:243],v[84:87]\n"
"GL v228\n"
"GH v229\n"
"LD a[240:243],v1,14336\n"
"EX v231,1\n"
"s_add_u32 m0, s64, 3968\n"
"EX v239,1\n"
"GA v232\n"
"GA v233\n"
"PF v231,v231,s57,s57\n"
"MB v[240:255],a[244:247],v[92:95]\n"
"buffer_load_dwordx4 v60, s[52:55], s65 offen offset:128 lds\n"
"GL v232\n"
"GH v233\n"
"LD a[244:247],v1,15360\n"
"PF v239,v239,s57,s57\n"
"WL 9\n"
"v_or_b32 v156, v156, v157\n"
"v_or_b32 v154, v154, v155\n"
"v_or_b32 v152, v152, v153\n"
"RC v231,0\n"
"PF v214,v156,v156,v214\n"
"PF v213,v154,v154,v213\n"
"PF v212,v152,v152,v212\n"
"GA v234\n"
"GA v235\n"
"AW a30,v156\n"
"AW a29,v154\n"
"AW a28,v152\n"
"RC v239,0\n"
"MB v[240:255],a[248:251],v[116:119]\n"
"GL v234\n"
"GH v235\n"
"LD a[248:251],v1,16384\n"
"RC v231,1\n"
"GA v236\n"
"GA v237\n"
"RC v239,1\n"
"MB v[240:255],a[252:255],v[124:127]\n"
"GL v236\n"
"GH v237\n"
"LD a[252:255],v1,17408\n"
"PM v230,v230,v231\n"
"PM v238,v238,v239\n"
"PF v211,v230,v230,v211\n"
"PF v215,v238,v238,v215\n"
"v_permlane16_swap_b32_e64 v230, v238\n"
"AW a211,v230\n"
"AW a215,v238\n"
"WL 6\n"
"v_or_b32 v228, v228, v229\n"
"v_or_b32_e64 v226, v226, v227\n"
"GA v240\n"
"GA v241\n"
"CH v246,v246,v247\n"
"s_add_u32 m0, s64, 1920\n"
"CH v254,v254,v255\n"
"MZ v[128:143],a[232:235],v[16:19]\n"
"GL v240\n"
"GH v241\n"
"buffer_load_dwordx4 v60, s[52:55], s65 offen offset:3200 lds\n"
"PF v247,v246,v246,s56\n"
"v_or_b32_e64 v224, v224, v225\n"
"PF v255,v254,v254,s56\n"
"PF v210,v228,v228,v210\n"
"PF v209,v226,v226,v209\n"
"PF v208,v224,v224,v208\n"
"GA v242\n"
"GA v243\n"
"PM v247,v247,v246\n"
"PM v255,v255,v254\n"
"MB v[128:143],a[236:239],v[24:27]\n"
"GL v242\n"
"GH v243\n"
"EX v247,0\n"
"GA v244\n"
"GA v245\n"
"EX v255,0\n"
"MB v[128:143],a[240:243],v[64:67]\n"
"GL v244\n"
"GH v245\n"
"EX v247,1\n"
"EX v255,1\n"
"GA v248\n"
"GA v249\n"
"PF v247,v247,s57,s57\n"
"MB v[128:143],a[244:247],v[72:75]\n"
"GL v248\n"
"GH v249\n"
"PF v255,v255,s57,s57\n"
"WL 6\n"
"v_or_b32 v236, v236, v237\n"
"v_or_b32 v234, v234, v235\n"
"v_or_b32 v232, v232, v233\n"
"RC v247,0\n"
"PF v214,v236,v236,v214\n"
"PF v213,v234,v234,v213\n"
"PF v212,v232,v232,v212\n"
"GA v250\n"
"GA v251\n"
"v_permlane16_swap_b32_e64 v228, v236\n"
"s_add_u32 m0, s64, 6144\n"
"s_add_u32 s65, s62, 6144\n"
"v_permlane16_swap_b32 v226, v234\n"
"v_permlane16_swap_b32 v224, v232\n"
"AW a210,v228\n"
"AW a214,v236\n"
"AW a209,v226\n"
"AW a213,v234\n"
"AW a208,v224\n"
"AW a212,v232\n"
"RC v255,0\n"
"MB v[128:143],a[248:251],v[96:99]\n"
"GL v250\n"
"GH v251\n"
"buffer_load_dwordx4 v60, s[52:55], s65 offen offset:0 lds\n"
"RC v247,1\n"
"GA v252\n"
"GA v253\n"
"RC v255,1\n"
"MB v[128:143],a[252:255],v[104:107]\n"
"GL v252\n"
"GH v253\n"
"PM v246,v246,v247\n"
"PM v254,v254,v255\n"
"PF v211,v246,v246,v211\n"
"PF v215,v254,v254,v215\n"
"v_permlane16_swap_b32_e64 v246, v254\n"
"AW a219,v246\n"
"AW a223,v254\n"
"WL 4\n"
"v_or_b32 v244, v244, v245\n"
"v_or_b32 v242, v242, v243\n"
"v_or_b32 v240, v240, v241\n"
"GA v128\n"
"GA v129\n"
"CH v134,v134,v135\n"
"CH v142,v142,v143\n"
"MZ v[144:159],a[232:235],v[20:23]\n"
"GL v128\n"
"GH v129\n"
"PF v135,v134,v134,s56\n"
"PF v143,v142,v142,s56\n"
"PF v210,v244,v244,v210\n"
"PF v209,v242,v242,v209\n"
"PF v208,v240,v240,v208\n"
"GA v130\n"
"GA v131\n"
"PM v135,v135,v134\n"
"v_mfma_f32_16x16x32_f16 v[208:211], v[208:211], v[56:59], 0\n"
"GL v130\n"
"GH v131\n"
"PM v143,v143,v142\n"
"MB v[144:159],a[236:239],v[28:31]\n"
"EX v135,0\n"
"s_add_u32 m0, s64, 4096\n"
"GA v132\n"
"GA v133\n"
"EX v143,0\n"
"EX v135,1\n"
"v_pk_add_f32 v[210:211], v[208:209], v[210:211]\n"
"MB v[144:159],a[240:243],v[68:71]\n"
"buffer_load_dwordx4 v60, s[52:55], s65 offen offset:3072 lds\n"
"GL v132\n"
"GH v133\n"
"v_add_f32_e64 v211, v210, v211\n"
"EX v143,1\n"
"GA v136\n"
"GA v137\n"
"PF v135,v135,s57,s57\n"
"MB v[144:159],a[244:247],v[76:79]\n"
"GL v136\n"
"GH v137\n"
"PF v143,v143,s57,s57\n"
"WL 6\n"
"v_or_b32 v252, v252, v253\n"
"v_or_b32 v250, v250, v251\n"
"v_or_b32 v248, v248, v249\n"
"RC v135,0\n"
"PF v214,v252,v252,v214\n"
"PF v213,v250,v250,v213\n"
"PF v212,v248,v248,v212\n"
"GA v138\n"
"GA v139\n"
"v_permlane16_swap_b32 v244, v252\n"
"v_permlane16_swap_b32 v242, v250\n"
"v_permlane16_swap_b32_e64 v240, v248\n"
"AW a218,v244\n"
"AW a222,v252\n"
"AW a217,v242\n"
"AW a221,v250\n"
"AW a216,v240\n"
"AW a220,v248\n"
"v_mfma_f32_16x16x32_f16 v[212:215], v[212:215], v[56:59], 0\n"
"GL v138\n"
"GH v139\n"
"RC v143,0\n"
"MB v[144:159],a[248:251],v[100:103]\n"
"RC v135,1\n"
"GA v140\n"
"GA v141\n"
"RC v143,1\n"
"PM v134,v134,v135\n"
"PM v142,v142,v143\n"
"PM v51,v134,v134\n"
"v_pk_add_f32 v[214:215], v[212:213], v[214:215]\n"
"v_add_f32_e64 v212, v214, v215\n"
"MB v[144:159],a[252:255],v[108:111]\n"
"GL v140\n"
"GH v141\n"
"v_permlane16_swap_b32_e64 v211, v212\n"
"s_add_u32 m0, s64, 8128\n"
"v_add_f32_e64 v212, v211, v212\n"
"PM v55,v142,v142\n"
"AW a35,v134\n"
"v_permlane32_swap_b32 v62, v212\n"
"v_add_f32 v212, v62, v212\n"
"v_add_f32_e64 v3, v3, v212\n"
"AW a39,v142\n"
"WL 4\n"
"v_or_b32 v132, v132, v133\n"
"GA v144\n"
"GA v145\n"
"CH v150,v150,v151\n"
"CH v158,v158,v159\n"
"MZ v[224:239],a[232:235],v[32:35]\n"
"buffer_load_dwordx4 v60, s[52:55], s65 offen offset:64 lds\n"
"GL v144\n"
"GH v145\n"
"PF v151,v150,v150,s56\n"
"v_or_b32 v130, v130, v131\n"
"v_or_b32 v128, v128, v129\n"
"PF v159,v158,v158,s56\n"
"PM v50,v132,v132\n"
"PM v49,v130,v130\n"
"PM v48,v128,v128\n"
"GA v146\n"
"GA v147\n"
"AW a34,v132\n"
"AW a33,v130\n"
"AW a32,v128\n"
"PM v151,v151,v150\n"
"PM v159,v159,v158\n"
"MB v[224:239],a[236:239],v[40:43]\n"
"GL v146\n"
"GH v147\n"
"EX v151,0\n"
"GA v148\n"
"GA v149\n"
"EX v159,0\n"
"MB v[224:239],a[240:243],v[80:83]\n"
"GL v148\n"
"GH v149\n"
"EX v151,1\n"
"EX v159,1\n"
"GA v152\n"
"GA v153\n"
"s_add_u32 m0, s64, 6080\n"
"PF v151,v151,s57,s57\n"
"MB v[224:239],a[244:247],v[88:91]\n"
"GL v152\n"
"GH v153\n"
"buffer_load_dwordx4 v60, s[52:55], s65 offen offset:3136 lds\n"
"PF v159,v159,s57,s57\n"
"WL 6\n"
"v_or_b32 v140, v140, v141\n"
"v_or_b32 v138, v138, v139\n"
"v_or_b32 v136, v136, v137\n"
"RC v151,0\n"
"PM v54,v140,v140\n"
"PM v53,v138,v138\n"
"PM v52,v136,v136\n"
"GA v154\n"
"GA v155\n"
"AW a38,v140\n"
"AW a37,v138\n"
"AW a36,v136\n"
"RC v159,0\n"
"MB v[224:239],a[248:251],v[112:115]\n"
"GL v154\n"
"GH v155\n"
"RC v151,1\n"
"GA v156\n"
"GA v157\n"
"RC v159,1\n"
"MB v[224:239],a[252:255],v[120:123]\n"
"GL v156\n"
"GH v157\n"
"PM v150,v150,v151\n"
"PM v158,v158,v159\n"
"PF v51,v150,v150,v51\n"
"PF v55,v158,v158,v55\n"
"AW a43,v150\n"
"AW a47,v158\n"
"WL 4\n"
"v_or_b32 v148, v148, v149\n"
"v_or_b32 v146, v146, v147\n"
"v_or_b32 v144, v144, v145\n"
"PF v50,v148,v148,v50\n"
"GA v224\n"
"GA v225\n"
"CH v230,v230,v231\n"
"CH v238,v238,v239\n"
"MZ v[240:255],a[232:235],v[36:39]\n"
"GL v224\n"
"GH v225\n"
"LD a[232:235],v1,18432\n"
"PF v231,v230,v230,s56\n"
"s_add_u32 m0, s64, 10112\n"
"PF v239,v238,v238,s56\n"
"PF v49,v146,v146,v49\n"
"PF v48,v144,v144,v48\n"
"GA v226\n"
"GA v227\n"
"AW a42,v148\n"
"AW a41,v146\n"
"AW a40,v144\n"
"PM v231,v231,v230\n"
"PM v239,v239,v238\n"
"MB v[240:255],a[236:239],v[44:47]\n"
"GL v226\n"
"GH v227\n"
"buffer_load_dwordx4 v60, s[52:55], s65 offen offset:128 lds\n"
"LD a[236:239],v1,19456\n"
"EX v231,0\n"
"GA v228\n"
"GA v229\n"
"EX v239,0\n"
"MB v[240:255],a[240:243],v[84:87]\n"
"GL v228\n"
"GH v229\n"
"LD a[240:243],v1,20480\n"
"EX v231,1\n"
"EX v239,1\n"
"GA v232\n"
"GA v233\n"
"PF v231,v231,s57,s57\n"
"MB v[240:255],a[244:247],v[92:95]\n"
"GL v232\n"
"GH v233\n"
"LD a[244:247],v1,21504\n"
"PF v239,v239,s57,s57\n"
"WL 9\n"
"v_or_b32 v156, v156, v157\n"
"v_or_b32 v154, v154, v155\n"
"v_or_b32 v152, v152, v153\n"
"RC v231,0\n"
"PF v54,v156,v156,v54\n"
"PF v53,v154,v154,v53\n"
"PF v52,v152,v152,v52\n"
"GA v234\n"
"GA v235\n"
"AW a46,v156\n"
"AW a45,v154\n"
"AW a44,v152\n"
"RC v239,0\n"
"MB v[240:255],a[248:251],v[116:119]\n"
"GL v234\n"
"GH v235\n"
"LD a[248:251],v1,22528\n"
"s_add_u32 m0, s64, 8064\n"
"RC v231,1\n"
"GA v236\n"
"GA v237\n"
"RC v239,1\n"
"MB v[240:255],a[252:255],v[124:127]\n"
"buffer_load_dwordx4 v60, s[52:55], s65 offen offset:3200 lds\n"
"GL v236\n"
"GH v237\n"
"LD a[252:255],v1,23552\n"
"PM v230,v230,v231\n"
"PM v238,v238,v239\n"
"PF v51,v230,v230,v51\n"
"PF v55,v238,v238,v55\n"
"v_permlane16_swap_b32_e64 v230, v238\n"
"AW a227,v230\n"
"AW a231,v238\n"
"WL 6\n"
"v_or_b32 v228, v228, v229\n"
"GA v240\n"
"GA v241\n"
"CH v246,v246,v247\n"
"CH v254,v254,v255\n"
"MZ v[128:143],a[232:235],v[16:19]\n"
"GL v240\n"
"GH v241\n"
"PF v247,v246,v246,s56\n"
"v_or_b32 v226, v226, v227\n"
"v_or_b32 v224, v224, v225\n"
"PF v255,v254,v254,s56\n"
"PF v50,v228,v228,v50\n"
"PF v49,v226,v226,v49\n"
"PF v48,v224,v224,v48\n"
"GA v242\n"
"GA v243\n"
"PM v247,v247,v246\n"
"PM v255,v255,v254\n"
"MB v[128:143],a[236:239],v[24:27]\n"
"GL v242\n"
"GH v243\n"
"EX v247,0\n"
"GA v244\n"
"GA v245\n"
"EX v255,0\n"
"s_add_u32 m0, s64, 12288\n"
"s_add_u32 s65, s62, 12288\n"
"MB v[128:143],a[240:243],v[64:67]\n"
"GL v244\n"
"GH v245\n"
"EX v247,1\n"
"EX v255,1\n"
"GA v248\n"
"GA v249\n"
"PF v247,v247,s57,s57\n"
"MB v[128:143],a[244:247],v[72:75]\n"
"buffer_load_dwordx4 v60, s[52:55], s65 offen offset:0 lds\n"
"GL v248\n"
"GH v249\n"
"PF v255,v255,s57,s57\n"
"WL 6\n"
"v_or_b32 v236, v236, v237\n"
"v_or_b32 v234, v234, v235\n"
"v_or_b32 v232, v232, v233\n"
"RC v247,0\n"
"PF v54,v236,v236,v54\n"
"PF v53,v234,v234,v53\n"
"PF v52,v232,v232,v52\n"
"GA v250\n"
"GA v251\n"
"v_permlane16_swap_b32 v228, v236\n"
"v_permlane16_swap_b32 v226, v234\n"
"v_permlane16_swap_b32_e64 v224, v232\n"
"AW a226,v228\n"
"AW a230,v236\n"
"AW a225,v226\n"
"AW a229,v234\n"
"AW a224,v224\n"
"AW a228,v232\n"
"RC v255,0\n"
"MB v[128:143],a[248:251],v[96:99]\n"
"GL v250\n"
"GH v251\n"
"RC v247,1\n"
"GA v252\n"
"GA v253\n"
"RC v255,1\n"
"MB v[128:143],a[252:255],v[104:107]\n"
"GL v252\n"
"GH v253\n"
"PM v11,v246,v247\n"
"PM v15,v254,v255\n"
"PF v51,v11,v11,v51\n"
"s_add_u32 m0, s64, 10240\n"
"PF v55,v15,v15,v55\n"
"WL 4\n"
"v_or_b32 v10, v244, v245\n"
"v_or_b32 v9, v242, v243\n"
"v_or_b32 v8, v240, v241\n"
"PF v50,v10,v10,v50\n"
"PF v49,v9,v9,v49\n"
"GA v128\n"
"GA v129\n"
"CH v134,v134,v135\n"
"CH v142,v142,v143\n"
"MZ v[144:159],a[232:235],v[20:23]\n"
"GL v128\n"
"GH v129\n"
"buffer_load_dwordx4 v60, s[52:55], s65 offen offset:3072 lds\n"
"PF v135,v134,v134,s56\n"
"PF v143,v142,v142,s56\n"
"PF v48,v8,v8,v48\n"
"GA v130\n"
"GA v131\n"
"PM v135,v135,v134\n"
"v_mfma_f32_16x16x32_f16 v[48:51], v[48:51], v[56:59], 0\n"
"GL v130\n"
"GH v131\n"
"PM v143,v143,v142\n"
"MB v[144:159],a[236:239],v[28:31]\n"
"EX v135,0\n"
"GA v132\n"
"GA v133\n"
"EX v143,0\n"
"EX v135,1\n"
"EX v143,1\n"
"v_pk_add_f32 v[50:51], v[48:49], v[50:51]\n"
"MB v[144:159],a[240:243],v[68:71]\n"
"GL v132\n"
"GH v133\n"
"v_add_f32_e64 v51, v50, v51\n"
"GA v136\n"
"GA v137\n"
"PF v135,v135,s57,s57\n"
"MB v[144:159],a[244:247],v[76:79]\n"
"GL v136\n"
"GH v137\n"
"PF v143,v143,s57,s57\n"
"WL 6\n"
"v_or_b32 v14, v252, v253\n"
"v_or_b32 v13, v250, v251\n"
"v_or_b32 v12, v248, v249\n"
"s_add_u32 m0, s64, 14272\n"
"RC v135,0\n"
"PF v54,v14,v14,v54\n"
"PF v53,v13,v13,v53\n"
"PF v52,v12,v12,v52\n"
"GA v138\n"
"GA v139\n"
"v_mfma_f32_16x16x32_f16 v[52:55], v[52:55], v[56:59], 0\n"
"GL v138\n"
"GH v139\n"
"buffer_load_dwordx4 v60, s[52:55], s65 offen offset:64 lds\n"
"RC v143,0\n"
"MB v[144:159],a[248:251],v[100:103]\n"
"RC v135,1\n"
"GA v140\n"
"GA v141\n"
"RC v143,1\n"
"PM v134,v134,v135\n"
"PM v142,v142,v143\n"
"PM v211,v134,v134\n"
"v_pk_add_f32 v[54:55], v[52:53], v[54:55]\n"
"v_add_f32_e64 v52, v54, v55\n"
"MB v[144:159],a[252:255],v[108:111]\n"
"GL v140\n"
"GH v141\n"
"v_permlane16_swap_b32 v51, v52\n"
"v_add_f32 v62, v51, v52\n"
"PM v215,v142,v142\n"
"AW a51,v134\n"
"AW a55,v142\n"
"WL 4\n"
"v_or_b32 v132, v132, v133\n"
"v_or_b32 v130, v130, v131\n"
"v_or_b32 v128, v128, v129\n"
"PM v210,v132,v132\n"
"PM v209,v130,v130\n"
"GA v144\n"
"GA v145\n"
"CH v150,v150,v151\n"
"CH v158,v158,v159\n"
"MZ v[224:239],a[232:235],v[32:35]\n"
"GL v144\n"
"GH v145\n"
"PF v151,v150,v150,s56\n"
"PF v159,v158,v158,s56\n"
"PM v208,v128,v128\n"
"GA v146\n"
"GA v147\n"
"AW a50,v132\n"
"AW a49,v130\n"
"AW a48,v128\n"
"PM v151,v151,v150\n"
"PM v159,v159,v158\n"
"s_add_u32 m0, s64, 12224\n"
"MB v[224:239],a[236:239],v[40:43]\n"
"GL v146\n"
"GH v147\n"
"EX v151,0\n"
"GA v148\n"
"GA v149\n"
"EX v159,0\n"
"MB v[224:239],a[240:243],v[80:83]\n"
"buffer_load_dwordx4 v60, s[52:55], s65 offen offset:3136 lds\n"
"GL v148\n"
"GH v149\n"
"EX v151,1\n"
"EX v159,1\n"
"GA v152\n"
"GA v153\n"
"PF v151,v151,s57,s57\n"
"MB v[224:239],a[244:247],v[88:91]\n"
"GL v152\n"
"GH v153\n"
"PF v159,v159,s57,s57\n"
"WL 6\n"
"v_or_b32 v140, v140, v141\n"
"v_or_b32 v138, v138, v139\n"
"v_or_b32 v136, v136, v137\n"
"RC v151,0\n"
"PM v214,v140,v140\n"
"PM v213,v138,v138\n"
"PM v212,v136,v136\n"
"GA v154\n"
"GA v155\n"
"AW a54,v140\n"
"AW a53,v138\n"
"AW a52,v136\n"
"RC v159,0\n"
"MB v[224:239],a[248:251],v[112:115]\n"
"GL v154\n"
"GH v155\n"
"RC v151,1\n"
"GA v156\n"
"GA v157\n"
"s_add_u32 m0, s64, 16256\n"
"RC v159,1\n"
"MB v[224:239],a[252:255],v[120:123]\n"
"GL v156\n"
"GH v157\n"
"PM v150,v150,v151\n"
"PM v158,v158,v159\n"
"PF v211,v150,v150,v211\n"
"PF v215,v158,v158,v215\n"
"AW a59,v150\n"
"AW a63,v158\n"
"WL 4\n"
"v_or_b32 v148, v148, v149\n"
"v_or_b32 v146, v146, v147\n"
"v_or_b32 v144, v144, v145\n"
"PF v210,v148,v148,v210\n"
"GA v224\n"
"GA v225\n"
"CH v230,v230,v231\n"
"CH v238,v238,v239\n"
"MZ v[240:255],a[232:235],v[36:39]\n"
"buffer_load_dwordx4 v60, s[52:55], s65 offen offset:128 lds\n"
"GL v224\n"
"GH v225\n"
"LD a[232:235],v1,24576\n"
"PF v231,v230,v230,s56\n"
"PF v239,v238,v238,s56\n"
"PF v209,v146,v146,v209\n"
"PF v208,v144,v144,v208\n"
"GA v226\n"
"GA v227\n"
"AW a58,v148\n"
"AW a57,v146\n"
"AW a56,v144\n"
"PM v231,v231,v230\n"
"PM v239,v239,v238\n"
"MB v[240:255],a[236:239],v[44:47]\n"
"GL v226\n"
"GH v227\n"
"LD a[236:239],v1,25600\n"
"EX v231,0\n"
"GA v228\n"
"GA v229\n"
"EX v239,0\n"
"MB v[240:255],a[240:243],v[84:87]\n"
"GL v228\n"
"GH v229\n"
"LD a[240:243],v1,26624\n"
"EX v231,1\n"
"s_add_u32 m0, s64, 14208\n"
"EX v239,1\n"
"GA v232\n"
"GA v233\n"
"PF v231,v231,s57,s57\n"
"MB v[240:255],a[244:247],v[92:95]\n"
"GL v232\n"
"GH v233\n"
"buffer_load_dwordx4 v60, s[52:55], s65 offen offset:3200 lds\n"
"LD a[244:247],v1,27648\n"
"PF v239,v239,s57,s57\n"
"WL 9\n"
"v_or_b32 v156, v156, v157\n"
"v_or_b32 v154, v154, v155\n"
"v_or_b32 v152, v152, v153\n"
"RC v231,0\n"
"PF v214,v156,v156,v214\n"
"PF v213,v154,v154,v213\n"
"PF v212,v152,v152,v212\n"
"GA v234\n"
"GA v235\n"
"AW a62,v156\n"
"AW a61,v154\n"
"AW a60,v152\n"
"RC v239,0\n"
"MB v[240:255],a[248:251],v[116:119]\n"
"GL v234\n"
"GH v235\n"
"LD a[248:251],v1,28672\n"
"RC v231,1\n"
"GA v236\n"
"GA v237\n"
"RC v239,1\n"
"MB v[240:255],a[252:255],v[124:127]\n"
"GL v236\n"
"GH v237\n"
"LD a[252:255],v1,29696\n"
"PM v163,v230,v231\n"
"PM v167,v238,v239\n"
"PF v211,v163,v163,v211\n"
"PF v215,v167,v167,v215\n"
"WL 6\n"
"v_or_b32 v162, v228, v229\n"
"v_or_b32 v161, v226, v227\n"
"v_or_b32 v160, v224, v225\n"
"PF v210,v162,v162,v210\n"
"PF v209,v161,v161,v209\n"
"GA v240\n"
"GA v241\n"
"CH v246,v246,v247\n"
"CH v254,v254,v255\n"
"MZ v[128:143],a[232:235],v[16:19]\n"
"GL v240\n"
"GH v241\n"
"PF v247,v246,v246,s56\n"
"s_add_u32 s22, s61, 0\n"
"s_nop 0\n"
"PF v255,v254,v254,s56\n"
"PF v208,v160,v160,v208\n"
"GA v242\n"
"GA v243\n"
"PM v247,v247,v246\n"
"PM v255,v255,v254\n"
"MB v[128:143],a[236:239],v[24:27]\n"
"GL v242\n"
"GH v243\n"
"EX v247,0\n"
"GA v244\n"
"GA v245\n"
"EX v255,0\n"
"MB v[128:143],a[240:243],v[64:67]\n"
"GL v244\n"
"GH v245\n"
"EX v247,1\n"
"EX v255,1\n"
"GA v248\n"
"GA v249\n"
"PF v247,v247,s57,s57\n"
"MB v[128:143],a[244:247],v[72:75]\n"
"buffer_store_dwordx4 a[96:99], v61, s[40:43], s22 offen offset:0 sc1\n"
"GL v248\n"
"GH v249\n"
"PF v255,v255,s57,s57\n"
"WL 8\n"
"v_or_b32 v166, v236, v237\n"
"v_or_b32 v165, v234, v235\n"
"v_or_b32 v164, v232, v233\n"
"RC v247,0\n"
"PF v214,v166,v166,v214\n"
"PF v213,v165,v165,v213\n"
"PF v212,v164,v164,v212\n"
"GA v250\n"
"GA v251\n"
"RC v255,0\n"
"MB v[128:143],a[248:251],v[96:99]\n"
"buffer_load_dword v60, v0, s[24:27], s28 offen\n"
"GL v250\n"
"GH v251\n"
"RC v247,1\n"
"GA v252\n"
"GA v253\n"
"RC v255,1\n"
"MB v[128:143],a[252:255],v[104:107]\n"
"GL v252\n"
"GH v253\n"
"PM v171,v246,v247\n"
"PM v175,v254,v255\n"
"PF v211,v171,v171,v211\n"
"PF v215,v175,v175,v215\n"
"WL 6\n"
"v_or_b32 v170, v244, v245\n"
"v_or_b32 v169, v242, v243\n"
"v_or_b32 v168, v240, v241\n"
"PF v210,v170,v170,v210\n"
"PF v209,v169,v169,v209\n"
"PF v208,v168,v168,v208\n"
"GA v128\n"
"GA v129\n"
"CH v134,v134,v135\n"
"CH v142,v142,v143\n"
"MZ v[144:159],a[232:235],v[20:23]\n"
"GL v128\n"
"GH v129\n"
"PF v135,v134,v134,s56\n"
"PF v143,v142,v142,s56\n"
"GA v130\n"
"GA v131\n"
"PM v135,v135,v134\n"
"v_mfma_f32_16x16x32_f16 v[208:211], v[208:211], v[56:59], 0\n"
"GL v130\n"
"GH v131\n"
"PM v143,v143,v142\n"
"MB v[144:159],a[236:239],v[28:31]\n"
"EX v135,0\n"
"GA v132\n"
"GA v133\n"
"EX v143,0\n"
"EX v135,1\n"
"EX v143,1\n"
"v_pk_add_f32 v[210:211], v[208:209], v[210:211]\n"
"MB v[144:159],a[240:243],v[68:71]\n"
"buffer_store_dwordx4 a[100:103], v61, s[40:43], s22 offen offset:256 sc1\n"
"GL v132\n"
"GH v133\n"
"v_add_f32_e64 v211, v210, v211\n"
"GA v136\n"
"GA v137\n"
"PF v135,v135,s57,s57\n"
"MB v[144:159],a[244:247],v[76:79]\n"
"GL v136\n"
"GH v137\n"
"PF v143,v143,s57,s57\n"
"WL 7\n"
"v_or_b32 v174, v252, v253\n"
"v_or_b32 v173, v250, v251\n"
"v_or_b32 v172, v248, v249\n"
"RC v135,0\n"
"PF v214,v174,v174,v214\n"
"PF v213,v173,v173,v213\n"
"PF v212,v172,v172,v212\n"
"GA v138\n"
"GA v139\n"
"v_mfma_f32_16x16x32_f16 v[212:215], v[212:215], v[56:59], 0\n"
"GL v138\n"
"GH v139\n"
"RC v143,0\n"
"MB v[144:159],a[248:251],v[100:103]\n"
"s_add_u32 s22, s61, 2048\n"
"RC v135,1\n"
"GA v140\n"
"GA v141\n"
"RC v143,1\n"
"PM v134,v134,v135\n"
"PM v142,v142,v143\n"
"v_pk_add_f32 v[214:215], v[212:213], v[214:215]\n"
"v_add_f32_e64 v212, v214, v215\n"
"MB v[144:159],a[252:255],v[108:111]\n"
"GL v140\n"
"GH v141\n"
"v_permlane16_swap_b32 v211, v212\n"
"v_add_f32 v212, v211, v212\n"
"PM v51,v134,v134\n"
"PM v55,v142,v142\n"
"v_permlane32_swap_b32 v62, v212\n"
"v_add_f32 v212, v62, v212\n"
"v_add_f32_e64 v4, v4, v212\n"
"AW a67,v134\n"
"AW a71,v142\n"
"WL 4\n"
"v_or_b32 v132, v132, v133\n"
"GA v144\n"
"GA v145\n"
"CH v150,v150,v151\n"
"CH v158,v158,v159\n"
"MZ v[224:239],a[232:235],v[32:35]\n"
"GL v144\n"
"GH v145\n"
"buffer_store_dwordx4 a[104:107], v61, s[40:43], s22 offen offset:0 sc1\n"
"PF v151,v150,v150,s56\n"
"v_or_b32 v130, v130, v131\n"
"v_or_b32 v128, v128, v129\n"
"PF v159,v158,v158,s56\n"
"PM v50,v132,v132\n"
"PM v49,v130,v130\n"
"PM v48,v128,v128\n"
"GA v146\n"
"GA v147\n"
"AW a66,v132\n"
"AW a65,v130\n"
"AW a64,v128\n"
"PM v151,v151,v150\n"
"PM v159,v159,v158\n"
"MB v[224:239],a[236:239],v[40:43]\n"
"GL v146\n"
"GH v147\n"
"EX v151,0\n"
"GA v148\n"
"GA v149\n"
"EX v159,0\n"
"MB v[224:239],a[240:243],v[80:83]\n"
"GL v148\n"
"GH v149\n"
"EX v151,1\n"
"EX v159,1\n"
"GA v152\n"
"GA v153\n"
"PF v151,v151,s57,s57\n"
"MB v[224:239],a[244:247],v[88:91]\n"
"GL v152\n"
"GH v153\n"
"PF v159,v159,s57,s57\n"
"WL 6\n"
"v_or_b32 v140, v140, v141\n"
"v_or_b32 v138, v138, v139\n"
"v_or_b32 v136, v136, v137\n"
"RC v151,0\n"
"PM v54,v140,v140\n"
"PM v53,v138,v138\n"
"PM v52,v136,v136\n"
"GA v154\n"
"GA v155\n"
"AW a70,v140\n"
"AW a69,v138\n"
"AW a68,v136\n"
"RC v159,0\n"
"MB v[224:239],a[248:251],v[112:115]\n"
"GL v154\n"
"GH v155\n"
"RC v151,1\n"
"GA v156\n"
"GA v157\n"
"RC v159,1\n"
"MB v[224:239],a[252:255],v[120:123]\n"
"GL v156\n"
"GH v157\n"
"buffer_store_dwordx4 a[108:111], v61, s[40:43], s22 offen offset:256 sc1\n"
"PM v150,v150,v151\n"
"PM v158,v158,v159\n"
"PF v51,v150,v150,v51\n"
"PF v55,v158,v158,v55\n"
"AW a75,v150\n"
"AW a79,v158\n"
"WL 4\n"
"v_or_b32 v148, v148, v149\n"
"v_or_b32 v146, v146, v147\n"
"v_or_b32 v144, v144, v145\n"
"GA v224\n"
"GA v225\n"
"CH v230,v230,v231\n"
"CH v238,v238,v239\n"
"MZ v[240:255],a[232:235],v[36:39]\n"
"GL v224\n"
"GH v225\n"
"LD a[232:235],v1,30720\n"
"PF v231,v230,v230,s56\n"
"PF v239,v238,v238,s56\n"
"PF v50,v148,v148,v50\n"
"PF v49,v146,v146,v49\n"
"PF v48,v144,v144,v48\n"
"GA v226\n"
"GA v227\n"
"AW a74,v148\n"
"AW a73,v146\n"
"AW a72,v144\n"
"PM v231,v231,v230\n"
"PM v239,v239,v238\n"
"MB v[240:255],a[236:239],v[44:47]\n"
"GL v226\n"
"GH v227\n"
"LD a[236:239],v1,31744\n"
"EX v231,0\n"
"GA v228\n"
"GA v229\n"
"s_add_u32 s22, s61, 4096\n"
"EX v239,0\n"
"MB v[240:255],a[240:243],v[84:87]\n"
"GL v228\n"
"GH v229\n"
"LD a[240:243],v1,32768\n"
"EX v231,1\n"
"EX v239,1\n"
"GA v232\n"
"GA v233\n"
"PF v231,v231,s57,s57\n"
"MB v[240:255],a[244:247],v[92:95]\n"
"GL v232\n"
"GH v233\n"
"LD a[244:247],v1,33792\n"
"PF v239,v239,s57,s57\n"
"WL 9\n"
"v_or_b32 v156, v156, v157\n"
"v_or_b32 v154, v154, v155\n"
"v_or_b32 v152, v152, v153\n"
"RC v231,0\n"
"PF v54,v156,v156,v54\n"
"PF v53,v154,v154,v53\n"
"PF v52,v152,v152,v52\n"
"GA v234\n"
"GA v235\n"
"AW a78,v156\n"
"AW a77,v154\n"
"AW a76,v152\n"
"RC v239,0\n"
"MB v[240:255],a[248:251],v[116:119]\n"
"GL v234\n"
"GH v235\n"
"buffer_store_dwordx4 a[112:115], v61, s[40:43], s22 offen offset:0 sc1\n"
"LD a[248:251],v1,34816\n"
"RC v231,1\n"
"GA v236\n"
"GA v237\n"
"RC v239,1\n"
"MB v[240:255],a[252:255],v[124:127]\n"
"GL v236\n"
"GH v237\n"
"LD a[252:255],v1,35840\n"
"PM v179,v230,v231\n"
"PM v183,v238,v239\n"
"PF v51,v179,v179,v51\n"
"PF v55,v183,v183,v55\n"
"WL 6\n"
"v_or_b32 v178, v228, v229\n"
"v_or_b32 v177, v226, v227\n"
"v_or_b32 v176, v224, v225\n"
"PF v50,v178,v178,v50\n"
"PF v49,v177,v177,v49\n"
"GA v240\n"
"GA v241\n"
"CH v246,v246,v247\n"
"CH v254,v254,v255\n"
"MZ v[128:143],a[232:235],v[16:19]\n"
"GL v240\n"
"GH v241\n"
"PF v247,v246,v246,s56\n"
"PF v255,v254,v254,s56\n"
"PF v48,v176,v176,v48\n"
"GA v242\n"
"GA v243\n"
"PM v247,v247,v246\n"
"PM v255,v255,v254\n"
"MB v[128:143],a[236:239],v[24:27]\n"
"GL v242\n"
"GH v243\n"
"EX v247,0\n"
"GA v244\n"
"GA v245\n"
"EX v255,0\n"
"MB v[128:143],a[240:243],v[64:67]\n"
"GL v244\n"
"GH v245\n"
"EX v247,1\n"
"EX v255,1\n"
"GA v248\n"
"GA v249\n"
"PF v247,v247,s57,s57\n"
"MB v[128:143],a[244:247],v[72:75]\n"
"buffer_store_dwordx4 a[116:119], v61, s[40:43], s22 offen offset:256 sc1\n"
"GL v248\n"
"GH v249\n"
"PF v255,v255,s57,s57\n"
"WL 8\n"
"v_or_b32 v182, v236, v237\n"
"v_or_b32 v181, v234, v235\n"
"v_or_b32 v180, v232, v233\n"
"RC v247,0\n"
"PF v54,v182,v182,v54\n"
"PF v53,v181,v181,v53\n"
"PF v52,v180,v180,v52\n"
"GA v250\n"
"GA v251\n"
"RC v255,0\n"
"MB v[128:143],a[248:251],v[96:99]\n"
"GL v250\n"
"GH v251\n"
"RC v247,1\n"
"GA v252\n"
"GA v253\n"
"RC v255,1\n"
"MB v[128:143],a[252:255],v[104:107]\n"
"GL v252\n"
"GH v253\n"
"s_add_u32 s22, s61, 98304\n"
"PM v187,v246,v247\n"
"PM v191,v254,v255\n"
"PF v51,v187,v187,v51\n"
"PF v55,v191,v191,v55\n"
"WL 6\n"
"v_or_b32 v186, v244, v245\n"
"v_or_b32 v185, v242, v243\n"
"v_or_b32 v184, v240, v241\n"
"PF v50,v186,v186,v50\n"
"PF v49,v185,v185,v49\n"
"GA v128\n"
"GA v129\n"
"CH v134,v134,v135\n"
"CH v142,v142,v143\n"
"MZ v[144:159],a[232:235],v[20:23]\n"
"GL v128\n"
"GH v129\n"
"PF v135,v134,v134,s56\n"
"PF v143,v142,v142,s56\n"
"PF v48,v184,v184,v48\n"
"GA v130\n"
"GA v131\n"
"PM v135,v135,v134\n"
"v_mfma_f32_16x16x32_f16 v[48:51], v[48:51], v[56:59], 0\n"
"GL v130\n"
"GH v131\n"
"PM v143,v143,v142\n"
"MB v[144:159],a[236:239],v[28:31]\n"
"EX v135,0\n"
"GA v132\n"
"GA v133\n"
"EX v143,0\n"
"EX v135,1\n"
"EX v143,1\n"
"v_pk_add_f32 v[50:51], v[48:49], v[50:51]\n"
"MB v[144:159],a[240:243],v[68:71]\n"
"buffer_store_dwordx4 a[120:123], v61, s[40:43], s22 offen offset:0 sc1\n"
"GL v132\n"
"GH v133\n"
"v_add_f32_e64 v51, v50, v51\n"
"GA v136\n"
"GA v137\n"
"PF v135,v135,s57,s57\n"
"MB v[144:159],a[244:247],v[76:79]\n"
"GL v136\n"
"GH v137\n"
"PF v143,v143,s57,s57\n"
"WL 6\n"
"v_or_b32 v190, v252, v253\n"
"v_or_b32 v189, v250, v251\n"
"v_or_b32 v188, v248, v249\n"
"RC v135,0\n"
"PF v54,v190,v190,v54\n"
"PF v53,v189,v189,v53\n"
"PF v52,v188,v188,v52\n"
"GA v138\n"
"GA v139\n"
"v_mfma_f32_16x16x32_f16 v[52:55], v[52:55], v[56:59], 0\n"
"GL v138\n"
"GH v139\n"
"RC v143,0\n"
"MB v[144:159],a[248:251],v[100:103]\n"
"RC v135,1\n"
"GA v140\n"
"GA v141\n"
"RC v143,1\n"
"PM v134,v134,v135\n"
"PM v142,v142,v143\n"
"PM v211,v134,v134\n"
"v_pk_add_f32 v[54:55], v[52:53], v[54:55]\n"
"v_add_f32_e64 v52, v54, v55\n"
"MB v[144:159],a[252:255],v[108:111]\n"
"GL v140\n"
"GH v141\n"
"v_permlane16_swap_b32 v51, v52\n"
"v_add_f32 v62, v51, v52\n"
"PM v215,v142,v142\n"
"AW a83,v134\n"
"AW a87,v142\n"
"WL 4\n"
"v_or_b32 v132, v132, v133\n"
"v_or_b32 v130, v130, v131\n"
"v_or_b32 v128, v128, v129\n"
"PM v210,v132,v132\n"
"PM v209,v130,v130\n"
"GA v144\n"
"GA v145\n"
"CH v150,v150,v151\n"
"CH v158,v158,v159\n"
"MZ v[224:239],a[232:235],v[32:35]\n"
"GL v144\n"
"GH v145\n"
"buffer_store_dwordx4 a[124:127], v61, s[40:43], s22 offen offset:256 sc1\n"
"PF v151,v150,v150,s56\n"
"PF v159,v158,v158,s56\n"
"PM v208,v128,v128\n"
"GA v146\n"
"GA v147\n"
"AW a82,v132\n"
"AW a81,v130\n"
"AW a80,v128\n"
"PM v151,v151,v150\n"
"PM v159,v159,v158\n"
"MB v[224:239],a[236:239],v[40:43]\n"
"GL v146\n"
"GH v147\n"
"EX v151,0\n"
"GA v148\n"
"GA v149\n"
"EX v159,0\n"
"MB v[224:239],a[240:243],v[80:83]\n"
"GL v148\n"
"GH v149\n"
"EX v151,1\n"
"EX v159,1\n"
"GA v152\n"
"GA v153\n"
"s_add_u32 s22, s61, 100352\n"
"PF v151,v151,s57,s57\n"
"MB v[224:239],a[244:247],v[88:91]\n"
"GL v152\n"
"GH v153\n"
"PF v159,v159,s57,s57\n"
"WL 6\n"
"v_or_b32 v140, v140, v141\n"
"v_or_b32 v138, v138, v139\n"
"v_or_b32 v136, v136, v137\n"
"RC v151,0\n"
"PM v214,v140,v140\n"
"PM v213,v138,v138\n"
"PM v212,v136,v136\n"
"GA v154\n"
"GA v155\n"
"AW a86,v140\n"
"AW a85,v138\n"
"AW a84,v136\n"
"RC v159,0\n"
"MB v[224:239],a[248:251],v[112:115]\n"
"GL v154\n"
"GH v155\n"
"RC v151,1\n"
"GA v156\n"
"GA v157\n"
"RC v159,1\n"
"MB v[224:239],a[252:255],v[120:123]\n"
"GL v156\n"
"GH v157\n"
"buffer_store_dwordx4 a[128:131], v61, s[40:43], s22 offen offset:0 sc1\n"
"PM v150,v150,v151\n"
"PM v158,v158,v159\n"
"PF v211,v150,v150,v211\n"
"PF v215,v158,v158,v215\n"
"AW a91,v150\n"
"AW a95,v158\n"
"WL 4\n"
"v_or_b32 v148, v148, v149\n"
"v_or_b32 v146, v146, v147\n"
"v_or_b32 v144, v144, v145\n"
"GA v224\n"
"GA v225\n"
"CH v230,v230,v231\n"
"CH v238,v238,v239\n"
"GA v226\n"
"MZ v[240:255],a[232:235],v[36:39]\n"
"GL v224\n"
"GH v225\n"
"LD a[232:235],v1,36864\n"
"GA v227\n"
"PF v231,v230,v230,s56\n"
"PF v239,v238,v238,s56\n"
"GA v228\n"
"GA v229\n"
"PF v210,v148,v148,v210\n"
"PF v209,v146,v146,v209\n"
"PF v208,v144,v144,v208\n"
"AW a90,v148\n"
"AW a89,v146\n"
"AW a88,v144\n"
"PM v231,v231,v230\n"
"PM v239,v239,v238\n"
"GA v232\n"
"GA v233\n"
"MB v[240:255],a[236:239],v[44:47]\n"
"GL v226\n"
"GH v227\n"
"GL v228\n"
"GH v229\n"
"GL v232\n"
"GH v233\n"
"LD a[236:239],v1,37888\n"
"EX v231,0\n"
"GA v234\n"
"GA v235\n"
"EX v239,0\n"
"GA v236\n"
"GA v237\n"
"MB v[240:255],a[240:243],v[84:87]\n"
"GL v234\n"
"GH v235\n"
"GL v236\n"
"GH v237\n"
"LD a[240:243],v1,38912\n"
"EX v231,1\n"
"EX v239,1\n"
"PF v231,v231,s57,s57\n"
"MB v[240:255],a[244:247],v[92:95]\n"
"LD a[244:247],v1,39936\n"
"PF v239,v239,s57,s57\n"
"WL 13\n"
"v_or_b32 v156, v156, v157\n"
"v_or_b32 v154, v154, v155\n"
"v_or_b32 v152, v152, v153\n"
"RC v231,0\n"
"PF v214,v156,v156,v214\n"
"PF v213,v154,v154,v213\n"
"PF v212,v152,v152,v212\n"
"AW a94,v156\n"
"AW a93,v154\n"
"AW a92,v152\n"
"WL 7\n"
"v_or_b32 v194, v228, v229\n"
"v_or_b32_e64 v193, v226, v227\n"
"RC v239,0\n"
"v_or_b32_e64 v192, v224, v225\n"
"MB v[240:255],a[248:251],v[116:119]\n"
"buffer_store_dwordx4 a[132:135], v61, s[40:43], s22 offen offset:256 sc1\n"
"LD a[248:251],v1,40960\n"
"PF v210,v194,v194,v210\n"
"PF v209,v193,v193,v209\n"
"PF v208,v192,v192,v208\n"
"RC v231,1\n"
"RC v239,1\n"
"MB v[240:255],a[252:255],v[124:127]\n"
"LD a[252:255],v1,41984\n"
"WL 3\n"
"v_or_b32 v198, v236, v237\n"
"v_or_b32 v197, v234, v235\n"
"v_or_b32 v196, v232, v233\n"
"PF v214,v198,v198,v214\n"
"PF v213,v197,v197,v213\n"
"PF v212,v196,v196,v212\n"
"PM v195,v230,v231\n"
"PM v199,v238,v239\n"
"PF v211,v195,v195,v211\n"
"PF v215,v199,v199,v215\n"
"GA v240\n"
"GA v241\n"
"CH v246,v246,v247\n"
"CH v254,v254,v255\n"
"GA v242\n"
"MZ v[128:143],a[232:235],v[16:19]\n"
"GL v240\n"
"GH v241\n"
"GA v243\n"
"PF v247,v246,v246,s56\n"
"s_add_u32 s22, s61, 102400\n"
"PF v255,v254,v254,s56\n"
"GA v244\n"
"GA v245\n"
"PM v247,v247,v246\n"
"PM v255,v255,v254\n"
"GA v248\n"
"GA v249\n"
"MB v[128:143],a[236:239],v[24:27]\n"
"GL v242\n"
"GH v243\n"
"GL v244\n"
"GH v245\n"
"GL v248\n"
"GH v249\n"
"EX v247,0\n"
"GA v250\n"
"GA v251\n"
"EX v255,0\n"
"GA v252\n"
"GA v253\n"
"MB v[128:143],a[240:243],v[64:67]\n"
"GL v250\n"
"GH v251\n"
"GL v252\n"
"GH v253\n"
"EX v247,1\n"
"EX v255,1\n"
"PF v247,v247,s57,s57\n"
"WL 10\n"
"MB v[128:143],a[244:247],v[72:75]\n"
"buffer_store_dwordx4 a[136:139], v61, s[40:43], s22 offen offset:0 sc1\n"
"PF v255,v255,s57,s57\n"
"RC v247,0\n"
"WL 6\n"
"v_or_b32 v202, v244, v245\n"
"v_or_b32 v201, v242, v243\n"
"RC v255,0\n"
"v_or_b32_e64 v200, v240, v241\n"
"MB v[128:143],a[248:251],v[96:99]\n"
"PF v210,v202,v202,v210\n"
"PF v209,v201,v201,v209\n"
"PF v208,v200,v200,v208\n"
"RC v247,1\n"
"RC v255,1\n"
"MB v[128:143],a[252:255],v[104:107]\n"
"WL 0\n"
"v_or_b32 v206, v252, v253\n"
"v_or_b32_e64 v205, v250, v251\n"
"v_or_b32 v204, v248, v249\n"
"PF v214,v206,v206,v214\n"
"PF v213,v205,v205,v213\n"
"PF v212,v204,v204,v212\n"
"PM v203,v246,v247\n"
"PM v207,v254,v255\n"
"PF v211,v203,v203,v211\n"
"PF v215,v207,v207,v215\n"
"s_nop 0\n"
"GA v128\n"
"GA v129\n"
"v_mfma_f32_16x16x32_f16 v[208:211], v[208:211], v[56:59], 0\n"
"CH v134,v134,v135\n"
"GL v128\n"
"GH v129\n"
"v_mfma_f32_16x16x32_f16 v[212:215], v[212:215], v[56:59], 0\n"
"CH v142,v142,v143\n"
"GA v130\n"
"MZ v[144:159],a[232:235],v[20:23]\n"
"GA v131\n"
"PF v135,v134,v134,s56\n"
"PF v143,v142,v142,s56\n"
"GA v132\n"
"GA v133\n"
"PM v135,v135,v134\n"
"PM v143,v143,v142\n"
"GA v136\n"
"GA v137\n"
"v_pk_add_f32 v[210:211], v[208:209], v[210:211]\n"
"v_pk_add_f32 v[214:215], v[212:213], v[214:215]\n"
"v_add_f32 v211, v210, v211\n"
"v_add_f32 v212, v214, v215\n"
"MB v[144:159],a[236:239],v[28:31]\n"
"GL v130\n"
"GH v131\n"
"GL v132\n"
"GH v133\n"
"GL v136\n"
"GH v137\n"
"v_permlane16_swap_b32_e64 v211, v212\n"
"EX v135,0\n"
"v_add_f32_e64 v212, v211, v212\n"
"GA v138\n"
"GA v139\n"
"v_permlane32_swap_b32 v62, v212\n"
"v_add_f32 v212, v62, v212\n"
"EX v143,0\n"
"v_add_f32_e64 v5, v5, v212\n"
"GA v140\n"
"GA v141\n"
"MB v[144:159],a[240:243],v[68:71]\n"
"buffer_store_dwordx4 a[140:143], v61, s[40:43], s22 offen offset:256 sc1\n"
"GL v138\n"
"GH v139\n"
"GL v140\n"
"GH v141\n"
"EX v135,1\n"
"EX v143,1\n"
"PF v135,v135,s57,s57\n"
"MB v[144:159],a[244:247],v[76:79]\n"
"PF v143,v143,s57,s57\n"
"RC v135,0\n"
"WL 6\n"
"v_or_b32 v132, v132, v133\n"
"v_or_b32_e64 v130, v130, v131\n"
"RC v143,0\n"
"v_or_b32_e64 v128, v128, v129\n"
"MB v[144:159],a[248:251],v[100:103]\n"
"s_add_u32 s22, s61, 294912\n"
"PM v50,v132,v132\n"
"PM v49,v130,v130\n"
"PM v48,v128,v128\n"
"AW a98,v132\n"
"AW a97,v130\n"
"AW a96,v128\n"
"RC v135,1\n"
"RC v143,1\n"
"MB v[144:159],a[252:255],v[108:111]\n"
"WL 0\n"
"v_or_b32 v140, v140, v141\n"
"v_or_b32 v138, v138, v139\n"
"v_or_b32 v136, v136, v137\n"
"PM v54,v140,v140\n"
"PM v53,v138,v138\n"
"PM v52,v136,v136\n"
"PM v134,v134,v135\n"
"AW a102,v140\n"
"AW a101,v138\n"
"PM v142,v142,v143\n"
"AW a100,v136\n"
"PM v51,v134,v134\n"
"PM v55,v142,v142\n"
"AW a99,v134\n"
"AW a103,v142\n"
"GA v144\n"
"GA v145\n"
"CH v150,v150,v151\n"
"CH v158,v158,v159\n"
"GA v146\n"
"MZ v[128:143],a[232:235],v[32:35]\n"
"GL v144\n"
"GH v145\n"
"buffer_store_dwordx4 a[144:147], v61, s[40:43], s22 offen offset:0 sc1\n"
"GA v147\n"
"PF v151,v150,v150,s56\n"
"PF v159,v158,v158,s56\n"
"GA v148\n"
"GA v149\n"
"PM v151,v151,v150\n"
"PM v159,v159,v158\n"
"GA v152\n"
"GA v153\n"
"MB v[128:143],a[236:239],v[40:43]\n"
"GL v146\n"
"GH v147\n"
"GL v148\n"
"GH v149\n"
"GL v152\n"
"GH v153\n"
"EX v151,0\n"
"GA v154\n"
"GA v155\n"
"EX v159,0\n"
"GA v156\n"
"GA v157\n"
"MB v[128:143],a[240:243],v[80:83]\n"
"GL v154\n"
"GH v155\n"
"GL v156\n"
"GH v157\n"
"EX v151,1\n"
"EX v159,1\n"
"PF v151,v151,s57,s57\n"
"MB v[128:143],a[244:247],v[88:91]\n"
"PF v159,v159,s57,s57\n"
"RC v151,0\n"
"WL 6\n"
"v_or_b32 v148, v148, v149\n"
"v_or_b32_e64 v146, v146, v147\n"
"RC v159,0\n"
"v_or_b32_e64 v144, v144, v145\n"
"MB v[128:143],a[248:251],v[112:115]\n"
"PF v50,v148,v148,v50\n"
"PF v49,v146,v146,v49\n"
"PF v48,v144,v144,v48\n"
"AW a106,v148\n"
"AW a105,v146\n"
"AW a104,v144\n"
"RC v151,1\n"
"RC v159,1\n"
"MB v[128:143],a[252:255],v[120:123]\n"
"buffer_store_dwordx4 a[148:151], v61, s[40:43], s22 offen offset:256 sc1\n"
"WL 0\n"
"v_or_b32 v156, v156, v157\n"
"v_or_b32 v154, v154, v155\n"
"v_or_b32 v152, v152, v153\n"
"PF v54,v156,v156,v54\n"
"PF v53,v154,v154,v53\n"
"PF v52,v152,v152,v52\n"
"PM v150,v150,v151\n"
"AW a110,v156\n"
"AW a109,v154\n"
"PM v158,v158,v159\n"
"AW a108,v152\n"
"PF v51,v150,v150,v51\n"
"PF v55,v158,v158,v55\n"
"AW a107,v150\n"
"AW a111,v158\n"
"GA v128\n"
"GA v129\n"
"CH v134,v134,v135\n"
"CH v142,v142,v143\n"
"GA v130\n"
"MZ v[144:159],a[232:235],v[36:39]\n"
"GL v128\n"
"GH v129\n"
"LD a[232:235],v1,43008\n"
"GA v131\n"
"PF v135,v134,v134,s56\n"
"PF v143,v142,v142,s56\n"
"GA v132\n"
"GA v133\n"
"PM v135,v135,v134\n"
"PM v143,v143,v142\n"
"GA v136\n"
"GA v137\n"
"MB v[144:159],a[236:239],v[44:47]\n"
"GL v130\n"
"GH v131\n"
"GL v132\n"
"GH v133\n"
"GL v136\n"
"GH v137\n"
"LD a[236:239],v1,44032\n"
"EX v135,0\n"
"GA v138\n"
"GA v139\n"
"s_add_u32 s22, s61, 296960\n"
"EX v143,0\n"
"GA v140\n"
"GA v141\n"
"MB v[144:159],a[240:243],v[84:87]\n"
"GL v138\n"
"GH v139\n"
"GL v140\n"
"GH v141\n"
"LD a[240:243],v1,45056\n"
"EX v135,1\n"
"EX v143,1\n"
"PF v135,v135,s57,s57\n"
"MB v[144:159],a[244:247],v[92:95]\n"
"LD a[244:247],v1,46080\n"
"PF v143,v143,s57,s57\n"
"RC v135,0\n"
"WL 9\n"
"v_or_b32 v210, v132, v133\n"
"v_or_b32_e64 v209, v130, v131\n"
"RC v143,0\n"
"v_or_b32_e64 v208, v128, v129\n"
"MB v[144:159],a[248:251],v[116:119]\n"
"buffer_store_dwordx4 a[152:155], v61, s[40:43], s22 offen offset:0 sc1\n"
"LD a[248:251],v1,47104\n"
"PF v50,v210,v210,v50\n"
"PF v49,v209,v209,v49\n"
"PF v48,v208,v208,v48\n"
"RC v135,1\n"
"RC v143,1\n"
"MB v[144:159],a[252:255],v[124:127]\n"
"LD a[252:255],v1,48128\n"
"WL 4\n"
"v_or_b32 v214, v140, v141\n"
"v_or_b32 v213, v138, v139\n"
"v_or_b32 v212, v136, v137\n"
"PF v54,v214,v214,v54\n"
"PF v53,v213,v213,v53\n"
"PF v52,v212,v212,v52\n"
"PM v211,v134,v135\n"
"PM v215,v142,v143\n"
"PF v51,v211,v211,v51\n"
"PF v55,v215,v215,v55\n"
"GA v144\n"
"GA v145\n"
"CH v150,v150,v151\n"
"CH v158,v158,v159\n"
"GA v146\n"
"MZ v[128:143],a[232:235],v[16:19]\n"
"GL v144\n"
"GH v145\n"
"GA v147\n"
"PF v151,v150,v150,s56\n"
"PF v159,v158,v158,s56\n"
"GA v148\n"
"GA v149\n"
"PM v151,v151,v150\n"
"PM v159,v159,v158\n"
"GA v152\n"
"GA v153\n"
"MB v[128:143],a[236:239],v[24:27]\n"
"GL v146\n"
"GH v147\n"
"GL v148\n"
"GH v149\n"
"GL v152\n"
"GH v153\n"
"EX v151,0\n"
"GA v154\n"
"GA v155\n"
"EX v159,0\n"
"GA v156\n"
"GA v157\n"
"WL 8\n"
"s_nop 0\n"
"MB v[128:143],a[240:243],v[64:67]\n"
"GL v154\n"
"GH v155\n"
"GL v156\n"
"GH v157\n"
"EX v151,1\n"
"EX v159,1\n"
"PF v151,v151,s57,s57\n"
"MB v[128:143],a[244:247],v[72:75]\n"
"buffer_store_dwordx4 a[156:159], v61, s[40:43], s22 offen offset:256 sc1\n"
"PF v159,v159,s57,s57\n"
"RC v151,0\n"
"WL 6\n"
"v_or_b32 v218, v148, v149\n"
"v_or_b32_e64 v217, v146, v147\n"
"RC v159,0\n"
"v_or_b32_e64 v216, v144, v145\n"
"MB v[128:143],a[248:251],v[96:99]\n"
"PF v50,v218,v218,v50\n"
"PF v49,v217,v217,v49\n"
"PF v48,v216,v216,v48\n"
"RC v151,1\n"
"RC v159,1\n"
"MB v[128:143],a[252:255],v[104:107]\n"
"WL 0\n"
"v_or_b32 v222, v156, v157\n"
"v_or_b32 v221, v154, v155\n"
"v_or_b32 v220, v152, v153\n"
"s_add_u32 s22, s61, 299008\n"
"PF v54,v222,v222,v54\n"
"PF v53,v221,v221,v53\n"
"PF v52,v220,v220,v52\n"
"PM v219,v150,v151\n"
"PM v223,v158,v159\n"
"PF v51,v219,v219,v51\n"
"PF v55,v223,v223,v55\n"
"GA v128\n"
"GA v129\n"
"v_mfma_f32_16x16x32_f16 v[48:51], v[48:51], v[56:59], 0\n"
"CH v134,v134,v135\n"
"GL v128\n"
"GH v129\n"
"v_mfma_f32_16x16x32_f16 v[52:55], v[52:55], v[56:59], 0\n"
"CH v142,v142,v143\n"
"GA v130\n"
"MZ v[144:159],a[232:235],v[20:23]\n"
"GA v131\n"
"PF v135,v134,v134,s56\n"
"PF v143,v142,v142,s56\n"
"GA v132\n"
"GA v133\n"
"PM v135,v135,v134\n"
"PM v143,v143,v142\n"
"GA v136\n"
"GA v137\n"
"v_pk_add_f32 v[50:51], v[48:49], v[50:51]\n"
"v_pk_add_f32 v[54:55], v[52:53], v[54:55]\n"
"v_add_f32 v51, v50, v51\n"
"v_add_f32 v52, v54, v55\n"
"MB v[144:159],a[236:239],v[28:31]\n"
"GL v130\n"
"GH v131\n"
"GL v132\n"
"GH v133\n"
"GL v136\n"
"GH v137\n"
"v_permlane16_swap_b32_e64 v51, v52\n"
"EX v135,0\n"
"v_add_f32_e64 v62, v51, v52\n"
"GA v138\n"
"GA v139\n"
"EX v143,0\n"
"GA v140\n"
"GA v141\n"
"MB v[144:159],a[240:243],v[68:71]\n"
"buffer_store_dwordx4 a[160:163], v61, s[40:43], s22 offen offset:0 sc1\n"
"GL v138\n"
"GH v139\n"
"GL v140\n"
"GH v141\n"
"EX v135,1\n"
"EX v143,1\n"
"PF v135,v135,s57,s57\n"
"MB v[144:159],a[244:247],v[76:79]\n"
"PF v143,v143,s57,s57\n"
"RC v135,0\n"
"WL 6\n"
"v_or_b32 v132, v132, v133\n"
"v_or_b32_e64 v130, v130, v131\n"
"RC v143,0\n"
"v_or_b32_e64 v128, v128, v129\n"
"MB v[144:159],a[248:251],v[100:103]\n"
"PM v50,v132,v132\n"
"PM v49,v130,v130\n"
"PM v48,v128,v128\n"
"AW a114,v132\n"
"AW a113,v130\n"
"AW a112,v128\n"
"RC v135,1\n"
"RC v143,1\n"
"MB v[144:159],a[252:255],v[108:111]\n"
"WL 0\n"
"v_or_b32 v140, v140, v141\n"
"v_or_b32 v138, v138, v139\n"
"v_or_b32 v136, v136, v137\n"
"PM v54,v140,v140\n"
"PM v53,v138,v138\n"
"PM v52,v136,v136\n"
"PM v134,v134,v135\n"
"AW a118,v140\n"
"AW a117,v138\n"
"PM v142,v142,v143\n"
"AW a116,v136\n"
"PM v51,v134,v134\n"
"PM v55,v142,v142\n"
"AW a115,v134\n"
"AW a119,v142\n"
"GA v144\n"
"GA v145\n"
"CH v150,v150,v151\n"
"CH v158,v158,v159\n"
"GA v146\n"
"MZ v[128:143],a[232:235],v[32:35]\n"
"GL v144\n"
"GH v145\n"
"buffer_store_dwordx4 a[164:167], v61, s[40:43], s22 offen offset:256 sc1\n"
"GA v147\n"
"PF v151,v150,v150,s56\n"
"PF v159,v158,v158,s56\n"
"GA v148\n"
"GA v149\n"
"PM v151,v151,v150\n"
"PM v159,v159,v158\n"
"GA v152\n"
"GA v153\n"
"MB v[128:143],a[236:239],v[40:43]\n"
"GL v146\n"
"GH v147\n"
"GL v148\n"
"GH v149\n"
"GL v152\n"
"GH v153\n"
"EX v151,0\n"
"GA v154\n"
"GA v155\n"
"EX v159,0\n"
"GA v156\n"
"GA v157\n"
"MB v[128:143],a[240:243],v[80:83]\n"
"GL v154\n"
"GH v155\n"
"GL v156\n"
"GH v157\n"
"EX v151,1\n"
"EX v159,1\n"
"s_add_u32 s22, s61, 198656\n"
"PF v151,v151,s57,s57\n"
"MB v[128:143],a[244:247],v[88:91]\n"
"PF v159,v159,s57,s57\n"
"RC v151,0\n"
"WL 6\n"
"v_or_b32 v148, v148, v149\n"
"v_or_b32_e64 v146, v146, v147\n"
"RC v159,0\n"
"v_or_b32_e64 v144, v144, v145\n"
"MB v[128:143],a[248:251],v[112:115]\n"
"PF v50,v148,v148,v50\n"
"PF v49,v146,v146,v49\n"
"PF v48,v144,v144,v48\n"
"AW a122,v148\n"
"AW a121,v146\n"
"AW a120,v144\n"
"RC v151,1\n"
"RC v159,1\n"
"MB v[128:143],a[252:255],v[120:123]\n"
"buffer_store_dwordx4 a[168:171], v61, s[40:43], s22 offen offset:0 sc1\n"
"WL 0\n"
"v_or_b32 v156, v156, v157\n"
"v_or_b32 v154, v154, v155\n"
"v_or_b32 v152, v152, v153\n"
"PF v54,v156,v156,v54\n"
"PF v53,v154,v154,v53\n"
"PF v52,v152,v152,v52\n"
"PM v150,v150,v151\n"
"AW a126,v156\n"
"AW a125,v154\n"
"PM v158,v158,v159\n"
"AW a124,v152\n"
"PF v51,v150,v150,v51\n"
"PF v55,v158,v158,v55\n"
"AW a123,v150\n"
"AW a127,v158\n"
"GA v128\n"
"GA v129\n"
"CH v134,v134,v135\n"
"CH v142,v142,v143\n"
"GA v130\n"
"MZ v[144:159],a[232:235],v[36:39]\n"
"GL v128\n"
"GH v129\n"
"LD a[232:235],v1,49152\n"
"GA v131\n"
"PF v135,v134,v134,s56\n"
"PF v143,v142,v142,s56\n"
"GA v132\n"
"GA v133\n"
"PM v135,v135,v134\n"
"PM v143,v143,v142\n"
"GA v136\n"
"GA v137\n"
"MB v[144:159],a[236:239],v[44:47]\n"
"GL v130\n"
"GH v131\n"
"GL v132\n"
"GH v133\n"
"buffer_load_dword v60, v0, s[24:27], s28 offen\n"
"GL v136\n"
"GH v137\n"
"LD a[236:239],v1,50176\n"
"EX v135,0\n"
"GA v138\n"
"GA v139\n"
"EX v143,0\n"
"GA v140\n"
"GA v141\n"
"MB v[144:159],a[240:243],v[84:87]\n"
"GL v138\n"
"GH v139\n"
"GL v140\n"
"GH v141\n"
"LD a[240:243],v1,51200\n"
"EX v135,1\n"
"EX v143,1\n"
"PF v135,v135,s57,s57\n"
"MB v[144:159],a[244:247],v[92:95]\n"
"LD a[244:247],v1,52224\n"
"PF v143,v143,s57,s57\n"
"RC v135,0\n"
"WL 9\n"
"v_or_b32 v226, v132, v133\n"
"v_or_b32_e64 v225, v130, v131\n"
"RC v143,0\n"
"v_or_b32_e64 v224, v128, v129\n"
"MB v[144:159],a[248:251],v[116:119]\n"
"buffer_store_dwordx4 a[172:175], v61, s[40:43], s22 offen offset:256 sc1\n"
"LD a[248:251],v1,53248\n"
"PF v50,v226,v226,v50\n"
"PF v49,v225,v225,v49\n"
"PF v48,v224,v224,v48\n"
"RC v135,1\n"
"RC v143,1\n"
"MB v[144:159],a[252:255],v[124:127]\n"
"LD a[252:255],v1,54272\n"
"WL 4\n"
"v_or_b32 v230, v140, v141\n"
"v_or_b32 v229, v138, v139\n"
"v_or_b32 v228, v136, v137\n"
"PF v54,v230,v230,v54\n"
"PF v53,v229,v229,v53\n"
"PF v52,v228,v228,v52\n"
"PM v227,v134,v135\n"
"PM v231,v142,v143\n"
"PF v51,v227,v227,v51\n"
"PF v55,v231,v231,v55\n"
"GA v144\n"
"GA v145\n"
"CH v150,v150,v151\n"
"CH v158,v158,v159\n"
"GA v146\n"
"MZ v[128:143],a[232:235],v[16:19]\n"
"GL v144\n"
"GH v145\n"
"GA v147\n"
"PF v151,v150,v150,s56\n"
"s_add_u32 s22, s61, 196608\n"
"PF v159,v158,v158,s56\n"
"GA v148\n"
"GA v149\n"
"PM v151,v151,v150\n"
"PM v159,v159,v158\n"
"GA v152\n"
"GA v153\n"
"MB v[128:143],a[236:239],v[24:27]\n"
"GL v146\n"
"GH v147\n"
"GL v148\n"
"GH v149\n"
"GL v152\n"
"GH v153\n"
"EX v151,0\n"
"GA v154\n"
"GA v155\n"
"EX v159,0\n"
"GA v156\n"
"GA v157\n"
"WL 8\n"
"s_nop 0\n"
"MB v[128:143],a[240:243],v[64:67]\n"
"GL v154\n"
"GH v155\n"
"GL v156\n"
"GH v157\n"
"EX v151,1\n"
"EX v159,1\n"
"PF v151,v151,s57,s57\n"
"MB v[128:143],a[244:247],v[72:75]\n"
"buffer_store_dwordx4 a[176:179], v61, s[40:43], s22 offen offset:0 sc1\n"
"PF v159,v159,s57,s57\n"
"RC v151,0\n"
"WL 6\n"
"v_or_b32 v234, v148, v149\n"
"v_or_b32_e64 v233, v146, v147\n"
"RC v159,0\n"
"v_or_b32_e64 v232, v144, v145\n"
"MB v[128:143],a[248:251],v[96:99]\n"
"PF v50,v234,v234,v50\n"
"PF v49,v233,v233,v49\n"
"PF v48,v232,v232,v48\n"
"RC v151,1\n"
"RC v159,1\n"
"MB v[128:143],a[252:255],v[104:107]\n"
"WL 0\n"
"v_or_b32 v238, v156, v157\n"
"v_or_b32_e64 v237, v154, v155\n"
"v_or_b32 v236, v152, v153\n"
"PF v54,v238,v238,v54\n"
"PF v53,v237,v237,v53\n"
"PF v52,v236,v236,v52\n"
"PM v235,v150,v151\n"
"PM v239,v158,v159\n"
"PF v51,v235,v235,v51\n"
"PF v55,v239,v239,v55\n"
"s_nop 0\n"
"GA v128\n"
"GA v129\n"
"v_mfma_f32_16x16x32_f16 v[48:51], v[48:51], v[56:59], 0\n"
"CH v134,v134,v135\n"
"GL v128\n"
"GH v129\n"
"v_mfma_f32_16x16x32_f16 v[52:55], v[52:55], v[56:59], 0\n"
"CH v142,v142,v143\n"
"GA v130\n"
"MZ v[144:159],a[232:235],v[20:23]\n"
"GA v131\n"
"PF v135,v134,v134,s56\n"
"PF v143,v142,v142,s56\n"
"GA v132\n"
"GA v133\n"
"PM v135,v135,v134\n"
"PM v143,v143,v142\n"
"GA v136\n"
"GA v137\n"
"v_pk_add_f32 v[50:51], v[48:49], v[50:51]\n"
"v_pk_add_f32 v[54:55], v[52:53], v[54:55]\n"
"v_add_f32 v51, v50, v51\n"
"v_add_f32 v52, v54, v55\n"
"MB v[144:159],a[236:239],v[28:31]\n"
"GL v130\n"
"GH v131\n"
"GL v132\n"
"GH v133\n"
"GL v136\n"
"GH v137\n"
"v_permlane16_swap_b32_e64 v51, v52\n"
"EX v135,0\n"
"v_add_f32_e64 v52, v51, v52\n"
"GA v138\n"
"GA v139\n"
"v_permlane32_swap_b32 v62, v52\n"
"v_add_f32 v52, v62, v52\n"
"EX v143,0\n"
"v_add_f32_e64 v6, v6, v52\n"
"GA v140\n"
"GA v141\n"
"MB v[144:159],a[240:243],v[68:71]\n"
"buffer_store_dwordx4 a[180:183], v61, s[40:43], s22 offen offset:256 sc1\n"
"GL v138\n"
"GH v139\n"
"GL v140\n"
"GH v141\n"
"EX v135,1\n"
"EX v143,1\n"
"PF v135,v135,s57,s57\n"
"MB v[144:159],a[244:247],v[76:79]\n"
"PF v143,v143,s57,s57\n"
"RC v135,0\n"
"WL 6\n"
"v_or_b32 v132, v132, v133\n"
"v_or_b32_e64 v130, v130, v131\n"
"RC v143,0\n"
"v_or_b32_e64 v128, v128, v129\n"
"MB v[144:159],a[248:251],v[100:103]\n"
"s_add_u32 s22, s61, 200704\n"
"PM v50,v132,v132\n"
"PM v49,v130,v130\n"
"PM v48,v128,v128\n"
"AW a130,v132\n"
"AW a129,v130\n"
"AW a128,v128\n"
"RC v135,1\n"
"RC v143,1\n"
"MB v[144:159],a[252:255],v[108:111]\n"
"WL 0\n"
"v_or_b32 v140, v140, v141\n"
"v_or_b32 v138, v138, v139\n"
"v_or_b32 v136, v136, v137\n"
"PM v54,v140,v140\n"
"PM v53,v138,v138\n"
"PM v52,v136,v136\n"
"PM v134,v134,v135\n"
"AW a134,v140\n"
"AW a133,v138\n"
"PM v142,v142,v143\n"
"AW a132,v136\n"
"PM v51,v134,v134\n"
"PM v55,v142,v142\n"
"AW a131,v134\n"
"AW a135,v142\n"
"GA v144\n"
"GA v145\n"
"CH v150,v150,v151\n"
"CH v158,v158,v159\n"
"GA v146\n"
"MZ v[128:143],a[232:235],v[32:35]\n"
"GL v144\n"
"GH v145\n"
"buffer_store_dwordx4 a[184:187], v61, s[40:43], s22 offen offset:0 sc1\n"
"GA v147\n"
"PF v151,v150,v150,s56\n"
"PF v159,v158,v158,s56\n"
"GA v148\n"
"GA v149\n"
"PM v151,v151,v150\n"
"PM v159,v159,v158\n"
"GA v152\n"
"GA v153\n"
"MB v[128:143],a[236:239],v[40:43]\n"
"GL v146\n"
"GH v147\n"
"GL v148\n"
"GH v149\n"
"GL v152\n"
"GH v153\n"
"EX v151,0\n"
"GA v154\n"
"GA v155\n"
"EX v159,0\n"
"GA v156\n"
"GA v157\n"
"MB v[128:143],a[240:243],v[80:83]\n"
"GL v154\n"
"GH v155\n"
"GL v156\n"
"GH v157\n"
"EX v151,1\n"
"EX v159,1\n"
"PF v151,v151,s57,s57\n"
"MB v[128:143],a[244:247],v[88:91]\n"
"PF v159,v159,s57,s57\n"
"RC v151,0\n"
"WL 6\n"
"v_or_b32 v148, v148, v149\n"
"v_or_b32_e64 v146, v146, v147\n"
"RC v159,0\n"
"v_or_b32_e64 v144, v144, v145\n"
"MB v[128:143],a[248:251],v[112:115]\n"
"PF v50,v148,v148,v50\n"
"PF v49,v146,v146,v49\n"
"PF v48,v144,v144,v48\n"
"AW a138,v148\n"
"AW a137,v146\n"
"AW a136,v144\n"
"RC v151,1\n"
"RC v159,1\n"
"MB v[128:143],a[252:255],v[120:123]\n"
"buffer_store_dwordx4 a[188:191], v61, s[40:43], s22 offen offset:256 sc1\n"
"WL 0\n"
"v_or_b32 v156, v156, v157\n"
"v_or_b32 v154, v154, v155\n"
"v_or_b32 v152, v152, v153\n"
"PF v54,v156,v156,v54\n"
"PF v53,v154,v154,v53\n"
"PF v52,v152,v152,v52\n"
"PM v150,v150,v151\n"
"AW a142,v156\n"
"AW a141,v154\n"
"PM v158,v158,v159\n"
"AW a140,v152\n"
"PF v51,v150,v150,v51\n"
"PF v55,v158,v158,v55\n"
"AW a139,v150\n"
"AW a143,v158\n"
"GA v128\n"
"GA v129\n"
"CH v134,v134,v135\n"
"CH v142,v142,v143\n"
"GA v130\n"
"MZ v[144:159],a[232:235],v[36:39]\n"
"GL v128\n"
"GH v129\n"
"LD a[232:235],v1,55296\n"
"GA v131\n"
"PF v135,v134,v134,s56\n"
"PF v143,v142,v142,s56\n"
"GA v132\n"
"GA v133\n"
"PM v135,v135,v134\n"
"PM v143,v143,v142\n"
"GA v136\n"
"GA v137\n"
"MB v[144:159],a[236:239],v[44:47]\n"
"GL v130\n"
"GH v131\n"
"GL v132\n"
"GH v133\n"
"GL v136\n"
"GH v137\n"
"LD a[236:239],v1,56320\n"
"EX v135,0\n"
"GA v138\n"
"GA v139\n"
"EX v143,0\n"
"GA v140\n"
"GA v141\n"
"MB v[144:159],a[240:243],v[84:87]\n"
"GL v138\n"
"GH v139\n"
"GL v140\n"
"GH v141\n"
"LD a[240:243],v1,57344\n"
"EX v135,1\n"
"EX v143,1\n"
"PF v135,v135,s57,s57\n"
"MB v[144:159],a[244:247],v[92:95]\n"
"LD a[244:247],v1,58368\n"
"PF v143,v143,s57,s57\n"
"RC v135,0\n"
"WL 9\n"
"v_or_b32 v242, v132, v133\n"
"v_or_b32_e64 v241, v130, v131\n"
"RC v143,0\n"
"v_or_b32_e64 v240, v128, v129\n"
"MB v[144:159],a[248:251],v[116:119]\n"
"LD a[248:251],v1,59392\n"
"PF v50,v242,v242,v50\n"
"PF v49,v241,v241,v49\n"
"PF v48,v240,v240,v48\n"
"RC v135,1\n"
"RC v143,1\n"
"MB v[144:159],a[252:255],v[124:127]\n"
"LD a[252:255],v1,60416\n"
"WL 4\n"
"v_or_b32 v246, v140, v141\n"
"v_or_b32 v245, v138, v139\n"
"v_or_b32 v244, v136, v137\n"
"PF v54,v246,v246,v54\n"
"PF v53,v245,v245,v53\n"
"PF v52,v244,v244,v52\n"
"PM v243,v134,v135\n"
"PM v247,v142,v143\n"
"PF v51,v243,v243,v51\n"
"PF v55,v247,v247,v55\n"
"GA v144\n"
"GA v145\n"
"CH v150,v150,v151\n"
"CH v158,v158,v159\n"
"GA v146\n"
"MZ v[128:143],a[232:235],v[16:19]\n"
"GL v144\n"
"GH v145\n"
"GA v147\n"
"PF v151,v150,v150,s56\n"
"PF v159,v158,v158,s56\n"
"GA v148\n"
"GA v149\n"
"PM v151,v151,v150\n"
"PM v159,v159,v158\n"
"GA v152\n"
"GA v153\n"
"MB v[128:143],a[236:239],v[24:27]\n"
"GL v146\n"
"GH v147\n"
"GL v148\n"
"GH v149\n"
"GL v152\n"
"GH v153\n"
"EX v151,0\n"
"GA v154\n"
"GA v155\n"
"EX v159,0\n"
"GA v156\n"
"GA v157\n"
"WL 8\n"
"s_nop 0\n"
"MB v[128:143],a[240:243],v[64:67]\n"
"GL v154\n"
"GH v155\n"
"GL v156\n"
"GH v157\n"
"EX v151,1\n"
"EX v159,1\n"
"PF v151,v151,s57,s57\n"
"MB v[128:143],a[244:247],v[72:75]\n"
"PF v159,v159,s57,s57\n"
"RC v151,0\n"
"WL 6\n"
"v_or_b32 v250, v148, v149\n"
"v_or_b32_e64 v249, v146, v147\n"
"RC v159,0\n"
"v_or_b32_e64 v248, v144, v145\n"
"MB v[128:143],a[248:251],v[96:99]\n"
"PF v50,v250,v250,v50\n"
"PF v49,v249,v249,v49\n"
"PF v48,v248,v248,v48\n"
"RC v151,1\n"
"RC v159,1\n"
"MB v[128:143],a[252:255],v[104:107]\n"
"WL 0\n"
"v_or_b32 v254, v156, v157\n"
"v_or_b32_e64 v253, v154, v155\n"
"v_or_b32 v252, v152, v153\n"
"PF v54,v254,v254,v54\n"
"PF v53,v253,v253,v53\n"
"PF v52,v252,v252,v52\n"
"PM v251,v150,v151\n"
"PM v255,v158,v159\n"
"PF v51,v251,v251,v51\n"
"PF v55,v255,v255,v55\n"
"s_nop 0\n"
"GA v128\n"
"GA v129\n"
"v_mfma_f32_16x16x32_f16 v[48:51], v[48:51], v[56:59], 0\n"
"CH v134,v134,v135\n"
"GL v128\n"
"GH v129\n"
"v_mfma_f32_16x16x32_f16 v[52:55], v[52:55], v[56:59], 0\n"
"CH v142,v142,v143\n"
"GA v130\n"
"MZ v[144:159],a[232:235],v[20:23]\n"
"LD a[232:235],v1,61440\n"
"GA v131\n"
"PF v135,v134,v134,s56\n"
"PF v143,v142,v142,s56\n"
"GA v132\n"
"GA v133\n"
"PM v135,v135,v134\n"
"PM v143,v143,v142\n"
"GA v136\n"
"v_pk_add_f32 v[50:51], v[48:49], v[50:51]\n"
"v_pk_add_f32 v[54:55], v[52:53], v[54:55]\n"
"GA v137\n"
"v_add_f32 v51, v50, v51\n"
"v_add_f32 v52, v54, v55\n"
"MB v[144:159],a[236:239],v[28:31]\n"
"GL v130\n"
"GH v131\n"
"GL v132\n"
"GH v133\n"
"GL v136\n"
"GH v137\n"
"LD a[236:239],v1,62464\n"
"v_permlane16_swap_b32_e64 v51, v52\n"
"EX v135,0\n"
"v_add_f32_e64 v62, v51, v52\n"
"GA v138\n"
"GA v139\n"
"EX v143,0\n"
"GA v140\n"
"GA v141\n"
"MB v[144:159],a[240:243],v[68:71]\n"
"GL v138\n"
"GH v139\n"
"GL v140\n"
"GH v141\n"
"LD a[240:243],v1,63488\n"
"EX v135,1\n"
"EX v143,1\n"
"PF v135,v135,s57,s57\n"
"MB v[144:159],a[244:247],v[76:79]\n"
"LD a[244:247],v1,64512\n"
"PF v143,v143,s57,s57\n"
"RC v135,0\n"
"WL 9\n"
"v_or_b32 v132, v132, v133\n"
"v_or_b32_e64 v130, v130, v131\n"
"RC v143,0\n"
"v_or_b32_e64 v128, v128, v129\n"
"MB v[144:159],a[248:251],v[100:103]\n"
"LD a[248:251],v2,0\n"
"PM v50,v132,v132\n"
"PM v49,v130,v130\n"
"PM v48,v128,v128\n"
"AW a146,v132\n"
"AW a145,v130\n"
"AW a144,v128\n"
"RC v135,1\n"
"RC v143,1\n"
"MB v[144:159],a[252:255],v[108:111]\n"
"LD a[252:255],v2,1024\n"
"WL 4\n"
"v_or_b32 v140, v140, v141\n"
"v_or_b32 v138, v138, v139\n"
"v_or_b32 v136, v136, v137\n"
"PM v54,v140,v140\n"
"PM v53,v138,v138\n"
"PM v52,v136,v136\n"
"PM v134,v134,v135\n"
"AW a150,v140\n"
"AW a149,v138\n"
"PM v142,v142,v143\n"
"AW a148,v136\n"
"PM v51,v134,v134\n"
"PM v55,v142,v142\n"
"AW a147,v134\n"
"AW a151,v142\n"
"GA v144\n"
"GA v145\n"
"CH v150,v150,v151\n"
"CH v158,v158,v159\n"
"GA v146\n"
"MZ v[128:143],a[232:235],v[16:19]\n"
"GL v144\n"
"GH v145\n"
"GA v147\n"
"PF v151,v150,v150,s56\n"
"PF v159,v158,v158,s56\n"
"GA v148\n"
"GA v149\n"
"PM v151,v151,v150\n"
"PM v159,v159,v158\n"
"GA v152\n"
"GA v153\n"
"MB v[128:143],a[236:239],v[24:27]\n"
"GL v146\n"
"GH v147\n"
"GL v148\n"
"GH v149\n"
"GL v152\n"
"GH v153\n"
"EX v151,0\n"
"GA v154\n"
"GA v155\n"
"EX v159,0\n"
"GA v156\n"
"GA v157\n"
"WL 8\n"
"s_nop 0\n"
"MB v[128:143],a[240:243],v[64:67]\n"
"GL v154\n"
"GH v155\n"
"GL v156\n"
"GH v157\n"
"EX v151,1\n"
"EX v159,1\n"
"PF v151,v151,s57,s57\n"
"MB v[128:143],a[244:247],v[72:75]\n"
"PF v159,v159,s57,s57\n"
"RC v151,0\n"
"WL 6\n"
"v_or_b32 v148, v148, v149\n"
"v_or_b32_e64 v146, v146, v147\n"
"RC v159,0\n"
"v_or_b32_e64 v144, v144, v145\n"
"MB v[128:143],a[248:251],v[96:99]\n"
"PF v50,v148,v148,v50\n"
"PF v49,v146,v146,v49\n"
"PF v48,v144,v144,v48\n"
"AW a154,v148\n"
"AW a153,v146\n"
"AW a152,v144\n"
"RC v151,1\n"
"RC v159,1\n"
"MB v[128:143],a[252:255],v[104:107]\n"
"WL 0\n"
"v_or_b32 v156, v156, v157\n"
"v_or_b32 v154, v154, v155\n"
"v_or_b32 v152, v152, v153\n"
"PF v54,v156,v156,v54\n"
"PF v53,v154,v154,v53\n"
"PF v52,v152,v152,v52\n"
"PM v150,v150,v151\n"
"AW a158,v156\n"
"AW a157,v154\n"
"PM v158,v158,v159\n"
"AW a156,v152\n"
"PF v51,v150,v150,v51\n"
"PF v55,v158,v158,v55\n"
"AW a155,v150\n"
"AW a159,v158\n"
"GA v128\n"
"GA v129\n"
"v_mfma_f32_16x16x32_f16 v[48:51], v[48:51], v[56:59], 0\n"
"CH v134,v134,v135\n"
"GL v128\n"
"GH v129\n"
"v_mfma_f32_16x16x32_f16 v[52:55], v[52:55], v[56:59], 0\n"
"CH v142,v142,v143\n"
"GA v130\n"
"MZ v[144:159],a[232:235],v[20:23]\n"
"LD a[232:235],v2,2048\n"
"GA v131\n"
"PF v135,v134,v134,s56\n"
"PF v143,v142,v142,s56\n"
"GA v132\n"
"GA v133\n"
"PM v135,v135,v134\n"
"PM v143,v143,v142\n"
"GA v136\n"
"v_pk_add_f32 v[50:51], v[48:49], v[50:51]\n"
"v_pk_add_f32 v[54:55], v[52:53], v[54:55]\n"
"GA v137\n"
"v_add_f32 v51, v50, v51\n"
"v_add_f32 v52, v54, v55\n"
"MB v[144:159],a[236:239],v[28:31]\n"
"GL v130\n"
"GH v131\n"
"GL v132\n"
"GH v133\n"
"GL v136\n"
"GH v137\n"
"LD a[236:239],v2,3072\n"
"v_permlane16_swap_b32_e64 v51, v52\n"
"EX v135,0\n"
"v_add_f32_e64 v52, v51, v52\n"
"GA v138\n"
"GA v139\n"
"v_permlane32_swap_b32 v62, v52\n"
"v_add_f32 v52, v62, v52\n"
"EX v143,0\n"
"v_add_f32_e64 v7, v7, v52\n"
"GA v140\n"
"GA v141\n"
"MB v[144:159],a[240:243],v[68:71]\n"
"GL v138\n"
"GH v139\n"
"GL v140\n"
"GH v141\n"
"LD a[240:243],v2,4096\n"
"EX v135,1\n"
"EX v143,1\n"
"PF v135,v135,s57,s57\n"
"MB v[144:159],a[244:247],v[76:79]\n"
"LD a[244:247],v2,5120\n"
"PF v143,v143,s57,s57\n"
"RC v135,0\n"
"WL 9\n"
"v_or_b32 v132, v132, v133\n"
"v_or_b32_e64 v130, v130, v131\n"
"RC v143,0\n"
"v_or_b32_e64 v128, v128, v129\n"
"MB v[144:159],a[248:251],v[100:103]\n"
"LD a[248:251],v2,6144\n"
"PM v50,v132,v132\n"
"PM v49,v130,v130\n"
"PM v48,v128,v128\n"
"AW a162,v132\n"
"AW a161,v130\n"
"AW a160,v128\n"
"RC v135,1\n"
"RC v143,1\n"
"MB v[144:159],a[252:255],v[108:111]\n"
"LD a[252:255],v2,7168\n"
"WL 4\n"
"v_or_b32 v140, v140, v141\n"
"v_or_b32 v138, v138, v139\n"
"v_or_b32 v136, v136, v137\n"
"PM v54,v140,v140\n"
"PM v53,v138,v138\n"
"PM v52,v136,v136\n"
"PM v134,v134,v135\n"
"AW a166,v140\n"
"AW a165,v138\n"
"PM v142,v142,v143\n"
"AW a164,v136\n"
"PM v51,v134,v134\n"
"PM v55,v142,v142\n"
"AW a163,v134\n"
"AW a167,v142\n"
"GA v144\n"
"GA v145\n"
"CH v150,v150,v151\n"
"CH v158,v158,v159\n"
"GA v146\n"
"MZ v[128:143],a[232:235],v[16:19]\n"
"GL v144\n"
"GH v145\n"
"GA v147\n"
"PF v151,v150,v150,s56\n"
"PF v159,v158,v158,s56\n"
"GA v148\n"
"GA v149\n"
"PM v151,v151,v150\n"
"PM v159,v159,v158\n"
"GA v152\n"
"GA v153\n"
"MB v[128:143],a[236:239],v[24:27]\n"
"GL v146\n"
"GH v147\n"
"GL v148\n"
"GH v149\n"
"GL v152\n"
"GH v153\n"
"EX v151,0\n"
"GA v154\n"
"GA v155\n"
"EX v159,0\n"
"GA v156\n"
"GA v157\n"
"WL 8\n"
"s_nop 0\n"
"MB v[128:143],a[240:243],v[64:67]\n"
"GL v154\n"
"GH v155\n"
"GL v156\n"
"GH v157\n"
"EX v151,1\n"
"EX v159,1\n"
"PF v151,v151,s57,s57\n"
"MB v[128:143],a[244:247],v[72:75]\n"
"PF v159,v159,s57,s57\n"
"RC v151,0\n"
"WL 6\n"
"v_or_b32 v148, v148, v149\n"
"v_or_b32_e64 v146, v146, v147\n"
"RC v159,0\n"
"v_or_b32_e64 v144, v144, v145\n"
"MB v[128:143],a[248:251],v[96:99]\n"
"PF v50,v148,v148,v50\n"
"PF v49,v146,v146,v49\n"
"PF v48,v144,v144,v48\n"
"AW a170,v148\n"
"AW a169,v146\n"
"AW a168,v144\n"
"RC v151,1\n"
"RC v159,1\n"
"MB v[128:143],a[252:255],v[104:107]\n"
"WL 0\n"
"v_or_b32 v156, v156, v157\n"
"v_or_b32 v154, v154, v155\n"
"v_or_b32 v152, v152, v153\n"
"PF v54,v156,v156,v54\n"
"PF v53,v154,v154,v53\n"
"PF v52,v152,v152,v52\n"
"PM v150,v150,v151\n"
"AW a174,v156\n"
"AW a173,v154\n"
"PM v158,v158,v159\n"
"AW a172,v152\n"
"PF v51,v150,v150,v51\n"
"PF v55,v158,v158,v55\n"
"AW a171,v150\n"
"AW a175,v158\n"
"GA v128\n"
"GA v129\n"
"v_mfma_f32_16x16x32_f16 v[48:51], v[48:51], v[56:59], 0\n"
"CH v134,v134,v135\n"
"GL v128\n"
"GH v129\n"
"v_mfma_f32_16x16x32_f16 v[52:55], v[52:55], v[56:59], 0\n"
"CH v142,v142,v143\n"
"GA v130\n"
"MZ v[144:159],a[232:235],v[20:23]\n"
"LD a[232:235],v1,55296\n"
"GA v131\n"
"PF v135,v134,v134,s56\n"
"PF v143,v142,v142,s56\n"
"GA v132\n"
"GA v133\n"
"PM v135,v135,v134\n"
"PM v143,v143,v142\n"
"GA v136\n"
"v_pk_add_f32 v[50:51], v[48:49], v[50:51]\n"
"v_pk_add_f32 v[54:55], v[52:53], v[54:55]\n"
"GA v137\n"
"v_add_f32 v51, v50, v51\n"
"v_add_f32 v52, v54, v55\n"
"MB v[144:159],a[236:239],v[28:31]\n"
"GL v130\n"
"GH v131\n"
"GL v132\n"
"GH v133\n"
"GL v136\n"
"GH v137\n"
"LD a[236:239],v1,56320\n"
"v_permlane16_swap_b32_e64 v51, v52\n"
"EX v135,0\n"
"v_add_f32_e64 v62, v51, v52\n"
"GA v138\n"
"GA v139\n"
"EX v143,0\n"
"GA v140\n"
"GA v141\n"
"MB v[144:159],a[240:243],v[68:71]\n"
"GL v138\n"
"GH v139\n"
"GL v140\n"
"GH v141\n"
"LD a[240:243],v1,57344\n"
"EX v135,1\n"
"EX v143,1\n"
"PF v135,v135,s57,s57\n"
"MB v[144:159],a[244:247],v[76:79]\n"
"LD a[244:247],v1,58368\n"
"PF v143,v143,s57,s57\n"
"RC v135,0\n"
"WL 9\n"
"v_or_b32 v132, v132, v133\n"
"v_or_b32_e64 v130, v130, v131\n"
"RC v143,0\n"
"v_or_b32_e64 v128, v128, v129\n"
"MB v[144:159],a[248:251],v[100:103]\n"
"LD a[248:251],v1,59392\n"
"PM v50,v132,v132\n"
"PM v49,v130,v130\n"
"PM v48,v128,v128\n"
"AW a178,v132\n"
"AW a177,v130\n"
"AW a176,v128\n"
"RC v135,1\n"
"RC v143,1\n"
"MB v[144:159],a[252:255],v[108:111]\n"
"LD a[252:255],v1,60416\n"
"WL 4\n"
"v_or_b32 v140, v140, v141\n"
"v_or_b32 v138, v138, v139\n"
"v_or_b32 v136, v136, v137\n"
"PM v54,v140,v140\n"
"PM v53,v138,v138\n"
"PM v52,v136,v136\n"
"PM v134,v134,v135\n"
"AW a182,v140\n"
"AW a181,v138\n"
"PM v142,v142,v143\n"
"AW a180,v136\n"
"PM v51,v134,v134\n"
"PM v55,v142,v142\n"
"AW a179,v134\n"
"AW a183,v142\n"
"GA v144\n"
"GA v145\n"
"CH v150,v150,v151\n"
"CH v158,v158,v159\n"
"GA v146\n"
"MZ v[128:143],a[232:235],v[32:35]\n"
"GL v144\n"
"GH v145\n"
"GA v147\n"
"PF v151,v150,v150,s56\n"
"PF v159,v158,v158,s56\n"
"GA v148\n"
"GA v149\n"
"PM v151,v151,v150\n"
"PM v159,v159,v158\n"
"GA v152\n"
"GA v153\n"
"MB v[128:143],a[236:239],v[40:43]\n"
"GL v146\n"
"GH v147\n"
"GL v148\n"
"GH v149\n"
"buffer_load_dword v60, v0, s[24:27], s28 offen\n"
"GL v152\n"
"GH v153\n"
"EX v151,0\n"
"GA v154\n"
"GA v155\n"
"EX v159,0\n"
"GA v156\n"
"GA v157\n"
"WL 8\n"
"s_nop 0\n"
"MB v[128:143],a[240:243],v[80:83]\n"
"GL v154\n"
"GH v155\n"
"GL v156\n"
"GH v157\n"
"EX v151,1\n"
"EX v159,1\n"
"PF v151,v151,s57,s57\n"
"MB v[128:143],a[244:247],v[88:91]\n"
"PF v159,v159,s57,s57\n"
"RC v151,0\n"
"WL 6\n"
"v_or_b32 v148, v148, v149\n"
"v_or_b32_e64 v146, v146, v147\n"
"RC v159,0\n"
"v_or_b32_e64 v144, v144, v145\n"
"MB v[128:143],a[248:251],v[112:115]\n"
"PF v50,v148,v148,v50\n"
"PF v49,v146,v146,v49\n"
"PF v48,v144,v144,v48\n"
"AW a186,v148\n"
"AW a185,v146\n"
"AW a184,v144\n"
"RC v151,1\n"
"RC v159,1\n"
"MB v[128:143],a[252:255],v[120:123]\n"
"WL 0\n"
"v_or_b32 v156, v156, v157\n"
"v_or_b32 v154, v154, v155\n"
"v_or_b32 v152, v152, v153\n"
"PF v54,v156,v156,v54\n"
"PF v53,v154,v154,v53\n"
"PF v52,v152,v152,v52\n"
"PM v150,v150,v151\n"
"AW a190,v156\n"
"AW a189,v154\n"
"PM v158,v158,v159\n"
"AW a188,v152\n"
"PF v51,v150,v150,v51\n"
"PF v55,v158,v158,v55\n"
"AW a187,v150\n"
"AW a191,v158\n"
"GA v128\n"
"GA v129\n"
"v_mfma_f32_16x16x32_f16 v[48:51], v[48:51], v[56:59], 0\n"
"CH v134,v134,v135\n"
"GL v128\n"
"GH v129\n"
"v_mfma_f32_16x16x32_f16 v[52:55], v[52:55], v[56:59], 0\n"
"CH v142,v142,v143\n"
"GA v130\n"
"MZ v[144:159],a[232:235],v[36:39]\n"
"LD a[232:235],v1,61440\n"
"GA v131\n"
"PF v135,v134,v134,s56\n"
"PF v143,v142,v142,s56\n"
"GA v132\n"
"GA v133\n"
"PM v135,v135,v134\n"
"PM v143,v143,v142\n"
"GA v136\n"
"v_pk_add_f32 v[50:51], v[48:49], v[50:51]\n"
"v_pk_add_f32 v[54:55], v[52:53], v[54:55]\n"
"GA v137\n"
"v_add_f32 v51, v50, v51\n"
"v_add_f32 v52, v54, v55\n"
"MB v[144:159],a[236:239],v[44:47]\n"
"GL v130\n"
"GH v131\n"
"GL v132\n"
"GH v133\n"
"GL v136\n"
"GH v137\n"
"LD a[236:239],v1,62464\n"
"v_permlane16_swap_b32_e64 v51, v52\n"
"EX v135,0\n"
"v_add_f32_e64 v52, v51, v52\n"
"GA v138\n"
"GA v139\n"
"v_permlane32_swap_b32 v62, v52\n"
"v_add_f32 v52, v62, v52\n"
"EX v143,0\n"
"v_add_f32_e64 v63, v63, v52\n"
"GA v140\n"
"GA v141\n"
"MB v[144:159],a[240:243],v[84:87]\n"
"GL v138\n"
"GH v139\n"
"GL v140\n"
"GH v141\n"
"LD a[240:243],v1,63488\n"
"EX v135,1\n"
"EX v143,1\n"
"PF v135,v135,s57,s57\n"
"MB v[144:159],a[244:247],v[92:95]\n"
"LD a[244:247],v1,64512\n"
"PF v143,v143,s57,s57\n"
"RC v135,0\n"
"WL 9\n"
"v_or_b32 v18, v132, v133\n"
"v_or_b32_e64 v17, v130, v131\n"
"RC v143,0\n"
"v_or_b32_e64 v16, v128, v129\n"
"MB v[144:159],a[248:251],v[116:119]\n"
"LD a[248:251],v2,0\n"
"PM v50,v18,v18\n"
"PM v49,v17,v17\n"
"PM v48,v16,v16\n"
"RC v135,1\n"
"RC v143,1\n"
"MB v[144:159],a[252:255],v[124:127]\n"
"LD a[252:255],v2,1024\n"
"WL 4\n"
"v_or_b32 v22, v140, v141\n"
"v_or_b32 v21, v138, v139\n"
"v_or_b32 v20, v136, v137\n"
"PM v54,v22,v22\n"
"PM v53,v21,v21\n"
"PM v52,v20,v20\n"
"PM v19,v134,v135\n"
"PM v23,v142,v143\n"
"PM v51,v19,v19\n"
"PM v55,v23,v23\n"
"GA v144\n"
"GA v145\n"
"CH v150,v150,v151\n"
"CH v158,v158,v159\n"
"GA v146\n"
"MZ v[128:143],a[232:235],v[32:35]\n"
"GL v144\n"
"GH v145\n"
"GA v147\n"
"PF v151,v150,v150,s56\n"
"PF v159,v158,v158,s56\n"
"GA v148\n"
"GA v149\n"
"PM v151,v151,v150\n"
"PM v159,v159,v158\n"
"GA v152\n"
"GA v153\n"
"MB v[128:143],a[236:239],v[40:43]\n"
"GL v146\n"
"GH v147\n"
"GL v148\n"
"GH v149\n"
"GL v152\n"
"GH v153\n"
"EX v151,0\n"
"GA v154\n"
"GA v155\n"
"EX v159,0\n"
"GA v156\n"
"GA v157\n"
"WL 8\n"
"s_nop 0\n"
"MB v[128:143],a[240:243],v[80:83]\n"
"GL v154\n"
"GH v155\n"
"GL v156\n"
"GH v157\n"
"EX v151,1\n"
"EX v159,1\n"
"PF v151,v151,s57,s57\n"
"MB v[128:143],a[244:247],v[88:91]\n"
"PF v159,v159,s57,s57\n"
"RC v151,0\n"
"WL 6\n"
"v_or_b32 v26, v148, v149\n"
"v_or_b32_e64 v25, v146, v147\n"
"RC v159,0\n"
"v_or_b32_e64 v24, v144, v145\n"
"MB v[128:143],a[248:251],v[112:115]\n"
"PF v50,v26,v26,v50\n"
"PF v49,v25,v25,v49\n"
"PF v48,v24,v24,v48\n"
"RC v151,1\n"
"RC v159,1\n"
"MB v[128:143],a[252:255],v[120:123]\n"
"WL 0\n"
"v_or_b32 v30, v156, v157\n"
"v_or_b32 v29, v154, v155\n"
"v_or_b32 v28, v152, v153\n"
"PF v54,v30,v30,v54\n"
"PF v53,v29,v29,v53\n"
"PF v52,v28,v28,v52\n"
"PM v27,v150,v151\n"
"PM v31,v158,v159\n"
"PF v51,v27,v27,v51\n"
"PF v55,v31,v31,v55\n"
"v_mov_b32_e64 v62, 0\n"
"GA v128\n"
"GA v129\n"
"v_mfma_f32_16x16x32_f16 v[48:51], v[48:51], v[56:59], 0\n"
"CH v134,v134,v135\n"
"GL v128\n"
"GH v129\n"
"v_mfma_f32_16x16x32_f16 v[52:55], v[52:55], v[56:59], 0\n"
"CH v142,v142,v143\n"
"GA v130\n"
"MZ v[144:159],a[232:235],v[36:39]\n"
"LD a[232:235],v2,2048\n"
"GA v131\n"
"PF v135,v134,v134,s56\n"
"PF v143,v142,v142,s56\n"
"GA v132\n"
"GA v133\n"
"PM v135,v135,v134\n"
"PM v143,v143,v142\n"
"GA v136\n"
"v_pk_add_f32 v[50:51], v[48:49], v[50:51]\n"
"v_pk_add_f32 v[54:55], v[52:53], v[54:55]\n"
"GA v137\n"
"v_add_f32 v51, v50, v51\n"
"v_add_f32 v52, v54, v55\n"
"MB v[144:159],a[236:239],v[44:47]\n"
"GL v130\n"
"GH v131\n"
"GL v132\n"
"GH v133\n"
"GL v136\n"
"GH v137\n"
"LD a[236:239],v2,3072\n"
"v_permlane16_swap_b32_e64 v51, v52\n"
"EX v135,0\n"
"v_add_f32_e64 v52, v51, v52\n"
"GA v138\n"
"GA v139\n"
"v_permlane32_swap_b32 v62, v52\n"
"v_add_f32 v52, v62, v52\n"
"EX v143,0\n"
"v_add_f32_e64 v7, v7, v52\n"
"GA v140\n"
"GA v141\n"
"MB v[144:159],a[240:243],v[84:87]\n"
"GL v138\n"
"GH v139\n"
"GL v140\n"
"GH v141\n"
"LD a[240:243],v2,4096\n"
"EX v135,1\n"
"EX v143,1\n"
"PF v135,v135,s57,s57\n"
"MB v[144:159],a[244:247],v[92:95]\n"
"LD a[244:247],v2,5120\n"
"PF v143,v143,s57,s57\n"
"RC v135,0\n"
"WL 9\n"
"v_or_b32 v66, v132, v133\n"
"v_or_b32_e64 v65, v130, v131\n"
"RC v143,0\n"
"v_or_b32_e64 v64, v128, v129\n"
"MB v[144:159],a[248:251],v[116:119]\n"
"LD a[248:251],v2,6144\n"
"PM v50,v66,v66\n"
"PM v49,v65,v65\n"
"PM v48,v64,v64\n"
"RC v135,1\n"
"RC v143,1\n"
"MB v[144:159],a[252:255],v[124:127]\n"
"LD a[252:255],v2,7168\n"
"WL 4\n"
"v_or_b32 v70, v140, v141\n"
"v_or_b32 v69, v138, v139\n"
"v_or_b32 v68, v136, v137\n"
"PM v54,v70,v70\n"
"PM v53,v69,v69\n"
"PM v52,v68,v68\n"
"PM v67,v134,v135\n"
"PM v71,v142,v143\n"
"PM v51,v67,v67\n"
"PM v55,v71,v71\n"
"GA v144\n"
"GA v145\n"
"CH v150,v150,v151\n"
"CH v158,v158,v159\n"
"GA v146\n"
"MZ v[128:143],a[232:235],v[32:35]\n"
"GL v144\n"
"GH v145\n"
"GA v147\n"
"PF v151,v150,v150,s56\n"
"PF v159,v158,v158,s56\n"
"GA v148\n"
"GA v149\n"
"PM v151,v151,v150\n"
"PM v159,v159,v158\n"
"GA v152\n"
"GA v153\n"
"MB v[128:143],a[236:239],v[40:43]\n"
"GL v146\n"
"GH v147\n"
"GL v148\n"
"GH v149\n"
"GL v152\n"
"GH v153\n"
"EX v151,0\n"
"GA v154\n"
"GA v155\n"
"EX v159,0\n"
"GA v156\n"
"GA v157\n"
"WL 8\n"
"s_nop 0\n"
"MB v[128:143],a[240:243],v[80:83]\n"
"GL v154\n"
"GH v155\n"
"GL v156\n"
"GH v157\n"
"EX v151,1\n"
"EX v159,1\n"
"PF v151,v151,s57,s57\n"
"MB v[128:143],a[244:247],v[88:91]\n"
"PF v159,v159,s57,s57\n"
"RC v151,0\n"
"WL 6\n"
"v_or_b32 v74, v148, v149\n"
"v_or_b32_e64 v73, v146, v147\n"
"RC v159,0\n"
"v_or_b32_e64 v72, v144, v145\n"
"MB v[128:143],a[248:251],v[112:115]\n"
"PF v50,v74,v74,v50\n"
"PF v49,v73,v73,v49\n"
"PF v48,v72,v72,v48\n"
"RC v151,1\n"
"RC v159,1\n"
"MB v[128:143],a[252:255],v[120:123]\n"
"WL 0\n"
"v_or_b32 v78, v156, v157\n"
"v_or_b32_e64 v77, v154, v155\n"
"v_or_b32 v76, v152, v153\n"
"PF v54,v78,v78,v54\n"
"PF v53,v77,v77,v53\n"
"PF v52,v76,v76,v52\n"
"PM v75,v150,v151\n"
"PM v79,v158,v159\n"
"PF v51,v75,v75,v51\n"
"PF v55,v79,v79,v55\n"
"s_nop 0\n"
"GA v128\n"
"GA v129\n"
"v_mfma_f32_16x16x32_f16 v[48:51], v[48:51], v[56:59], 0\n"
"CH v134,v134,v135\n"
"GL v128\n"
"GH v129\n"
"v_mfma_f32_16x16x32_f16 v[52:55], v[52:55], v[56:59], 0\n"
"CH v142,v142,v143\n"
"GA v130\n"
"MZ v[144:159],a[232:235],v[36:39]\n"
"GA v131\n"
"PF v135,v134,v134,s56\n"
"PF v143,v142,v142,s56\n"
"GA v132\n"
"GA v133\n"
"PM v135,v135,v134\n"
"PM v143,v143,v142\n"
"GA v136\n"
"GA v137\n"
"v_pk_add_f32 v[50:51], v[48:49], v[50:51]\n"
"v_pk_add_f32 v[54:55], v[52:53], v[54:55]\n"
"v_add_f32 v51, v50, v51\n"
"v_add_f32 v52, v54, v55\n"
"MB v[144:159],a[236:239],v[44:47]\n"
"GL v130\n"
"GH v131\n"
"GL v132\n"
"GH v133\n"
"GL v136\n"
"GH v137\n"
"v_permlane16_swap_b32_e64 v51, v52\n"
"EX v135,0\n"
"v_add_f32_e64 v62, v51, v52\n"
"GA v138\n"
"GA v139\n"
"EX v143,0\n"
"GA v140\n"
"GA v141\n"
"MB v[144:159],a[240:243],v[84:87]\n"
"GL v138\n"
"GH v139\n"
"GL v140\n"
"GH v141\n"
"EX v135,1\n"
"EX v143,1\n"
"PF v135,v135,s57,s57\n"
"MB v[144:159],a[244:247],v[92:95]\n"
"PF v143,v143,s57,s57\n"
"RC v135,0\n"
"WL 6\n"
"v_or_b32 v98, v132, v133\n"
"v_or_b32_e64 v97, v130, v131\n"
"RC v143,0\n"
"v_or_b32_e64 v96, v128, v129\n"
"MB v[144:159],a[248:251],v[116:119]\n"
"PM v50,v98,v98\n"
"PM v49,v97,v97\n"
"PM v48,v96,v96\n"
"RC v135,1\n"
"RC v143,1\n"
"MB v[144:159],a[252:255],v[124:127]\n"
"v_lshrrev_b32 v112, 4, v0\n"
"v_lshrrev_b32 v113, 5, v112\n"
"v_lshlrev_b32_e64 v113, 8, v113\n"
"v_bfe_u32 v114, v112, 2, 2\n"
"v_lshl_add_u32 v113, v114, 6, v113\n"
"v_bfe_u32 v114, v112, 4, 1\n"
"v_lshl_add_u32 v113, v114, 5, v113\n"
"v_and_b32_e64 v114, 1, v112\n"
"v_lshl_add_u32 v113, v114, 4, v113\n"
"v_bfe_u32 v114, v112, 1, 1\n"
"v_lshl_add_u32 v113, v114, 3, v113\n"
"v_add_u32 v114, s64, v113\n"
"v_add_u32 v113, s63, v113\n"
"v_lshl_add_u32 v115, v112, 2, s63\n"
"WL 0\n"
"v_or_b32 v102, v140, v141\n"
"v_or_b32 v101, v138, v139\n"
"v_or_b32 v100, v136, v137\n"
"PM v54,v102,v102\n"
"PM v53,v101,v101\n"
"PM v52,v100,v100\n"
"PM v99,v134,v135\n"
"PM v103,v142,v143\n"
"PM v51,v99,v99\n"
"PM v55,v103,v103\n"
"GA v144\n"
"GA v145\n"
"GL v144\n"
"GH v145\n"
"CH v150,v150,v151\n"
"CH v158,v158,v159\n"
"GA v146\n"
"GA v147\n"
"GL v146\n"
"GH v147\n"
"PF v151,v150,v150,s56\n"
"PF v159,v158,v158,s56\n"
"GA v148\n"
"GA v149\n"
"GL v148\n"
"GH v149\n"
"PM v151,v151,v150\n"
"PM v159,v159,v158\n"
"GA v152\n"
"GA v153\n"
"GL v152\n"
"GH v153\n"
"EX v151,0\n"
"GA v154\n"
"GA v155\n"
"GL v154\n"
"GH v155\n"
"EX v159,0\n"
"GA v156\n"
"GA v157\n"
"GL v156\n"
"GH v157\n"
"EX v151,1\n"
"s_waitcnt vmcnt(0)\n"
"s_nop 0\n"
"ds_read_b64_tr_b16 v[32:33], v113\n"
"ds_read_b64_tr_b16 v[34:35], v113 offset:512\n"
"ds_read_b64_tr_b16 v[36:37], v113 offset:1024\n"
"ds_read_b64_tr_b16 v[38:39], v113 offset:1536\n"
"ds_write_b32 v115, v3\n"
"ds_write_b32 v115, v4 offset:256\n"
"ds_write_b32 v115, v5 offset:512\n"
"ds_write_b32 v115, v6 offset:768\n"
"ds_write_b32 v115, v7 offset:1024\n"
"EX v159,1\n"
"PF v151,v151,s57,s57\n"
"PF v159,v159,s57,s57\n"
"RC v151,0\n"
"WL 14\n"
"v_or_b32 v106, v148, v149\n"
"v_or_b32_e64 v105, v146, v147\n"
"RC v159,0\n"
"v_or_b32_e64 v104, v144, v145\n"
"PF v50,v106,v106,v50\n"
"PF v49,v105,v105,v49\n"
"PF v48,v104,v104,v48\n"
"RC v151,1\n"
"RC v159,1\n"
"WL 9\n"
"v_or_b32 v110, v156, v157\n"
"v_or_b32 v109, v154, v155\n"
"v_or_b32 v108, v152, v153\n"
"PF v54,v110,v110,v54\n"
"PF v53,v109,v109,v53\n"
"PF v52,v108,v108,v52\n"
"PM v107,v150,v151\n"
"PM v111,v158,v159\n"
"PF v51,v107,v107,v51\n"
"PF v55,v111,v111,v55\n"
"v_mov_b32 v118, 0xc0088e99\n"
"v_mfma_f32_16x16x32_f16 v[48:51], v[48:51], v[56:59], 0\n"
"v_lshrrev_b32 v117, 2, v112\n"
"v_lshlrev_b32 v117, 14, v117\n"
"v_mfma_f32_16x16x32_f16 v[52:55], v[52:55], v[56:59], 0\n"
"s_sub_u32 s67, 0, s86\n"
"v_ldexp_f32 v118, v118, s67\n"
"s_lshl_b32 s66, s58, 9\n"
"s_add_u32 s66, s66, 8192\n"
"v_lshl_add_u32 v116, v112, 3, s66\n"
"v_readfirstlane_b32 s71, v118\n"
"v_pk_add_f32 v[50:51], v[48:49], v[50:51]\n"
"v_add_f32 v51, v50, v51\n"
"v_pk_add_f32 v[54:55], v[52:53], v[54:55]\n"
"v_add_f32_e64 v52, v54, v55\n"
"v_and_or_b32 v117, v0, 48, v117\n"
"v_mov_b32 v121, s60\n"
"v_permlane16_swap_b32 v51, v52\n"
"v_add_f32 v52, v51, v52\n"
"s_lshl_b32 s66, s58, 18\n"
"s_lshl_b32 s67, s59, 6\n"
"v_permlane32_swap_b32 v62, v52\n"
"s_add_u32 s66, s66, s67\n"
"v_add_f32 v52, v62, v52\n"
"s_add_u32 s66, s66, 131072\n"
"v_add_f32 v63, v63, v52\n"
"v_mov_b32 v123, s60\n"
"ds_write_b32 v115, v63 offset:1280\n"
"v_mov_b32_e64 v7, v114\n"
"s_cmp_eq_u32 s58, 3\n"
"WL 0\n"
"s_barrier\n"
"s_cbranch_scc1 Lnp1_%=\n"
"ds_read_b64 v[54:55], v116\n"
"ds_read_b64 v[56:57], v116 offset:18432\n"
"ds_read_b64 v[58:59], v116 offset:36864\n"
"ds_read_b64 v[60:61], v116 offset:55296\n"
"WL 2\n"
"v_pk_add_f32 v[54:55], v[54:55], v[56:57]\n"
"WL 0\n"
"v_pk_add_f32 v[58:59], v[58:59], v[60:61]\n"
"v_add_f32_e64 v120, v54, v58\n"
"v_add_f32 v122, v55, v59\n"
"buffer_store_dwordx4 v[120:123], v117, s[48:51], s66 offen sc1\n"
"s_branch Lnj2_%=\n"
"Lnp1_%=:\n"
"s_cmp_ge_u32 s30, 48\n"
"s_cbranch_scc1 Lnj2_%=\n"
"s_lshl_b32 s72, s30, 14\n"
"s_add_u32 s72, s72, 131072\n"
"s_add_u32 s73, s72, 4096\n"
"s_add_u32 s74, s72, 8192\n"
"s_add_u32 s75, s72, 12288\n"
"s_mov_b32 s70, 0\n"
"Lh13_%=:\n"
"buffer_load_dwordx4 v[40:43], v0, s[48:51], s72 offen offset:0 sc1\n"
"buffer_load_dwordx4 v[44:47], v0, s[48:51], s72 offen offset:1024 sc1\n"
"buffer_load_dwordx4 v[80:83], v0, s[48:51], s72 offen offset:2048 sc1\n"
"buffer_load_dwordx4 v[84:87], v0, s[48:51], s72 offen offset:3072 sc1\n"
"buffer_load_dwordx4 v[88:91], v0, s[48:51], s73 offen offset:0 sc1\n"
"buffer_load_dwordx4 v[92:95], v0, s[48:51], s73 offen offset:1024 sc1\n"
"buffer_load_dwordx4 v[112:115], v0, s[48:51], s73 offen offset:2048 sc1\n"
"buffer_load_dwordx4 v[116:119], v0, s[48:51], s73 offen offset:3072 sc1\n"
"buffer_load_dwordx4 v[120:123], v0, s[48:51], s74 offen offset:0 sc1\n"
"buffer_load_dwordx4 v[124:127], v0, s[48:51], s74 offen offset:1024 sc1\n"
"buffer_load_dwordx4 v[128:131], v0, s[48:51], s74 offen offset:2048 sc1\n"
"buffer_load_dwordx4 v[132:135], v0, s[48:51], s74 offen offset:3072 sc1\n"
"buffer_load_dwordx4 v[136:139], v0, s[48:51], s75 offen offset:0 sc1\n"
"buffer_load_dwordx4 v[140:143], v0, s[48:51], s75 offen offset:1024 sc1\n"
"buffer_load_dwordx4 v[144:147], v0, s[48:51], s75 offen offset:2048 sc1\n"
"buffer_load_dwordx4 v[148:151], v0, s[48:51], s75 offen offset:3072 sc1\n"
"s_waitcnt vmcnt(15)\n"
"v_min3_u32 v48, v41, v43, s60\n"
"v_max3_u32 v49, v41, v43, s60\n"
"s_waitcnt vmcnt(14)\n"
"v_min3_u32 v48, v45, v47, v48\n"
"v_max3_u32 v49, v45, v47, v49\n"
"s_waitcnt vmcnt(13)\n"
"v_min3_u32 v48, v81, v83, v48\n"
"v_max3_u32 v49, v81, v83, v49\n"
"s_waitcnt vmcnt(12)\n"
"v_min3_u32 v48, v85, v87, v48\n"
"v_max3_u32 v49, v85, v87, v49\n"
"s_waitcnt vmcnt(11)\n"
"v_min3_u32 v48, v89, v91, v48\n"
"v_max3_u32 v49, v89, v91, v49\n"
"s_waitcnt vmcnt(10)\n"
"v_min3_u32 v48, v93, v95, v48\n"
"v_max3_u32 v49, v93, v95, v49\n"
"s_waitcnt vmcnt(9)\n"
"v_min3_u32 v48, v113, v115, v48\n"
"v_max3_u32 v49, v113, v115, v49\n"
"s_waitcnt vmcnt(8)\n"
"v_min3_u32 v48, v117, v119, v48\n"
"v_max3_u32 v49, v117, v119, v49\n"
"s_waitcnt vmcnt(7)\n"
"v_min3_u32 v48, v121, v123, v48\n"
"v_max3_u32 v49, v121, v123, v49\n"
"s_waitcnt vmcnt(6)\n"
"v_min3_u32 v48, v125, v127, v48\n"
"v_max3_u32 v49, v125, v127, v49\n"
"s_waitcnt vmcnt(5)\n"
"v_min3_u32 v48, v129, v131, v48\n"
"v_max3_u32 v49, v129, v131, v49\n"
"s_waitcnt vmcnt(4)\n"
"v_min3_u32 v48, v133, v135, v48\n"
"v_max3_u32 v49, v133, v135, v49\n"
"s_waitcnt vmcnt(3)\n"
"v_min3_u32 v48, v137, v139, v48\n"
"v_max3_u32 v49, v137, v139, v49\n"
"s_waitcnt vmcnt(2)\n"
"v_min3_u32 v48, v141, v143, v48\n"
"v_max3_u32 v49, v141, v143, v49\n"
"s_waitcnt vmcnt(1)\n"
"v_min3_u32 v48, v145, v147, v48\n"
"v_max3_u32 v49, v145, v147, v49\n"
"s_waitcnt vmcnt(0)\n"
"v_min3_u32 v48, v149, v151, v48\n"
"v_max3_u32 v49, v149, v151, v49\n"
"v_cmp_eq_u32 vcc, v48, v49\n"
"s_add_u32 s70, s70, 1\n"
"s_cmp_eq_u64 vcc, -1\n"
"s_cbranch_scc1 Lh1x4_%=\n"
"s_cmp_lt_u32 s70, 0x40000\n"
"s_cbranch_scc1 Lh13_%=\n"
"Lh1x4_%=:\n"
"v_add_f32 v40, v40, v44\n"
"v_add_f32 v42, v42, v46\n"
"v_add_f32 v80, v80, v84\n"
"v_add_f32 v82, v82, v86\n"
"v_add_f32 v88, v88, v92\n"
"v_add_f32 v90, v90, v94\n"
"v_add_f32 v112, v112, v116\n"
"v_add_f32 v114, v114, v118\n"
"v_add_f32 v120, v120, v124\n"
"v_add_f32 v122, v122, v126\n"
"v_add_f32 v128, v128, v132\n"
"v_add_f32 v130, v130, v134\n"
"v_add_f32 v136, v136, v140\n"
"v_add_f32 v138, v138, v142\n"
"v_add_f32 v144, v144, v148\n"
"v_add_f32 v146, v146, v150\n"
"v_add_f32 v40, v40, v80\n"
"v_add_f32 v42, v42, v82\n"
"v_add_f32 v88, v88, v112\n"
"v_add_f32 v90, v90, v114\n"
"v_add_f32 v120, v120, v128\n"
"v_add_f32 v122, v122, v130\n"
"v_add_f32 v136, v136, v144\n"
"v_add_f32 v138, v138, v146\n"
"v_add_f32 v40, v40, v88\n"
"v_add_f32 v42, v42, v90\n"
"v_add_f32 v120, v120, v136\n"
"v_add_f32 v122, v122, v138\n"
"v_add_f32 v40, v40, v120\n"
"v_add_f32_e64 v42, v42, v122\n"
"v_mul_f32 v54, s71, s71\n"
"v_add_f32_dpp v40, v40, v40 row_ror:4 row_mask:0xf bank_mask:0xf\n"
"v_add_f32_dpp v42, v42, v42 row_ror:4 row_mask:0xf bank_mask:0xf\n"
"v_mov_b32 v55, 0x358637bd\n"
"v_add_f32_dpp v40, v40, v40 row_ror:8 row_mask:0xf bank_mask:0xf\n"
"v_add_f32_dpp v42, v42, v42 row_ror:8 row_mask:0xf bank_mask:0xf\n"
"v_lshrrev_b32_e64 v5, 5, v0\n"
"v_and_or_b32 v5, v0, 48, v5\n"
"v_permlane16_swap_b32 v40, v42\n"
"v_add_f32 v40, v40, v42\n"
"v_and_b32_e64 v5, 56, v5\n"
"v_mov_b32 v42, v40\n"
"s_lshl_b32 s66, s30, 6\n"
"s_add_u32 s66, s66, 4096\n"
"v_permlane32_swap_b32 v40, v42\n"
"v_add_f32 v40, v40, v42\n"
"v_fma_f32 v40, v40, v54, v55\n"
"v_sqrt_f32_e64 v40, v40\n"
"s_mov_b64 exec, 0x000f000f\n"
"buffer_store_dwordx2 v[40:41], v5, s[48:51], s66 offen sc1\n"
"s_mov_b64 exec, -1\n"
"Lnj2_%=:\n"
"s_nop 1\n"
"v_lshrrev_b32 v5, 4, v0\n"
"v_lshrrev_b32 v6, 2, v0\n"
"v_add_u32 v6, 72960, v6\n"
"ds_read_b32 v153, v6 offset:0\n"
"ds_read_b32 v155, v6 offset:256\n"
"ds_read_b32 v157, v6 offset:512\n"
"v_lshrrev_b32 v3, 5, v5\n"
"v_lshlrev_b32 v158, 4, v3\n"
"s_mul_i32 s66, s58, 192\n"
"s_add_u32 s67, s66, 71680\n"
"v_add_u32_e64 v158, s67, v158\n"
"v_lshlrev_b32 v3, 6, v3\n"
"s_lshl_b32 s66, s66, 1\n"
"s_add_u32 s66, s66, 6144\n"
"v_add_u32_e64 v6, s66, v3\n"
"v_add_u32 v3, 72448, v3\n"
"LD a[232:235],v158,0\n"
"LD a[236:239],v158,32\n"
"LD a[240:243],v158,64\n"
"LD a[244:247],v158,96\n"
"LD a[248:251],v158,128\n"
"LD a[252:255],v158,160\n"
"ds_read_b64_tr_b16 v[88:89], v7 offset:6144\n"
"ds_read_b64_tr_b16 v[90:91], v7 offset:6656\n"
"ds_read_b64_tr_b16 v[92:93], v7 offset:7168\n"
"ds_read_b64_tr_b16 v[94:95], v7 offset:7680\n"
"ds_read_b64_tr_b16 v[128:129], v7 offset:12288\n"
"ds_read_b64_tr_b16 v[130:131], v7 offset:12800\n"
"ds_read_b64_tr_b16 v[132:133], v7 offset:13312\n"
"ds_read_b64_tr_b16 v[134:135], v7 offset:13824\n"
"ds_read_b64_tr_b16 v[40:41], v7 offset:2048\n"
"ds_read_b64_tr_b16 v[42:43], v7 offset:2560\n"
"ds_read_b64_tr_b16 v[44:45], v7 offset:3072\n"
"ds_read_b64_tr_b16 v[46:47], v7 offset:3584\n"
"ds_read_b64_tr_b16 v[112:113], v7 offset:8192\n"
"ds_read_b64_tr_b16 v[114:115], v7 offset:8704\n"
"ds_read_b64_tr_b16 v[116:117], v7 offset:9216\n"
"ds_read_b64_tr_b16 v[118:119], v7 offset:9728\n"
"ds_read_b64_tr_b16 v[136:137], v7 offset:14336\n"
"ds_read_b64_tr_b16 v[138:139], v7 offset:14848\n"
"ds_read_b64_tr_b16 v[140:141], v7 offset:15360\n"
"ds_read_b64_tr_b16 v[142:143], v7 offset:15872\n"
"ds_read_b64_tr_b16 v[80:81], v7 offset:4096\n"
"ds_read_b64_tr_b16 v[82:83], v7 offset:4608\n"
"ds_read_b64_tr_b16 v[84:85], v7 offset:5120\n"
"ds_read_b64_tr_b16 v[86:87], v7 offset:5632\n"
"ds_read_b64_tr_b16 v[120:121], v7 offset:10240\n"
"ds_read_b64_tr_b16 v[122:123], v7 offset:10752\n"
"ds_read_b64_tr_b16 v[124:125], v7 offset:11264\n"
"ds_read_b64_tr_b16 v[126:127], v7 offset:11776\n"
"ds_read_b64_tr_b16 v[144:145], v7 offset:16384\n"
"ds_read_b64_tr_b16 v[146:147], v7 offset:16896\n"
"ds_read_b64_tr_b16 v[148:149], v7 offset:17408\n"
"ds_read_b64_tr_b16 v[150:151], v7 offset:17920\n"
"s_mov_b32 s68, 0xffff0000\n"
"WL 14\n"
"MZ v[48:63],v[32:35],a[232:235]\n"
"v_lshlrev_b32 v5, 16, v32\n"
"v_lshlrev_b32 v159, 16, v33\n"
"v_and_b32 v32, s68, v32\n"
"v_and_b32_e64 v33, s68, v33\n"
"CH v32,v5,v32\n"
"CH v33,v159,v33\n"
"v_lshlrev_b32 v5, 16, v34\n"
"v_lshlrev_b32 v159, 16, v35\n"
"v_and_b32 v34, s68, v34\n"
"v_and_b32 v35, s68, v35\n"
"CH v34,v5,v34\n"
"CH v35,v159,v35\n"
"MB v[48:63],v[36:39],a[236:239]\n"
"v_lshlrev_b32 v5, 16, v36\n"
"v_lshlrev_b32 v159, 16, v37\n"
"v_and_b32 v36, s68, v36\n"
"v_and_b32 v37, s68, v37\n"
"CH v36,v5,v36\n"
"CH v37,v159,v37\n"
"v_lshlrev_b32 v5, 16, v38\n"
"v_lshlrev_b32 v159, 16, v39\n"
"v_and_b32 v38, s68, v38\n"
"v_and_b32 v39, s68, v39\n"
"CH v38,v5,v38\n"
"CH v39,v159,v39\n"
"MB v[48:63],v[88:91],a[240:243]\n"
"v_lshlrev_b32 v5, 16, v88\n"
"v_lshlrev_b32 v159, 16, v89\n"
"v_and_b32 v88, s68, v88\n"
"v_and_b32 v89, s68, v89\n"
"CH v88,v5,v88\n"
"CH v89,v159,v89\n"
"v_lshlrev_b32 v5, 16, v90\n"
"v_lshlrev_b32 v159, 16, v91\n"
"v_and_b32 v90, s68, v90\n"
"v_and_b32 v91, s68, v91\n"
"CH v90,v5,v90\n"
"CH v91,v159,v91\n"
"MB v[48:63],v[92:95],a[244:247]\n"
"v_lshlrev_b32 v5, 16, v92\n"
"v_lshlrev_b32 v159, 16, v93\n"
"v_and_b32 v92, s68, v92\n"
"v_and_b32 v93, s68, v93\n"
"CH v92,v5,v92\n"
"CH v93,v159,v93\n"
"v_lshlrev_b32 v5, 16, v94\n"
"v_lshlrev_b32 v159, 16, v95\n"
"v_and_b32 v94, s68, v94\n"
"v_and_b32 v95, s68, v95\n"
"CH v94,v5,v94\n"
"CH v95,v159,v95\n"
"MB v[48:63],v[128:131],a[248:251]\n"
"v_lshlrev_b32 v5, 16, v128\n"
"v_lshlrev_b32 v159, 16, v129\n"
"v_and_b32 v128, s68, v128\n"
"v_and_b32 v129, s68, v129\n"
"CH v128,v5,v128\n"
"CH v129,v159,v129\n"
"v_lshlrev_b32 v5, 16, v130\n"
"v_lshlrev_b32 v159, 16, v131\n"
"v_and_b32 v130, s68, v130\n"
"v_and_b32 v131, s68, v131\n"
"CH v130,v5,v130\n"
"CH v131,v159,v131\n"
"MB v[48:63],v[132:135],a[252:255]\n"
"v_lshlrev_b32 v5, 16, v132\n"
"v_lshlrev_b32 v159, 16, v133\n"
"v_and_b32 v132, s68, v132\n"
"v_and_b32 v133, s68, v133\n"
"CH v132,v5,v132\n"
"CH v133,v159,v133\n"
"v_lshlrev_b32 v5, 16, v134\n"
"v_lshlrev_b32 v159, 16, v135\n"
"v_and_b32 v134, s68, v134\n"
"v_and_b32 v135, s68, v135\n"
"CH v134,v5,v134\n"
"CH v135,v159,v135\n"
"s_mov_b32 exec_lo, 1\n"
"s_mov_b32 exec_hi, 1\n"
"ds_write_b128 v6, v[48:51] offset:0\n"
"ds_write_b128 v6, v[52:55] offset:16\n"
"ds_write_b128 v6, v[56:59] offset:32\n"
"ds_write_b128 v6, v[60:63] offset:48\n"
"s_mov_b64 exec, -1\n"
"MZ v[48:63],v[40:43],a[232:235]\n"
"v_lshlrev_b32 v5, 16, v40\n"
"v_lshlrev_b32 v159, 16, v41\n"
"v_and_b32 v40, s68, v40\n"
"v_and_b32_e64 v41, s68, v41\n"
"CH v40,v5,v40\n"
"CH v41,v159,v41\n"
"v_lshlrev_b32 v5, 16, v42\n"
"v_lshlrev_b32 v159, 16, v43\n"
"v_and_b32 v42, s68, v42\n"
"v_and_b32 v43, s68, v43\n"
"CH v42,v5,v42\n"
"CH v43,v159,v43\n"
"MB v[48:63],v[44:47],a[236:239]\n"
"v_lshlrev_b32 v5, 16, v44\n"
"v_lshlrev_b32 v159, 16, v45\n"
"v_and_b32 v44, s68, v44\n"
"v_and_b32 v45, s68, v45\n"
"CH v44,v5,v44\n"
"CH v45,v159,v45\n"
"v_lshlrev_b32 v5, 16, v46\n"
"v_lshlrev_b32 v159, 16, v47\n"
"v_and_b32 v46, s68, v46\n"
"v_and_b32 v47, s68, v47\n"
"CH v46,v5,v46\n"
"CH v47,v159,v47\n"
"MB v[48:63],v[112:115],a[240:243]\n"
"v_lshlrev_b32 v5, 16, v112\n"
"v_lshlrev_b32 v159, 16, v113\n"
"v_and_b32 v112, s68, v112\n"
"v_and_b32 v113, s68, v113\n"
"CH v112,v5,v112\n"
"CH v113,v159,v113\n"
"v_lshlrev_b32 v5, 16, v114\n"
"v_lshlrev_b32 v159, 16, v115\n"
"v_and_b32 v114, s68, v114\n"
"v_and_b32 v115, s68, v115\n"
"CH v114,v5,v114\n"
"CH v115,v159,v115\n"
"MB v[48:63],v[116:119],a[244:247]\n"
"v_lshlrev_b32 v5, 16, v116\n"
"v_lshlrev_b32 v159, 16, v117\n"
"v_and_b32 v116, s68, v116\n"
"v_and_b32 v117, s68, v117\n"
"CH v116,v5,v116\n"
"CH v117,v159,v117\n"
"v_lshlrev_b32 v5, 16, v118\n"
"v_lshlrev_b32 v159, 16, v119\n"
"v_and_b32 v118, s68, v118\n"
"v_and_b32 v119, s68, v119\n"
"CH v118,v5,v118\n"
"CH v119,v159,v119\n"
"MB v[48:63],v[136:139],a[248:251]\n"
"v_lshlrev_b32 v5, 16, v136\n"
"v_lshlrev_b32 v159, 16, v137\n"
"v_and_b32 v136, s68, v136\n"
"v_and_b32 v137, s68, v137\n"
"CH v136,v5,v136\n"
"CH v137,v159,v137\n"
"v_lshlrev_b32 v5, 16, v138\n"
"v_lshlrev_b32 v159, 16, v139\n"
"v_and_b32 v138, s68, v138\n"
"v_and_b32 v139, s68, v139\n"
"CH v138,v5,v138\n"
"CH v139,v159,v139\n"
"WL 0\n"
"MB v[48:63],v[140:143],a[252:255]\n"
"v_lshlrev_b32 v5, 16, v140\n"
"v_lshlrev_b32 v159, 16, v141\n"
"v_and_b32 v140, s68, v140\n"
"v_and_b32_e64 v141, s68, v141\n"
"CH v140,v5,v140\n"
"CH v141,v159,v141\n"
"v_lshlrev_b32 v5, 16, v142\n"
"v_lshlrev_b32 v159, 16, v143\n"
"v_and_b32 v142, s68, v142\n"
"v_and_b32 v143, s68, v143\n"
"CH v142,v5,v142\n"
"CH v143,v159,v143\n"
"s_mov_b32 exec_lo, 1\n"
"s_mov_b32 exec_hi, 1\n"
"ds_write_b128 v6, v[48:51] offset:128\n"
"ds_write_b128 v6, v[52:55] offset:144\n"
"ds_write_b128 v6, v[56:59] offset:160\n"
"ds_write_b128 v6, v[60:63] offset:176\n"
"s_mov_b64 exec, -1\n"
"MZ v[48:63],v[80:83],a[232:235]\n"
"v_lshlrev_b32 v5, 16, v80\n"
"v_lshlrev_b32 v159, 16, v81\n"
"v_and_b32 v80, s68, v80\n"
"v_and_b32_e64 v81, s68, v81\n"
"CH v80,v5,v80\n"
"CH v81,v159,v81\n"
"v_lshlrev_b32 v5, 16, v82\n"
"v_lshlrev_b32 v159, 16, v83\n"
"v_and_b32 v82, s68, v82\n"
"v_and_b32 v83, s68, v83\n"
"CH v82,v5,v82\n"
"CH v83,v159,v83\n"
"MB v[48:63],v[84:87],a[236:239]\n"
"v_lshlrev_b32 v5, 16, v84\n"
"v_lshlrev_b32 v159, 16, v85\n"
"v_and_b32 v84, s68, v84\n"
"v_and_b32 v85, s68, v85\n"
"CH v84,v5,v84\n"
"CH v85,v159,v85\n"
"v_lshlrev_b32 v5, 16, v86\n"
"v_lshlrev_b32 v159, 16, v87\n"
"v_and_b32 v86, s68, v86\n"
"v_and_b32 v87, s68, v87\n"
"CH v86,v5,v86\n"
"CH v87,v159,v87\n"
"MB v[48:63],v[120:123],a[240:243]\n"
"v_lshlrev_b32 v5, 16, v120\n"
"v_lshlrev_b32 v159, 16, v121\n"
"v_and_b32 v120, s68, v120\n"
"v_and_b32 v121, s68, v121\n"
"CH v120,v5,v120\n"
"CH v121,v159,v121\n"
"v_lshlrev_b32 v5, 16, v122\n"
"v_lshlrev_b32 v159, 16, v123\n"
"v_and_b32 v122, s68, v122\n"
"v_and_b32 v123, s68, v123\n"
"CH v122,v5,v122\n"
"CH v123,v159,v123\n"
"MB v[48:63],v[124:127],a[244:247]\n"
"v_lshlrev_b32 v5, 16, v124\n"
"v_lshlrev_b32 v159, 16, v125\n"
"v_and_b32 v124, s68, v124\n"
"v_and_b32 v125, s68, v125\n"
"CH v124,v5,v124\n"
"CH v125,v159,v125\n"
"v_lshlrev_b32 v5, 16, v126\n"
"v_lshlrev_b32 v159, 16, v127\n"
"v_and_b32 v126, s68, v126\n"
"v_and_b32 v127, s68, v127\n"
"CH v126,v5,v126\n"
"CH v127,v159,v127\n"
"MB v[48:63],v[144:147],a[248:251]\n"
"v_lshlrev_b32 v5, 16, v144\n"
"v_lshlrev_b32 v159, 16, v145\n"
"v_and_b32 v144, s68, v144\n"
"v_and_b32 v145, s68, v145\n"
"CH v144,v5,v144\n"
"CH v145,v159,v145\n"
"v_lshlrev_b32 v5, 16, v146\n"
"v_lshlrev_b32 v159, 16, v147\n"
"v_and_b32 v146, s68, v146\n"
"v_and_b32 v147, s68, v147\n"
"CH v146,v5,v146\n"
"CH v147,v159,v147\n"
"MB v[48:63],v[148:151],a[252:255]\n"
"v_lshlrev_b32 v5, 16, v148\n"
"v_lshlrev_b32 v159, 16, v149\n"
"v_and_b32 v148, s68, v148\n"
"v_and_b32 v149, s68, v149\n"
"CH v148,v5,v148\n"
"CH v149,v159,v149\n"
"v_lshlrev_b32 v5, 16, v150\n"
"v_lshlrev_b32 v159, 16, v151\n"
"v_and_b32 v150, s68, v150\n"
"v_and_b32 v151, s68, v151\n"
"CH v150,v5,v150\n"
"CH v151,v159,v151\n"
"s_mov_b32 exec_lo, 1\n"
"s_mov_b32 exec_hi, 1\n"
"ds_write_b128 v6, v[48:51] offset:256\n"
"ds_write_b128 v6, v[52:55] offset:272\n"
"ds_write_b128 v6, v[56:59] offset:288\n"
"ds_write_b128 v6, v[60:63] offset:304\n"
"s_mov_b64 exec, -1\n"
"v_subrev_u32 v7, 73728, v1\n"
"v_add_u32 v6, s63, v7\n"
"v_add_u32_e64 v7, s64, v7\n"
"v_and_b32 v1, 0x230, v0\n"
"v_and_b32_e64 v5, 0x40, v0\n"
"v_lshl_or_b32 v1, v5, 1, v1\n"
"v_and_b32 v5, 0x80, v0\n"
"v_lshrrev_b32_e64 v5, 1, v5\n"
"v_and_b32 v2, 0x100, v0\n"
"v_or_b32_e64 v1, v1, v5\n"
"v_lshl_or_b32 v1, v2, 2, v1\n"
"v_lshrrev_b32 v2, 3, v2\n"
"v_add_u32 v3, v3, v2\n"
"v_add_u32 v1, 73728, v1\n"
"v_add_u32 v2, 0x10000, v1\n"
"v_lshlrev_b32 v152, 16, v153\n"
"v_and_b32 v153, s68, v153\n"
"v_lshlrev_b32 v154, 16, v155\n"
"v_and_b32 v155, s68, v155\n"
"v_lshlrev_b32 v156, 16, v157\n"
"v_and_b32 v157, s68, v157\n"
"v_mul_f32 v152, s71, v152\n"
"v_mul_f32 v153, s71, v153\n"
"v_mul_f32 v154, s71, v154\n"
"v_mul_f32 v155, s71, v155\n"
"v_mul_f32 v156, s71, v156\n"
"v_mul_f32 v157, s71, v157\n"
"v_mov_b32 v159, v158\n"
"v_lshrrev_b32 v5, 2, v0\n"
"v_and_b32 v158, 0xe4, v5\n"
"v_and_b32_e64 v4, 8, v5\n"
"v_lshl_or_b32 v158, v4, 1, v158\n"
"v_and_b32 v5, 16, v5\n"
"v_lshrrev_b32 v5, 1, v5\n"
"v_or_b32_e64 v158, v158, v5\n"
"v_add_u32 v158, 71680, v158\n"
"v_lshrrev_b32 v5, 4, v0\n"
"v_and_b32 v4, 15, v5\n"
"v_mul_u32_u24 v4, 192, v4\n"
"v_bfe_u32 v48, v5, 4, 1\n"
"v_lshl_add_u32 v4, v48, 5, v4\n"
"v_lshrrev_b32_e64 v5, 5, v5\n"
"v_lshl_add_u32 v4, v5, 4, v4\n"
"s_add_u32 s88, s31, 196608\n"
"s_add_u32 s89, s31, 294912\n"
"s_add_u32 s90, s31, 0\n"
"s_add_u32 s91, s31, 98304\n"
"s_add_u32 s92, s61, 0\n"
"s_add_u32 s93, s61, 98304\n"
"s_add_u32 s94, s61, 196608\n"
"s_add_u32 s95, s61, 294912\n"
"WL 0\n"
"s_barrier\n"
"v_lshrrev_b32 v5, 1, v0\n"
"s_mov_b32 exec_hi, 0xffff\n"
"ds_read_b64 v[48:49], v5 offset:6144\n"
"ds_read_b64 v[50:51], v5 offset:6528\n"
"ds_read_b64 v[52:53], v5 offset:6912\n"
"ds_read_b64 v[54:55], v5 offset:7296\n"
"v_add_u32 v5, 72448, v5\n"
"WL 2\n"
"v_pk_add_f32 v[48:49], v[48:49], v[50:51]\n"
"WL 0\n"
"v_pk_add_f32 v[52:53], v[52:53], v[54:55]\n"
"v_pk_add_f32 v[62:63], v[48:49], v[52:53]\n"
"ds_write_b64 v5, v[62:63]\n"
"s_mov_b64 exec, -1\n"
"s_movk_i32 s72, 4096\n"
"s_mov_b32 s70, 0\n"
"Lh25_%=:\n"
"buffer_load_dwordx4 v[48:51], v0, s[48:51], s72 offen offset:0 sc1\n"
"buffer_load_dwordx4 v[52:55], v0, s[48:51], s72 offen offset:1024 sc1\n"
"buffer_load_dwordx4 v[56:59], v0, s[48:51], s72 offen offset:2048 sc1\n"
"v_mfma_f32_32x32x16_f16 a[232:247], v[32:35], v[44:47], 0\n"
"LD a[248:251],v1,0\n"
"LD a[248:251],v1,1024\n"
"PF v62,v160,v32,v165\n"
"PF v63,v163,v33,v176\n"
"PF v62,v166,v34,v187\n"
"PF v63,v169,v35,v198\n"
"v_mfma_f32_32x32x16_f16 a[232:247], v[84:87], v[120:123], a[232:247]\n"
"LD a[248:251],v1,2048\n"
"LD a[248:251],v1,3072\n"
"PF v62,v167,v37,v168\n"
"PF v63,v170,v38,v179\n"
"PF v62,v173,v39,v190\n"
"PF v63,v176,v40,v201\n"
"v_mfma_f32_32x32x16_f16 a[232:247], v[120:123], v[148:151], a[232:247]\n"
"LD a[248:251],v1,4096\n"
"LD a[248:251],v1,5120\n"
"PF v62,v174,v42,v171\n"
"PF v63,v177,v43,v182\n"
"PF v62,v180,v44,v193\n"
"PF v63,v183,v45,v204\n"
"v_mfma_f32_32x32x16_f16 a[232:247], v[140:143], v[88:91], a[232:247]\n"
"LD a[248:251],v1,6144\n"
"LD a[248:251],v1,7168\n"
"PF v62,v181,v47,v174\n"
"PF v63,v184,v32,v185\n"
"PF v62,v187,v33,v196\n"
"PF v63,v190,v34,v207\n"
"v_mfma_f32_32x32x16_f16 a[232:247], v[40:43], v[132:135], a[232:247]\n"
"LD a[248:251],v1,8192\n"
"LD a[248:251],v1,9216\n"
"PF v62,v188,v36,v177\n"
"PF v63,v191,v37,v188\n"
"PF v62,v194,v38,v199\n"
"PF v63,v197,v39,v210\n"
"v_mfma_f32_32x32x16_f16 a[232:247], v[92:95], v[40:43], a[232:247]\n"
"LD a[248:251],v1,10240\n"
"LD a[248:251],v1,11264\n"
"PF v62,v195,v41,v180\n"
"PF v63,v198,v42,v191\n"
"PF v62,v201,v43,v202\n"
"PF v63,v204,v44,v213\n"
"v_mfma_f32_32x32x16_f16 a[232:247], v[128:131], v[116:119], a[232:247]\n"
"LD a[248:251],v1,12288\n"
"LD a[248:251],v1,13312\n"
"PF v62,v202,v46,v183\n"
"PF v63,v205,v47,v194\n"
"PF v62,v208,v32,v205\n"
"PF v63,v211,v33,v216\n"
"v_mfma_f32_32x32x16_f16 a[232:247], v[148:151], v[144:147], a[232:247]\n"
"LD a[248:251],v1,14336\n"
"LD a[248:251],v1,15360\n"
"PF v62,v209,v35,v186\n"
"PF v63,v212,v36,v197\n"
"PF v62,v215,v37,v208\n"
"PF v63,v218,v38,v219\n"
"v_mfma_f32_32x32x16_f16 a[232:247], v[80:83], v[84:87], 0\n"
"LD a[248:251],v1,16384\n"
"LD a[248:251],v1,17408\n"
"PF v62,v216,v40,v189\n"
"PF v63,v219,v41,v200\n"
"PF v62,v222,v42,v211\n"
"PF v63,v225,v43,v222\n"
"v_mfma_f32_32x32x16_f16 a[232:247], v[116:119], v[128:131], a[232:247]\n"
"LD a[248:251],v1,18432\n"
"LD a[248:251],v1,19456\n"
"PF v62,v223,v45,v192\n"
"PF v63,v226,v46,v203\n"
"PF v62,v229,v47,v214\n"
"PF v63,v232,v32,v225\n"
"v_mfma_f32_32x32x16_f16 a[232:247], v[136:139], v[36:39], a[232:247]\n"
"LD a[248:251],v1,20480\n"
"LD a[248:251],v1,21504\n"
"PF v62,v230,v34,v195\n"
"PF v63,v233,v35,v206\n"
"PF v62,v236,v36,v217\n"
"PF v63,v239,v37,v228\n"
"v_mfma_f32_32x32x16_f16 a[232:247], v[36:39], v[112:115], a[232:247]\n"
"LD a[248:251],v1,22528\n"
"LD a[248:251],v1,23552\n"
"PF v62,v237,v39,v198\n"
"PF v63,v240,v40,v209\n"
"PF v62,v243,v41,v220\n"
"PF v63,v246,v42,v231\n"
"s_waitcnt vmcnt(2)\n"
"v_min3_u32 v60, v49, v51, s60\n"
"v_max3_u32 v61, v49, v51, s60\n"
"s_waitcnt vmcnt(1)\n"
"v_min3_u32 v60, v53, v55, v60\n"
"v_max3_u32 v61, v53, v55, v61\n"
"s_waitcnt vmcnt(0)\n"
"v_min3_u32 v60, v57, v59, v60\n"
"v_max3_u32 v61, v57, v59, v61\n"
"v_cmp_eq_u32 vcc, v60, v61\n"
"s_add_u32 s70, s70, 1\n"
"s_cmp_eq_u64 vcc, -1\n"
"s_cbranch_scc1 Lh2x6_%=\n"
"s_cmp_lt_u32 s70, 0x40000\n"
"s_cbranch_scc1 Lh25_%=\n"
"Lh2x6_%=:\n"
"WL 0\n"
"v_add_f32 v60, v48, v50\n"
"v_add_f32 v61, v52, v54\n"
"v_add_f32 v62, v56, v58\n"
"v_add_f32 v60, v60, v61\n"
"v_add_f32 v60, v60, v62\n"
"v_mul_f32 v48, v48, v152\n"
"v_mul_f32 v56, v56, v156\n"
"v_add_f32_dpp v60, v60, v60 row_ror:1 row_mask:0xf bank_mask:0xf\n"
"v_mul_f32 v50, v50, v153\n"
"v_mul_f32 v58, v58, v157\n"
"v_add_f32_dpp v60, v60, v60 row_ror:2 row_mask:0xf bank_mask:0xf\n"
"v_mul_f32 v52, v52, v154\n"
"s_nop 0\n"
"v_add_f32_dpp v60, v60, v60 row_ror:4 row_mask:0xf bank_mask:0xf\n"
"v_mul_f32 v54, v54, v155\n"
"s_nop 0\n"
"v_add_f32_dpp v60, v60, v60 row_ror:8 row_mask:0xf bank_mask:0xf\n"
"v_mov_b32 v61, v60\n"
"s_nop 1\n"
"v_permlane16_swap_b32 v60, v61\n"
"v_add_f32 v60, v60, v61\n"
"v_mov_b32 v61, v60\n"
"s_nop 1\n"
"v_permlane32_swap_b32 v60, v61\n"
"v_add_f32 v60, v60, v61\n"
"v_mov_b32 v61, 0x358637bd\n"
"v_fmac_f32 v61, 0x3b2aaaab, v60\n"
"v_rcp_f32 v60, v61\n"
"s_nop 0\n"
"v_fma_f32 v48, v48, v60, s71\n"
"v_fma_f32 v50, v50, v60, s71\n"
"v_fma_f32 v52, v52, v60, s71\n"
"v_fma_f32 v54, v54, v60, s71\n"
"v_fma_f32 v56, v56, v60, s71\n"
"v_fma_f32 v58, v58, v60, s71\n"
"CH v48,v48,v50\n"
"CH v52,v52,v54\n"
"CH v56,v56,v58\n"
"ds_write_b32 v158, v48 offset:0\n"
"ds_write_b32 v158, v52 offset:256\n"
"ds_write_b32 v158, v56 offset:512\n"
"LD v[48:51],v159,0\n"
"LD v[52:55],v159,32\n"
"LD v[56:59],v159,64\n"
"LD v[60:63],v159,96\n"
"WL 3\n"
"PM v32,v32,v48\n"
"PM v33,v33,v49\n"
"PM v34,v34,v50\n"
"PM v35,v35,v51\n"
"ds_write_b128 v6, v[32:35] offset:0\n"
"LD v[32:35],v3,0\n"
"WL 4\n"
"PM v36,v36,v52\n"
"PM v37,v37,v53\n"
"PM v38,v38,v54\n"
"PM v39,v39,v55\n"
"ds_write_b128 v6, v[36:39] offset:1024\n"
"LD v[36:39],v3,16\n"
"PM v40,v40,v48\n"
"PM v41,v41,v49\n"
"PM v42,v42,v50\n"
"PM v43,v43,v51\n"
"ds_write_b128 v7, v[40:43] offset:2048\n"
"PM v44,v44,v52\n"
"PM v45,v45,v53\n"
"PM v46,v46,v54\n"
"PM v47,v47,v55\n"
"ds_write_b128 v7, v[44:47] offset:3072\n"
"PM v80,v80,v48\n"
"PM v81,v81,v49\n"
"PM v82,v82,v50\n"
"PM v83,v83,v51\n"
"ds_write_b128 v7, v[80:83] offset:4096\n"
"LD v[48:51],v159,128\n"
"LD v[80:83],v3,128\n"
"PM v84,v84,v52\n"
"PM v85,v85,v53\n"
"PM v86,v86,v54\n"
"PM v87,v87,v55\n"
"ds_write_b128 v7, v[84:87] offset:5120\n"
"LD v[52:55],v159,160\n"
"LD v[84:87],v3,144\n"
"WL 10\n"
"s_nop 0\n"
"PM v88,v88,v56\n"
"PM v89,v89,v57\n"
"PM v90,v90,v58\n"
"PM v91,v91,v59\n"
"ds_write_b128 v7, v[88:91] offset:6144\n"
"PM v92,v92,v60\n"
"PM v93,v93,v61\n"
"PM v94,v94,v62\n"
"PM v95,v95,v63\n"
"ds_write_b128 v7, v[92:95] offset:7168\n"
"PM v112,v112,v56\n"
"PM v113,v113,v57\n"
"PM v114,v114,v58\n"
"PM v115,v115,v59\n"
"ds_write_b128 v7, v[112:115] offset:8192\n"
"LD v[112:115],v3,256\n"
"PM v116,v116,v60\n"
"PM v117,v117,v61\n"
"PM v118,v118,v62\n"
"PM v119,v119,v63\n"
"ds_write_b128 v7, v[116:119] offset:9216\n"
"LD v[116:119],v3,272\n"
"PM v120,v120,v56\n"
"PM v121,v121,v57\n"
"PM v122,v122,v58\n"
"PM v123,v123,v59\n"
"ds_write_b128 v7, v[120:123] offset:10240\n"
"PM v124,v124,v60\n"
"PM v125,v125,v61\n"
"PM v126,v126,v62\n"
"PM v127,v127,v63\n"
"ds_write_b128 v7, v[124:127] offset:11264\n"
"WL 8\n"
"s_nop 0\n"
"PM v128,v128,v48\n"
"PM v129,v129,v49\n"
"PM v130,v130,v50\n"
"PM v131,v131,v51\n"
"ds_write_b128 v7, v[128:131] offset:12288\n"
"LD v[128:131],v3,0\n"
"PM v132,v132,v52\n"
"PM v133,v133,v53\n"
"PM v134,v134,v54\n"
"PM v135,v135,v55\n"
"ds_write_b128 v7, v[132:135] offset:13312\n"
"LD v[132:135],v3,16\n"
"PM v136,v136,v48\n"
"PM v137,v137,v49\n"
"PM v138,v138,v50\n"
"PM v139,v139,v51\n"
"ds_write_b128 v7, v[136:139] offset:14336\n"
"PM v140,v140,v52\n"
"PM v141,v141,v53\n"
"PM v142,v142,v54\n"
"PM v143,v143,v55\n"
"ds_write_b128 v7, v[140:143] offset:15360\n"
"PM v144,v144,v48\n"
"PM v145,v145,v49\n"
"PM v146,v146,v50\n"
"PM v147,v147,v51\n"
"ds_write_b128 v7, v[144:147] offset:16384\n"
"PM v148,v148,v52\n"
"PM v149,v149,v53\n"
"PM v150,v150,v54\n"
"PM v151,v151,v55\n"
"ds_write_b128 v7, v[148:151] offset:17408\n"
"WL 0\n"
"s_barrier\n"
"v_subrev_u32 v7, 73728, v1\n"
"LD a[232:235],v7,8192\n"
"LD a[236:239],v7,8448\n"
"LD a[240:243],v7,10240\n"
"LD a[244:247],v7,10496\n"
"LD v[144:147],v3,128\n"
"LD v[148:151],v3,144\n"
"LD a[248:251],v7,12288\n"
"LD a[252:255],v7,12544\n"
"LD v[48:51],v3,256\n"
"LD v[52:55],v3,272\n"
"buffer_load_dword v6, v0, s[24:27], s28 offen\n"
"WL 9\n"
"v_mfma_f32_16x16x32_f16 v[40:43], a[232:235], a[196:199], v[32:35]\n"
"SH v[32:35],a[232:235],a[192:195]\n"
"v_mfma_f32_16x16x32_f16 v[136:139], a[232:235], a[204:207], v[128:131]\n"
"SH v[128:131],a[232:235],a[200:203]\n"
"LD a[232:235],v7,14336\n"
"WL 9\n"
"v_mfma_f32_16x16x32_f16 v[44:47], a[236:239], a[196:199], v[36:39]\n"
"SH v[36:39],a[236:239],a[192:195]\n"
"v_mfma_f32_16x16x32_f16 v[140:143], a[236:239], a[204:207], v[132:135]\n"
"SH v[132:135],a[236:239],a[200:203]\n"
"LD a[236:239],v7,14592\n"
"WL 9\n"
"v_mfma_f32_16x16x32_f16 v[88:91], a[240:243], a[196:199], v[80:83]\n"
"SH v[80:83],a[240:243],a[192:195]\n"
"WL 7\n"
"v_mfma_f32_16x16x32_f16 v[152:155], a[240:243], a[204:207], v[144:147]\n"
"SH v[144:147],a[240:243],a[200:203]\n"
"LD a[240:243],v7,16384\n"
"v_mfma_f32_16x16x32_f16 v[92:95], a[244:247], a[196:199], v[84:87]\n"
"SH v[84:87],a[244:247],a[192:195]\n"
"WL 7\n"
"v_mfma_f32_16x16x32_f16 v[156:159], a[244:247], a[204:207], v[148:151]\n"
"SH v[148:151],a[244:247],a[200:203]\n"
"LD a[244:247],v7,16640\n"
"WL 7\n"
"v_mfma_f32_16x16x32_f16 v[120:123], a[248:251], a[196:199], v[112:115]\n"
"SH v[112:115],a[248:251],a[192:195]\n"
"WL 5\n"
"v_mfma_f32_16x16x32_f16 v[56:59], a[248:251], a[204:207], v[48:51]\n"
"SH v[48:51],a[248:251],a[200:203]\n"
"LD a[248:251],v7,18432\n"
"v_permlane16_swap_b32 v8, v12\n"
"v_mfma_f32_16x16x32_f16 v[124:127], a[252:255], a[196:199], v[116:119]\n"
"SH v[116:119],a[252:255],a[192:195]\n"
"WL 5\n"
"v_mfma_f32_16x16x32_f16 v[60:63], a[252:255], a[204:207], v[52:55]\n"
"SH v[52:55],a[252:255],a[200:203]\n"
"LD a[252:255],v7,18688\n"
"WL 5\n"
"SH v[40:43],a[232:235],a[212:215]\n"
"s_add_u32 s29, s31, 294912\n"
"buffer_load_dwordx4 a[192:195], v0, s[40:43], s29 offen offset:0\n"
"SH v[32:35],a[232:235],a[208:211]\n"
"v_permlane16_swap_b32_e64 v9, v13\n"
"SH v[136:139],a[232:235],a[220:223]\n"
"v_permlane16_swap_b32_e64 v10, v14\n"
"SH v[128:131],a[232:235],a[216:219]\n"
"LD a[232:235],v7,20480\n"
"WL 4\n"
"SH v[44:47],a[236:239],a[212:215]\n"
"v_permlane16_swap_b32 v11, v15\n"
"SH v[36:39],a[236:239],a[208:211]\n"
"buffer_load_dwordx4 a[196:199], v0, s[40:43], s29 offen offset:1024\n"
"SH v[140:143],a[236:239],a[220:223]\n"
"v_permlane16_swap_b32_e64 v160, v164\n"
"SH v[132:135],a[236:239],a[216:219]\n"
"LD a[236:239],v7,20736\n"
"SH v[88:91],a[240:243],a[212:215]\n"
"SH v[80:83],a[240:243],a[208:211]\n"
"v_permlane16_swap_b32_e64 v161, v165\n"
"SH v[152:155],a[240:243],a[220:223]\n"
"SH v[144:147],a[240:243],a[216:219]\n"
"LD a[240:243],v7,22528\n"
"WL 4\n"
"SH v[92:95],a[244:247],a[212:215]\n"
"v_permlane16_swap_b32 v162, v166\n"
"SH v[84:87],a[244:247],a[208:211]\n"
"SH v[156:159],a[244:247],a[220:223]\n"
"v_permlane16_swap_b32_e64 v163, v167\n"
"SH v[148:151],a[244:247],a[216:219]\n"
"LD a[244:247],v7,22784\n"
"SH v[120:123],a[248:251],a[212:215]\n"
"SH v[112:115],a[248:251],a[208:211]\n"
"SH v[56:59],a[248:251],a[220:223]\n"
"v_permlane16_swap_b32 v168, v172\n"
"SH v[48:51],a[248:251],a[216:219]\n"
"LD a[248:251],v7,24576\n"
"WL 5\n"
"SH v[124:127],a[252:255],a[212:215]\n"
"SH v[116:119],a[252:255],a[208:211]\n"
"v_permlane16_swap_b32_e64 v169, v173\n"
"SH v[60:63],a[252:255],a[220:223]\n"
"SH v[52:55],a[252:255],a[216:219]\n"
"LD a[252:255],v7,24832\n"
"WL 5\n"
"SH v[40:43],a[232:235],a[228:231]\n"
"v_permlane16_swap_b32 v170, v174\n"
"SH v[32:35],a[232:235],a[224:227]\n"
"SH v[136:139],a[232:235],v[12:15]\n"
"SH v[128:131],a[232:235],v[8:11]\n"
"LD a[232:235],v7,26624\n"
"WL 5\n"
"SH v[44:47],a[236:239],a[228:231]\n"
"v_permlane16_swap_b32 v171, v175\n"
"SH v[36:39],a[236:239],a[224:227]\n"
"SH v[140:143],a[236:239],v[12:15]\n"
"SH v[132:135],a[236:239],v[8:11]\n"
"LD a[236:239],v7,26880\n"
"WL 5\n"
"SH v[88:91],a[240:243],a[228:231]\n"
"v_lshrrev_b32 v5, 4, v0\n"
"v_permlane16_swap_b32 v176, v180\n"
"SH v[80:83],a[240:243],a[224:227]\n"
"s_waitcnt vmcnt(2)\n"
"v_bfe_u32 v6, v5, 4, 1\n"
"SH v[152:155],a[240:243],v[12:15]\n"
"SH v[144:147],a[240:243],v[8:11]\n"
"LD a[240:243],v7,28672\n"
"WL 4\n"
"SH v[92:95],a[244:247],a[228:231]\n"
"v_permlane16_swap_b32 v177, v181\n"
"SH v[84:87],a[244:247],a[224:227]\n"
"SH v[156:159],a[244:247],v[12:15]\n"
"SH v[148:151],a[244:247],v[8:11]\n"
"LD a[244:247],v7,28928\n"
"SH v[120:123],a[248:251],a[228:231]\n"
"s_add_u32 s29, s31, 296960\n"
"buffer_load_dwordx4 a[200:203], v0, s[40:43], s29 offen offset:0\n"
"SH v[112:115],a[248:251],a[224:227]\n"
"v_permlane16_swap_b32_e64 v178, v182\n"
"SH v[56:59],a[248:251],v[12:15]\n"
"SH v[48:51],a[248:251],v[8:11]\n"
"LD a[248:251],v7,30720\n"
"WL 4\n"
"SH v[124:127],a[252:255],a[228:231]\n"
"v_permlane16_swap_b32 v179, v183\n"
"SH v[116:119],a[252:255],a[224:227]\n"
"SH v[60:63],a[252:255],v[12:15]\n"
"v_lshrrev_b32 v13, 5, v5\n"
"v_and_b32 v12, 3, v5\n"
"SH v[52:55],a[252:255],v[8:11]\n"
"LD a[252:255],v7,30976\n"
"SH v[40:43],a[232:235],v[164:167]\n"
"v_mul_u32_u24 v12, 0x1111, v12\n"
"v_permlane16_swap_b32_e64 v184, v188\n"
"SH v[32:35],a[232:235],v[160:163]\n"
"SH v[136:139],a[232:235],v[172:175]\n"
"SH v[128:131],a[232:235],v[168:171]\n"
"LD a[232:235],v7,32768\n"
"WL 4\n"
"SH v[44:47],a[236:239],v[164:167]\n"
"v_cmp_eq_u32 vcc, v6, v13\n"
"v_mov_b32 v13, 0x3f80\n"
"SH v[36:39],a[236:239],v[160:163]\n"
"v_permlane16_swap_b32_e64 v185, v189\n"
"SH v[140:143],a[236:239],v[172:175]\n"
"v_cndmask_b32_e64 v6, 0, v13, vcc\n"
"v_bfe_u32 v13, v5, 2, 1\n"
"SH v[132:135],a[236:239],v[168:171]\n"
"LD a[236:239],v7,33024\n"
"SH v[88:91],a[240:243],v[164:167]\n"
"v_bfe_u32 v5, v5, 3, 1\n"
"v_lshl_or_b32 v5, v13, 1, v5\n"
"SH v[80:83],a[240:243],v[160:163]\n"
"buffer_load_dwordx4 a[204:207], v0, s[40:43], s29 offen offset:1024\n"
"SH v[152:155],a[240:243],v[172:175]\n"
"v_permlane16_swap_b32_e64 v186, v190\n"
"SH v[144:147],a[240:243],v[168:171]\n"
"LD a[240:243],v7,34816\n"
"WL 4\n"
"SH v[92:95],a[244:247],v[164:167]\n"
"v_cmp_eq_u32 vcc, 0, v5\n"
"v_permlane16_swap_b32_e64 v187, v191\n"
"SH v[84:87],a[244:247],v[160:163]\n"
"v_cndmask_b32 v8, 0, v6, vcc\n"
"v_cmp_eq_u32 vcc, 1, v5\n"
"SH v[156:159],a[244:247],v[172:175]\n"
"v_permlane16_swap_b32_e64 v192, v196\n"
"SH v[148:151],a[244:247],v[168:171]\n"
"LD a[244:247],v7,35072\n"
"SH v[120:123],a[248:251],v[164:167]\n"
"v_cndmask_b32 v9, 0, v6, vcc\n"
"v_cmp_eq_u32 vcc, 2, v5\n"
"SH v[112:115],a[248:251],v[160:163]\n"
"v_permlane16_swap_b32_e64 v193, v197\n"
"SH v[56:59],a[248:251],v[172:175]\n"
"v_cndmask_b32 v10, 0, v6, vcc\n"
"v_cmp_eq_u32 vcc, 3, v5\n"
"SH v[48:51],a[248:251],v[168:171]\n"
"LD a[248:251],v7,36864\n"
"WL 3\n"
"SH v[124:127],a[252:255],v[164:167]\n"
"v_cndmask_b32 v11, 0, v6, vcc\n"
"v_permlane16_swap_b32_e64 v194, v198\n"
"SH v[116:119],a[252:255],v[160:163]\n"
"v_permlane16_swap_b32_e64 v195, v199\n"
"SH v[60:63],a[252:255],v[172:175]\n"
"v_permlane16_swap_b32_e64 v200, v204\n"
"SH v[52:55],a[252:255],v[168:171]\n"
"LD a[252:255],v7,37120\n"
"SH v[40:43],a[232:235],v[180:183]\n"
"v_permlane16_swap_b32_e64 v201, v205\n"
"SH v[32:35],a[232:235],v[176:179]\n"
"v_permlane16_swap_b32_e64 v202, v206\n"
"SH v[136:139],a[232:235],v[188:191]\n"
"SH v[128:131],a[232:235],v[184:187]\n"
"LD a[232:235],v7,38912\n"
"SH v[44:47],a[236:239],v[180:183]\n"
"v_permlane16_swap_b32 v203, v207\n"
"SH v[36:39],a[236:239],v[176:179]\n"
"SH v[140:143],a[236:239],v[188:191]\n"
"SH v[132:135],a[236:239],v[184:187]\n"
"LD a[236:239],v7,39168\n"
"WL 4\n"
"SH v[88:91],a[240:243],v[180:183]\n"
"s_add_u32 s29, s31, 299008\n"
"buffer_load_dwordx4 a[208:211], v0, s[40:43], s29 offen offset:0\n"
"SH v[80:83],a[240:243],v[176:179]\n"
"v_permlane16_swap_b32_e64 v208, v212\n"
"SH v[152:155],a[240:243],v[188:191]\n"
"buffer_load_dwordx4 v[160:163], v4, s[36:39], s88 offen offset:0\n"
"SH v[144:147],a[240:243],v[184:187]\n"
"LD a[240:243],v7,40960\n"
"SH v[92:95],a[244:247],v[180:183]\n"
"v_permlane16_swap_b32_e64 v209, v213\n"
"SH v[84:87],a[244:247],v[176:179]\n"
"v_permlane16_swap_b32_e64 v210, v214\n"
"SH v[156:159],a[244:247],v[188:191]\n"
"buffer_load_dwordx4 v[168:171], v4, s[36:39], s88 offen offset:64\n"
"SH v[148:151],a[244:247],v[184:187]\n"
"LD a[244:247],v7,41216\n"
"WL 4\n"
"SH v[120:123],a[248:251],v[180:183]\n"
"v_permlane16_swap_b32 v211, v215\n"
"SH v[112:115],a[248:251],v[176:179]\n"
"SH v[56:59],a[248:251],v[188:191]\n"
"SH v[48:51],a[248:251],v[184:187]\n"
"LD a[248:251],v7,43008\n"
"SH v[124:127],a[252:255],v[180:183]\n"
"v_permlane16_swap_b32_e64 v216, v220\n"
"SH v[116:119],a[252:255],v[176:179]\n"
"buffer_load_dwordx4 v[176:179], v4, s[36:39], s88 offen offset:128\n"
"SH v[60:63],a[252:255],v[188:191]\n"
"buffer_load_dwordx4 a[212:215], v0, s[40:43], s29 offen offset:1024\n"
"SH v[52:55],a[252:255],v[184:187]\n"
"LD a[252:255],v7,43264\n"
"WL 4\n"
"SH v[40:43],a[232:235],v[196:199]\n"
"v_permlane16_swap_b32 v217, v221\n"
"SH v[32:35],a[232:235],v[192:195]\n"
"v_permlane16_swap_b32_e64 v218, v222\n"
"SH v[136:139],a[232:235],v[204:207]\n"
"v_permlane16_swap_b32_e64 v219, v223\n"
"SH v[128:131],a[232:235],v[200:203]\n"
"LD a[232:235],v7,45056\n"
"SH v[44:47],a[236:239],v[196:199]\n"
"buffer_load_dwordx4 v[164:167], v4, s[36:39], s88 offen offset:3072\n"
"SH v[36:39],a[236:239],v[192:195]\n"
"buffer_load_dwordx4 v[172:175], v4, s[36:39], s88 offen offset:3136\n"
"SH v[140:143],a[236:239],v[204:207]\n"
"buffer_load_dwordx4 v[180:183], v4, s[36:39], s88 offen offset:3200\n"
"SH v[132:135],a[236:239],v[200:203]\n"
"LD a[236:239],v7,45312\n"
"WL 4\n"
"SH v[88:91],a[240:243],v[196:199]\n"
"v_permlane16_swap_b32 v224, v228\n"
"SH v[80:83],a[240:243],v[192:195]\n"
"SH v[152:155],a[240:243],v[204:207]\n"
"SH v[144:147],a[240:243],v[200:203]\n"
"LD a[240:243],v7,47104\n"
"SH v[92:95],a[244:247],v[196:199]\n"
"v_permlane16_swap_b32_e64 v225, v229\n"
"SH v[84:87],a[244:247],v[192:195]\n"
"v_permlane16_swap_b32_e64 v226, v230\n"
"SH v[156:159],a[244:247],v[204:207]\n"
"SH v[148:151],a[244:247],v[200:203]\n"
"LD a[244:247],v7,47360\n"
"WL 4\n"
"SH v[120:123],a[248:251],v[196:199]\n"
"v_permlane16_swap_b32 v227, v231\n"
"SH v[112:115],a[248:251],v[192:195]\n"
"SH v[56:59],a[248:251],v[204:207]\n"
"SH v[48:51],a[248:251],v[200:203]\n"
"LD a[248:251],v7,49152\n"
"SH v[124:127],a[252:255],v[196:199]\n"
"v_permlane16_swap_b32_e64 v232, v236\n"
"SH v[116:119],a[252:255],v[192:195]\n"
"SH v[60:63],a[252:255],v[204:207]\n"
"SH v[52:55],a[252:255],v[200:203]\n"
"LD a[252:255],v7,49408\n"
"WL 5\n"
"SH v[40:43],a[232:235],v[212:215]\n"
"s_add_u32 s29, s31, 0\n"
"buffer_load_dwordx4 a[216:219], v0, s[40:43], s29 offen offset:0\n"
"SH v[32:35],a[232:235],v[208:211]\n"
"v_permlane16_swap_b32_e64 v233, v237\n"
"SH v[136:139],a[232:235],v[220:223]\n"
"v_permlane16_swap_b32_e64 v234, v238\n"
"SH v[128:131],a[232:235],v[216:219]\n"
"LD a[232:235],v7,51200\n"
"WL 4\n"
"SH v[44:47],a[236:239],v[212:215]\n"
"v_permlane16_swap_b32 v235, v239\n"
"SH v[36:39],a[236:239],v[208:211]\n"
"buffer_load_dwordx4 v[184:187], v4, s[36:39], s89 offen offset:0\n"
"SH v[140:143],a[236:239],v[220:223]\n"
"buffer_load_dwordx4 v[192:195], v4, s[36:39], s89 offen offset:64\n"
"SH v[132:135],a[236:239],v[216:219]\n"
"LD a[236:239],v7,51456\n"
"SH v[88:91],a[240:243],v[212:215]\n"
"v_permlane16_swap_b32_e64 v240, v244\n"
"SH v[80:83],a[240:243],v[208:211]\n"
"buffer_load_dwordx4 v[200:203], v4, s[36:39], s89 offen offset:128\n"
"SH v[152:155],a[240:243],v[220:223]\n"
"buffer_load_dwordx4 a[220:223], v0, s[40:43], s29 offen offset:1024\n"
"SH v[144:147],a[240:243],v[216:219]\n"
"LD a[240:243],v7,53248\n"
"WL 4\n"
"SH v[92:95],a[244:247],v[212:215]\n"
"v_permlane16_swap_b32 v241, v245\n"
"SH v[84:87],a[244:247],v[208:211]\n"
"v_permlane16_swap_b32_e64 v242, v246\n"
"SH v[156:159],a[244:247],v[220:223]\n"
"SH v[148:151],a[244:247],v[216:219]\n"
"LD a[244:247],v7,53504\n"
"SH v[120:123],a[248:251],v[212:215]\n"
"v_permlane16_swap_b32_e64 v243, v247\n"
"SH v[112:115],a[248:251],v[208:211]\n"
"SH v[56:59],a[248:251],v[220:223]\n"
"SH v[48:51],a[248:251],v[216:219]\n"
"LD a[248:251],v7,55296\n"
"WL 4\n"
"SH v[124:127],a[252:255],v[212:215]\n"
"v_permlane16_swap_b32 v248, v252\n"
"SH v[116:119],a[252:255],v[208:211]\n"
"buffer_load_dwordx4 v[188:191], v4, s[36:39], s89 offen offset:3072\n"
"SH v[60:63],a[252:255],v[220:223]\n"
"buffer_load_dwordx4 v[196:199], v4, s[36:39], s89 offen offset:3136\n"
"SH v[52:55],a[252:255],v[216:219]\n"
"LD a[252:255],v7,55552\n"
"SH v[40:43],a[232:235],v[228:231]\n"
"LD v[208:211],v3,0\n"
"v_permlane16_swap_b32_e64 v249, v253\n"
"SH v[32:35],a[232:235],v[224:227]\n"
"buffer_load_dwordx4 v[204:207], v4, s[36:39], s89 offen offset:3200\n"
"SH v[136:139],a[232:235],v[236:239]\n"
"v_permlane16_swap_b32 v250, v254\n"
"SH v[128:131],a[232:235],v[232:235]\n"
"LD a[232:235],v7,57344\n"
"WL 5\n"
"SH v[44:47],a[236:239],v[228:231]\n"
"LD v[212:215],v3,16\n"
"v_permlane16_swap_b32_e64 v251, v255\n"
"SH v[36:39],a[236:239],v[224:227]\n"
"SH v[140:143],a[236:239],v[236:239]\n"
"SH v[132:135],a[236:239],v[232:235]\n"
"LD a[236:239],v7,57600\n"
"SH v[88:91],a[240:243],v[228:231]\n"
"v_permlane16_swap_b32_e64 v16, v20\n"
"SH v[80:83],a[240:243],v[224:227]\n"
"SH v[152:155],a[240:243],v[236:239]\n"
"SH v[144:147],a[240:243],v[232:235]\n"
"LD a[240:243],v7,59392\n"
"WL 6\n"
"SH v[92:95],a[244:247],v[228:231]\n"
"v_permlane16_swap_b32 v17, v21\n"
"SH v[84:87],a[244:247],v[224:227]\n"
"SH v[156:159],a[244:247],v[236:239]\n"
"SH v[148:151],a[244:247],v[232:235]\n"
"LD a[244:247],v7,59648\n"
"SH v[120:123],a[248:251],v[228:231]\n"
"s_add_u32 s29, s31, 2048\n"
"buffer_load_dwordx4 a[224:227], v0, s[40:43], s29 offen offset:0\n"
"SH v[112:115],a[248:251],v[224:227]\n"
"v_permlane16_swap_b32_e64 v18, v22\n"
"SH v[56:59],a[248:251],v[236:239]\n"
"v_permlane16_swap_b32_e64 v19, v23\n"
"SH v[48:51],a[248:251],v[232:235]\n"
"LD a[248:251],v7,61440\n"
"WL 5\n"
"SH v[124:127],a[252:255],v[228:231]\n"
"v_permlane16_swap_b32 v24, v28\n"
"SH v[116:119],a[252:255],v[224:227]\n"
"buffer_load_dwordx4 a[228:231], v0, s[40:43], s29 offen offset:1024\n"
"SH v[60:63],a[252:255],v[236:239]\n"
"SH v[52:55],a[252:255],v[232:235]\n"
"LD a[252:255],v7,61696\n"
"SH v[40:43],a[232:235],v[244:247]\n"
"LD v[224:227],v3,128\n"
"v_permlane16_swap_b32_e64 v25, v29\n"
"SH v[32:35],a[232:235],v[240:243]\n"
"v_permlane16_swap_b32_e64 v26, v30\n"
"SH v[136:139],a[232:235],v[252:255]\n"
"SH v[128:131],a[232:235],v[248:251]\n"
"LD a[232:235],v7,63488\n"
"WL 5\n"
"SH v[44:47],a[236:239],v[244:247]\n"
"LD v[228:231],v3,144\n"
"v_permlane16_swap_b32 v27, v31\n"
"SH v[36:39],a[236:239],v[240:243]\n"
"SH v[140:143],a[236:239],v[252:255]\n"
"SH v[132:135],a[236:239],v[248:251]\n"
"LD a[236:239],v7,63744\n"
"SH v[88:91],a[240:243],v[244:247]\n"
"v_permlane16_swap_b32_e64 v64, v68\n"
"SH v[80:83],a[240:243],v[240:243]\n"
"SH v[152:155],a[240:243],v[252:255]\n"
"v_permlane16_swap_b32 v65, v69\n"
"SH v[144:147],a[240:243],v[248:251]\n"
"LD a[240:243],v2,8192\n"
"WL 6\n"
"SH v[92:95],a[244:247],v[244:247]\n"
"SH v[84:87],a[244:247],v[240:243]\n"
"SH v[156:159],a[244:247],v[252:255]\n"
"v_permlane16_swap_b32_e64 v66, v70\n"
"SH v[148:151],a[244:247],v[248:251]\n"
"LD a[244:247],v2,8448\n"
"SH v[120:123],a[248:251],v[244:247]\n"
"v_permlane16_swap_b32_e64 v67, v71\n"
"SH v[112:115],a[248:251],v[240:243]\n"
"SH v[56:59],a[248:251],v[252:255]\n"
"SH v[48:51],a[248:251],v[248:251]\n"
"LD a[248:251],v2,10240\n"
"WL 6\n"
"SH v[124:127],a[252:255],v[244:247]\n"
"v_permlane16_swap_b32 v72, v76\n"
"SH v[116:119],a[252:255],v[240:243]\n"
"SH v[60:63],a[252:255],v[252:255]\n"
"SH v[52:55],a[252:255],v[248:251]\n"
"LD a[252:255],v2,10496\n"
"WL 5\n"
"SH v[40:43],a[232:235],v[20:23]\n"
"LD v[240:243],v3,256\n"
"v_permlane16_swap_b32 v73, v77\n"
"SH v[32:35],a[232:235],v[16:19]\n"
"SH v[136:139],a[232:235],v[28:31]\n"
"v_permlane16_swap_b32 v74, v78\n"
"SH v[128:131],a[232:235],v[24:27]\n"
"LD a[232:235],v2,12288\n"
"WL 6\n"
"SH v[44:47],a[236:239],v[20:23]\n"
"LD v[244:247],v3,272\n"
"v_permlane16_swap_b32 v75, v79\n"
"s_waitcnt vmcnt(10)\n"
"v_smfmac_f32_32x32x32_bf16 v[112:127], v[8:11], v[176:183], v12\n"
"SH v[36:39],a[236:239],v[16:19]\n"
"SH v[140:143],a[236:239],v[28:31]\n"
"SH v[132:135],a[236:239],v[24:27]\n"
"LD a[236:239],v2,12544\n"
"WL 6\n"
"SH v[88:91],a[240:243],v[20:23]\n"
"v_permlane16_swap_b32 v96, v100\n"
"SH v[80:83],a[240:243],v[16:19]\n"
"SH v[152:155],a[240:243],v[28:31]\n"
"SH v[144:147],a[240:243],v[24:27]\n"
"LD a[240:243],v2,14336\n"
"SH v[92:95],a[244:247],v[20:23]\n"
"v_permlane16_swap_b32_e64 v97, v101\n"
"v_smfmac_f32_32x32x32_bf16 v[32:47], v[8:11], v[160:167], v12\n"
"SH v[84:87],a[244:247],v[16:19]\n"
"SH v[156:159],a[244:247],v[28:31]\n"
"SH v[148:151],a[244:247],v[24:27]\n"
"LD a[244:247],v2,14592\n"
"WL 5\n"
"SH v[120:123],a[248:251],v[20:23]\n"
"v_permlane16_swap_b32 v98, v102\n"
"SH v[112:115],a[248:251],v[16:19]\n"
"v_permlane16_swap_b32_e64 v99, v103\n"
"SH v[56:59],a[248:251],v[28:31]\n"
"SH v[48:51],a[248:251],v[24:27]\n"
"LD a[248:251],v2,16384\n"
"SH v[124:127],a[252:255],v[20:23]\n"
"v_smfmac_f32_32x32x32_bf16 v[80:95], v[8:11], v[168:175], v12\n"
"v_permlane16_swap_b32_e64 v104, v108\n"
"SH v[116:119],a[252:255],v[16:19]\n"
"SH v[60:63],a[252:255],v[28:31]\n"
"SH v[52:55],a[252:255],v[24:27]\n"
"LD a[252:255],v2,16640\n"
"WL 5\n"
"SH v[40:43],a[232:235],v[68:71]\n"
"LD v[16:19],v3,0\n"
"v_permlane16_swap_b32 v105, v109\n"
"SH v[32:35],a[232:235],v[64:67]\n"
"SH v[136:139],a[232:235],v[76:79]\n"
"v_permlane16_swap_b32 v106, v110\n"
"SH v[128:131],a[232:235],v[72:75]\n"
"LD a[232:235],v2,18432\n"
"WL 5\n"
"SH v[44:47],a[236:239],v[68:71]\n"
"LD v[20:23],v3,16\n"
"v_permlane16_swap_b32 v107, v111\n"
"s_waitcnt vmcnt(2)\n"
"v_smfmac_f32_32x32x32_bf16 v[48:63], v[8:11], v[200:207], v12\n"
"SH v[36:39],a[236:239],v[64:67]\n"
"SH v[140:143],a[236:239],v[76:79]\n"
"SH v[132:135],a[236:239],v[72:75]\n"
"LD a[236:239],v2,18688\n"
"SH v[88:91],a[240:243],v[68:71]\n"
"SH v[80:83],a[240:243],v[64:67]\n"
"SH v[152:155],a[240:243],v[76:79]\n"
"SH v[144:147],a[240:243],v[72:75]\n"
"LD a[240:243],v2,20480\n"
"WL 6\n"
"SH v[92:95],a[244:247],v[68:71]\n"
"v_smfmac_f32_32x32x32_bf16 v[128:143], v[8:11], v[184:191], v12\n"
"SH v[84:87],a[244:247],v[64:67]\n"
"SH v[156:159],a[244:247],v[76:79]\n"
"SH v[148:151],a[244:247],v[72:75]\n"
"LD a[244:247],v2,20736\n"
"SH v[120:123],a[248:251],v[68:71]\n"
"SH v[112:115],a[248:251],v[64:67]\n"
"SH v[56:59],a[248:251],v[76:79]\n"
"SH v[48:51],a[248:251],v[72:75]\n"
"LD a[248:251],v2,22528\n"
"WL 6\n"
"SH v[124:127],a[252:255],v[68:71]\n"
"v_smfmac_f32_32x32x32_bf16 v[144:159], v[8:11], v[192:199], v12\n"
"AR v192,a0\n"
"AR v196,a4\n"
"buffer_load_dwordx4 v[160:163], v4, s[36:39], s90 offen offset:0\n"
"SH v[116:119],a[252:255],v[64:67]\n"
"buffer_load_dwordx4 v[168:171], v4, s[36:39], s90 offen offset:64\n"
"SH v[60:63],a[252:255],v[76:79]\n"
"AR v193,a1\n"
"AR v197,a5\n"
"SH v[52:55],a[252:255],v[72:75]\n"
"LD a[252:255],v2,22784\n"
"WL 4\n"
"SH v[40:43],a[232:235],v[100:103]\n"
"LD v[64:67],v3,128\n"
"AR v194,a2\n"
"SH v[32:35],a[232:235],v[96:99]\n"
"AR v198,a6\n"
"AR v195,a3\n"
"SH v[136:139],a[232:235],v[108:111]\n"
"AR v199,a7\n"
"v_permlane16_swap_b32 v192, v196\n"
"SH v[128:131],a[232:235],v[104:107]\n"
"LD a[232:235],v7,8192\n"
"SH v[44:47],a[236:239],v[100:103]\n"
"LD v[68:71],v3,144\n"
"v_permlane16_swap_b32_e64 v193, v197\n"
"SH v[36:39],a[236:239],v[96:99]\n"
"buffer_load_dwordx4 v[176:179], v4, s[36:39], s90 offen offset:128\n"
"SH v[140:143],a[236:239],v[108:111]\n"
"v_permlane16_swap_b32_e64 v194, v198\n"
"SH v[132:135],a[236:239],v[104:107]\n"
"LD a[236:239],v7,8448\n"
"WL 5\n"
"SH v[88:91],a[240:243],v[100:103]\n"
"v_permlane16_swap_b32 v195, v199\n"
"SH v[80:83],a[240:243],v[96:99]\n"
"AR v200,a8\n"
"AR v204,a12\n"
"SH v[152:155],a[240:243],v[108:111]\n"
"AR v201,a9\n"
"AR v205,a13\n"
"SH v[144:147],a[240:243],v[104:107]\n"
"LD a[240:243],v7,10240\n"
"SH v[92:95],a[244:247],v[100:103]\n"
"AR v202,a10\n"
"AR v206,a14\n"
"SH v[84:87],a[244:247],v[96:99]\n"
"AR v203,a11\n"
"AR v207,a15\n"
"SH v[156:159],a[244:247],v[108:111]\n"
"v_permlane16_swap_b32_e64 v200, v204\n"
"SH v[148:151],a[244:247],v[104:107]\n"
"LD a[244:247],v7,10496\n"
"SH v[120:123],a[248:251],v[100:103]\n"
"v_permlane16_swap_b32_e64 v201, v205\n"
"SH v[112:115],a[248:251],v[96:99]\n"
"v_permlane16_swap_b32_e64 v202, v206\n"
"SH v[56:59],a[248:251],v[108:111]\n"
"v_permlane16_swap_b32 v203, v207\n"
"SH v[48:51],a[248:251],v[104:107]\n"
"LD a[248:251],v7,12288\n"
"WL 4\n"
"SH v[124:127],a[252:255],v[100:103]\n"
"buffer_load_dwordx4 v[164:167], v4, s[36:39], s90 offen offset:3072\n"
"SH v[116:119],a[252:255],v[96:99]\n"
"buffer_load_dwordx4 v[172:175], v4, s[36:39], s90 offen offset:3136\n"
"SH v[60:63],a[252:255],v[108:111]\n"
"buffer_load_dwordx4 v[180:183], v4, s[36:39], s90 offen offset:3200\n"
"SH v[52:55],a[252:255],v[104:107]\n"
"LD a[252:255],v7,12544\n"
"v_mfma_f32_16x16x32_f16 v[216:219], a[232:235], v[196:199], v[208:211]\n"
"LD v[96:99],v3,256\n"
"LD v[100:103],v3,272\n"
"SH v[208:211],a[232:235],v[192:195]\n"
"WL 5\n"
"v_mfma_f32_16x16x32_f16 v[220:223], a[236:239], v[196:199], v[212:215]\n"
"SH v[212:215],a[236:239],v[192:195]\n"
"v_mfma_f32_16x16x32_f16 v[232:235], a[240:243], v[196:199], v[224:227]\n"
"SH v[224:227],a[240:243],v[192:195]\n"
"WL 4\n"
"v_mfma_f32_16x16x32_f16 v[236:239], a[244:247], v[196:199], v[228:231]\n"
"SH v[228:231],a[244:247],v[192:195]\n"
"WL 3\n"
"v_mfma_f32_16x16x32_f16 v[248:251], a[248:251], v[196:199], v[240:243]\n"
"SH v[240:243],a[248:251],v[192:195]\n"
"WL 2\n"
"v_mfma_f32_16x16x32_f16 v[252:255], a[252:255], v[196:199], v[244:247]\n"
"AR v196,a20\n"
"AR v197,a21\n"
"SH v[244:247],a[252:255],v[192:195]\n"
"AR v192,a16\n"
"AR v193,a17\n"
"v_mfma_f32_16x16x32_f16 v[24:27], a[232:235], v[204:207], v[16:19]\n"
"AR v194,a18\n"
"AR v198,a22\n"
"SH v[16:19],a[232:235],v[200:203]\n"
"LD a[232:235],v7,14336\n"
"AR v195,a19\n"
"v_mfma_f32_16x16x32_f16 v[28:31], a[236:239], v[204:207], v[20:23]\n"
"AR v199,a23\n"
"v_permlane16_swap_b32_e64 v192, v196\n"
"SH v[20:23],a[236:239],v[200:203]\n"
"LD a[236:239],v7,14592\n"
"v_permlane16_swap_b32_e64 v193, v197\n"
"v_mfma_f32_16x16x32_f16 v[72:75], a[240:243], v[204:207], v[64:67]\n"
"v_permlane16_swap_b32_e64 v194, v198\n"
"SH v[64:67],a[240:243],v[200:203]\n"
"LD a[240:243],v7,16384\n"
"v_permlane16_swap_b32 v195, v199\n"
"v_mfma_f32_16x16x32_f16 v[76:79], a[244:247], v[204:207], v[68:71]\n"
"CB v32,v32,v33\n"
"CB v40,v40,v41\n"
"SH v[68:71],a[244:247],v[200:203]\n"
"LD a[244:247],v7,16640\n"
"WL 4\n"
"v_mfma_f32_16x16x32_f16 v[104:107], a[248:251], v[204:207], v[96:99]\n"
"CB v33,v34,v35\n"
"SH v[96:99],a[248:251],v[200:203]\n"
"LD a[248:251],v7,18432\n"
"v_mfma_f32_16x16x32_f16 v[108:111], a[252:255], v[204:207], v[100:103]\n"
"AR v204,a28\n"
"AR v205,a29\n"
"SH v[100:103],a[252:255],v[200:203]\n"
"LD a[252:255],v7,18688\n"
"WL 5\n"
"SH v[216:219],a[232:235],v[196:199]\n"
"AR v200,a24\n"
"AR v201,a25\n"
"SH v[208:211],a[232:235],v[192:195]\n"
"AR v202,a26\n"
"WL 4\n"
"SH v[220:223],a[236:239],v[196:199]\n"
"AR v206,a30\n"
"AR v203,a27\n"
"SH v[212:215],a[236:239],v[192:195]\n"
"AR v207,a31\n"
"WL 3\n"
"SH v[232:235],a[240:243],v[196:199]\n"
"v_permlane16_swap_b32 v200, v204\n"
"SH v[224:227],a[240:243],v[192:195]\n"
"CB v41,v42,v43\n"
"WL 2\n"
"SH v[236:239],a[244:247],v[196:199]\n"
"v_permlane16_swap_b32 v201, v205\n"
"SH v[228:231],a[244:247],v[192:195]\n"
"CB v34,v36,v37\n"
"WL 1\n"
"SH v[248:251],a[248:251],v[196:199]\n"
"v_permlane16_swap_b32 v202, v206\n"
"SH v[240:243],a[248:251],v[192:195]\n"
"CB v42,v44,v45\n"
"WL 0\n"
"SH v[252:255],a[252:255],v[196:199]\n"
"v_permlane16_swap_b32 v203, v207\n"
"SH v[244:247],a[252:255],v[192:195]\n"
"AR v192,a32\n"
"AR v196,a36\n"
"SH v[24:27],a[232:235],v[204:207]\n"
"CB v35,v38,v39\n"
"AR v193,a33\n"
"SH v[16:19],a[232:235],v[200:203]\n"
"LD a[232:235],v7,20480\n"
"AR v197,a37\n"
"SH v[28:31],a[236:239],v[204:207]\n"
"CB v43,v46,v47\n"
"AR v194,a34\n"
"SH v[20:23],a[236:239],v[200:203]\n"
"LD a[236:239],v7,20736\n"
"AR v198,a38\n"
"SH v[72:75],a[240:243],v[204:207]\n"
"AR v195,a35\n"
"AR v199,a39\n"
"SH v[64:67],a[240:243],v[200:203]\n"
"LD a[240:243],v7,22528\n"
"s_add_u32 s29, s31, 4096\n"
"SH v[76:79],a[244:247],v[204:207]\n"
"buffer_load_dwordx4 a[0:3], v0, s[40:43], s29 offen offset:0\n"
"SH v[68:71],a[244:247],v[200:203]\n"
"LD a[244:247],v7,22784\n"
"v_permlane16_swap_b32_e64 v192, v196\n"
"SH v[104:107],a[248:251],v[204:207]\n"
"v_permlane16_swap_b32_e64 v193, v197\n"
"SH v[96:99],a[248:251],v[200:203]\n"
"LD a[248:251],v7,24576\n"
"v_permlane16_swap_b32_e64 v194, v198\n"
"SH v[108:111],a[252:255],v[204:207]\n"
"v_permlane16_swap_b32 v195, v199\n"
"SH v[100:103],a[252:255],v[200:203]\n"
"LD a[252:255],v7,24832\n"
"WL 5\n"
"SH v[216:219],a[232:235],v[196:199]\n"
"AR v200,a40\n"
"CB v80,v80,v81\n"
"SH v[208:211],a[232:235],v[192:195]\n"
"AR v204,a44\n"
"WL 4\n"
"SH v[220:223],a[236:239],v[196:199]\n"
"AR v201,a41\n"
"CB v88,v88,v89\n"
"SH v[212:215],a[236:239],v[192:195]\n"
"AR v205,a45\n"
"WL 3\n"
"SH v[232:235],a[240:243],v[196:199]\n"
"AR v202,a42\n"
"CB v81,v82,v83\n"
"SH v[224:227],a[240:243],v[192:195]\n"
"AR v206,a46\n"
"WL 2\n"
"SH v[236:239],a[244:247],v[196:199]\n"
"AR v203,a43\n"
"CB v89,v90,v91\n"
"SH v[228:231],a[244:247],v[192:195]\n"
"AR v207,a47\n"
"WL 1\n"
"SH v[248:251],a[248:251],v[196:199]\n"
"buffer_load_dwordx4 a[4:7], v0, s[40:43], s29 offen offset:1024\n"
"SH v[240:243],a[248:251],v[192:195]\n"
"v_permlane16_swap_b32 v200, v204\n"
"CB v82,v84,v85\n"
"WL 0\n"
"SH v[252:255],a[252:255],v[196:199]\n"
"v_permlane16_swap_b32_e64 v201, v205\n"
"SH v[244:247],a[252:255],v[192:195]\n"
"CB v90,v92,v93\n"
"v_permlane16_swap_b32_e64 v202, v206\n"
"CB v83,v86,v87\n"
"v_permlane16_swap_b32_e64 v203, v207\n"
"CB v91,v94,v95\n"
"AR v192,a48\n"
"SH v[24:27],a[232:235],v[204:207]\n"
"AR v196,a52\n"
"AR v193,a49\n"
"SH v[16:19],a[232:235],v[200:203]\n"
"LD a[232:235],v7,26624\n"
"AR v197,a53\n"
"s_waitcnt vmcnt(4)\n"
"v_smfmac_f32_32x32x32_bf16 v[208:223], v[8:11], v[160:167], v12\n"
"AR v194,a50\n"
"AR v198,a54\n"
"AR v195,a51\n"
"AR v199,a55\n"
"v_permlane16_swap_b32 v192, v196\n"
"SH v[28:31],a[236:239],v[204:207]\n"
"v_permlane16_swap_b32_e64 v193, v197\n"
"SH v[20:23],a[236:239],v[200:203]\n"
"LD a[236:239],v7,26880\n"
"CB v112,v112,v113\n"
"SH v[72:75],a[240:243],v[204:207]\n"
"v_permlane16_swap_b32 v194, v198\n"
"s_waitcnt vmcnt(3)\n"
"v_smfmac_f32_32x32x32_bf16 v[224:239], v[8:11], v[168:175], v12\n"
"CB v120,v120,v121\n"
"v_permlane16_swap_b32_e64 v195, v199\n"
"CB v113,v114,v115\n"
"CB v121,v122,v123\n"
"CB v114,v116,v117\n"
"SH v[64:67],a[240:243],v[200:203]\n"
"LD a[240:243],v7,28672\n"
"CB v122,v124,v125\n"
"SH v[76:79],a[244:247],v[204:207]\n"
"buffer_load_dwordx4 v[160:163], v4, s[36:39], s91 offen offset:0\n"
"SH v[68:71],a[244:247],v[200:203]\n"
"LD a[244:247],v7,28928\n"
"CB v115,v118,v119\n"
"s_waitcnt vmcnt(3)\n"
"v_smfmac_f32_32x32x32_bf16 v[240:255], v[8:11], v[176:183], v12\n"
"buffer_load_dwordx4 v[168:171], v4, s[36:39], s91 offen offset:64\n"
"buffer_load_dwordx4 v[176:179], v4, s[36:39], s91 offen offset:128\n"
"SH v[104:107],a[248:251],v[204:207]\n"
"CB v123,v126,v127\n"
"SH v[96:99],a[248:251],v[200:203]\n"
"LD a[248:251],v7,30720\n"
"SH v[108:111],a[252:255],v[204:207]\n"
"AR v204,a60\n"
"AR v205,a61\n"
"SH v[100:103],a[252:255],v[200:203]\n"
"LD a[252:255],v7,30976\n"
"WL 4\n"
"SH v[216:219],a[232:235],v[196:199]\n"
"AR v200,a56\n"
"AR v201,a57\n"
"SH v[208:211],a[232:235],v[192:195]\n"
"AR v202,a58\n"
"SH v[220:223],a[236:239],v[196:199]\n"
"AR v206,a62\n"
"AR v203,a59\n"
"SH v[212:215],a[236:239],v[192:195]\n"
"AR v207,a63\n"
"WL 3\n"
"SH v[232:235],a[240:243],v[196:199]\n"
"v_permlane16_swap_b32 v200, v204\n"
"SH v[224:227],a[240:243],v[192:195]\n"
"v_permlane16_swap_b32 v201, v205\n"
"WL 2\n"
"SH v[236:239],a[244:247],v[196:199]\n"
"v_permlane16_swap_b32_e64 v202, v206\n"
"SH v[228:231],a[244:247],v[192:195]\n"
"v_permlane16_swap_b32_e64 v203, v207\n"
"WL 1\n"
"SH v[248:251],a[248:251],v[196:199]\n"
"buffer_store_dwordx4 v[32:35], v4, s[40:43], s88 offen offset:0\n"
"SH v[240:243],a[248:251],v[192:195]\n"
"buffer_store_dwordx4 v[80:83], v4, s[40:43], s88 offen offset:64\n"
"WL 0\n"
"SH v[252:255],a[252:255],v[196:199]\n"
"AR v196,a68\n"
"AR v197,a69\n"
"SH v[244:247],a[252:255],v[192:195]\n"
"AR v192,a64\n"
"AR v193,a65\n"
"SH v[24:27],a[232:235],v[204:207]\n"
"AR v194,a66\n"
"AR v198,a70\n"
"SH v[16:19],a[232:235],v[200:203]\n"
"LD a[232:235],v7,32768\n"
"buffer_store_dwordx4 v[112:115], v4, s[40:43], s88 offen offset:128\n"
"SH v[28:31],a[236:239],v[204:207]\n"
"AR v195,a67\n"
"AR v199,a71\n"
"SH v[20:23],a[236:239],v[200:203]\n"
"LD a[236:239],v7,33024\n"
"buffer_load_dwordx4 v[164:167], v4, s[36:39], s91 offen offset:3072\n"
"SH v[72:75],a[240:243],v[204:207]\n"
"buffer_load_dwordx4 v[172:175], v4, s[36:39], s91 offen offset:3136\n"
"SH v[64:67],a[240:243],v[200:203]\n"
"LD a[240:243],v7,34816\n"
"buffer_load_dwordx4 v[180:183], v4, s[36:39], s91 offen offset:3200\n"
"SH v[76:79],a[244:247],v[204:207]\n"
"v_permlane16_swap_b32_e64 v192, v196\n"
"SH v[68:71],a[244:247],v[200:203]\n"
"LD a[244:247],v7,35072\n"
"v_permlane16_swap_b32_e64 v193, v197\n"
"SH v[104:107],a[248:251],v[204:207]\n"
"v_permlane16_swap_b32_e64 v194, v198\n"
"SH v[96:99],a[248:251],v[200:203]\n"
"LD a[248:251],v7,36864\n"
"v_permlane16_swap_b32_e64 v195, v199\n"
"SH v[108:111],a[252:255],v[204:207]\n"
"AR v204,a76\n"
"AR v205,a77\n"
"SH v[100:103],a[252:255],v[200:203]\n"
"LD a[252:255],v7,37120\n"
"WL 5\n"
"SH v[216:219],a[232:235],v[196:199]\n"
"AR v200,a72\n"
"AR v201,a73\n"
"SH v[208:211],a[232:235],v[192:195]\n"
"AR v202,a74\n"
"WL 4\n"
"SH v[220:223],a[236:239],v[196:199]\n"
"AR v206,a78\n"
"AR v203,a75\n"
"SH v[212:215],a[236:239],v[192:195]\n"
"AR v207,a79\n"
"WL 3\n"
"SH v[232:235],a[240:243],v[196:199]\n"
"v_permlane16_swap_b32 v200, v204\n"
"SH v[224:227],a[240:243],v[192:195]\n"
"v_permlane16_swap_b32 v201, v205\n"
"WL 2\n"
"SH v[236:239],a[244:247],v[196:199]\n"
"v_permlane16_swap_b32_e64 v202, v206\n"
"SH v[228:231],a[244:247],v[192:195]\n"
"v_permlane16_swap_b32_e64 v203, v207\n"
"WL 1\n"
"SH v[248:251],a[248:251],v[196:199]\n"
"SH v[240:243],a[248:251],v[192:195]\n"
"WL 0\n"
"SH v[252:255],a[252:255],v[196:199]\n"
"AR v196,a84\n"
"AR v197,a85\n"
"SH v[244:247],a[252:255],v[192:195]\n"
"AR v192,a80\n"
"AR v193,a81\n"
"SH v[24:27],a[232:235],v[204:207]\n"
"AR v194,a82\n"
"AR v198,a86\n"
"SH v[16:19],a[232:235],v[200:203]\n"
"LD a[232:235],v7,38912\n"
"AR v195,a83\n"
"SH v[28:31],a[236:239],v[204:207]\n"
"AR v199,a87\n"
"v_permlane16_swap_b32_e64 v192, v196\n"
"SH v[20:23],a[236:239],v[200:203]\n"
"LD a[236:239],v7,39168\n"
"v_permlane16_swap_b32_e64 v193, v197\n"
"SH v[72:75],a[240:243],v[204:207]\n"
"v_permlane16_swap_b32_e64 v194, v198\n"
"SH v[64:67],a[240:243],v[200:203]\n"
"LD a[240:243],v7,40960\n"
"s_add_u32 s29, s31, 98304\n"
"SH v[76:79],a[244:247],v[204:207]\n"
"buffer_load_dwordx4 a[32:35], v0, s[40:43], s29 offen offset:0\n"
"SH v[68:71],a[244:247],v[200:203]\n"
"LD a[244:247],v7,41216\n"
"v_permlane16_swap_b32_e64 v195, v199\n"
"SH v[104:107],a[248:251],v[204:207]\n"
"SH v[96:99],a[248:251],v[200:203]\n"
"LD a[248:251],v7,43008\n"
"SH v[108:111],a[252:255],v[204:207]\n"
"AR v204,a92\n"
"AR v205,a93\n"
"SH v[100:103],a[252:255],v[200:203]\n"
"LD a[252:255],v7,43264\n"
"WL 5\n"
"SH v[216:219],a[232:235],v[196:199]\n"
"AR v200,a88\n"
"AR v201,a89\n"
"SH v[208:211],a[232:235],v[192:195]\n"
"AR v202,a90\n"
"WL 4\n"
"SH v[220:223],a[236:239],v[196:199]\n"
"AR v206,a94\n"
"AR v203,a91\n"
"SH v[212:215],a[236:239],v[192:195]\n"
"AR v207,a95\n"
"WL 3\n"
"SH v[232:235],a[240:243],v[196:199]\n"
"v_permlane16_swap_b32 v200, v204\n"
"SH v[224:227],a[240:243],v[192:195]\n"
"v_permlane16_swap_b32 v201, v205\n"
"WL 2\n"
"SH v[236:239],a[244:247],v[196:199]\n"
"v_permlane16_swap_b32_e64 v202, v206\n"
"SH v[228:231],a[244:247],v[192:195]\n"
"v_permlane16_swap_b32_e64 v203, v207\n"
"WL 1\n"
"SH v[248:251],a[248:251],v[196:199]\n"
"buffer_load_dwordx4 a[36:39], v0, s[40:43], s29 offen offset:1024\n"
"SH v[240:243],a[248:251],v[192:195]\n"
"WL 0\n"
"SH v[252:255],a[252:255],v[196:199]\n"
"AR v196,a100\n"
"AR v197,a101\n"
"SH v[244:247],a[252:255],v[192:195]\n"
"AR v192,a96\n"
"AR v193,a97\n"
"SH v[24:27],a[232:235],v[204:207]\n"
"AR v194,a98\n"
"AR v198,a102\n"
"SH v[16:19],a[232:235],v[200:203]\n"
"LD a[232:235],v7,45056\n"
"AR v195,a99\n"
"SH v[28:31],a[236:239],v[204:207]\n"
"AR v199,a103\n"
"v_permlane16_swap_b32_e64 v192, v196\n"
"SH v[20:23],a[236:239],v[200:203]\n"
"LD a[236:239],v7,45312\n"
"v_permlane16_swap_b32_e64 v193, v197\n"
"SH v[72:75],a[240:243],v[204:207]\n"
"v_permlane16_swap_b32_e64 v194, v198\n"
"SH v[64:67],a[240:243],v[200:203]\n"
"LD a[240:243],v7,47104\n"
"v_permlane16_swap_b32_e64 v195, v199\n"
"SH v[76:79],a[244:247],v[204:207]\n"
"CB v128,v128,v129\n"
"SH v[68:71],a[244:247],v[200:203]\n"
"LD a[244:247],v7,47360\n"
"buffer_store_dwordx4 v[40:43], v4, s[40:43], s88 offen offset:3072\n"
"SH v[104:107],a[248:251],v[204:207]\n"
"CB v136,v136,v137\n"
"SH v[96:99],a[248:251],v[200:203]\n"
"LD a[248:251],v7,49152\n"
"buffer_store_dwordx4 v[88:91], v4, s[40:43], s88 offen offset:3136\n"
"SH v[108:111],a[252:255],v[204:207]\n"
"AR v204,a108\n"
"AR v205,a109\n"
"SH v[100:103],a[252:255],v[200:203]\n"
"LD a[252:255],v7,49408\n"
"WL 5\n"
"SH v[216:219],a[232:235],v[196:199]\n"
"buffer_store_dwordx4 v[120:123], v4, s[40:43], s88 offen offset:3200\n"
"SH v[208:211],a[232:235],v[192:195]\n"
"buffer_load_dword a80, v0, s[24:27], s28 offen\n"
"WL 4\n"
"SH v[220:223],a[236:239],v[196:199]\n"
"AR v200,a104\n"
"AR v201,a105\n"
"SH v[212:215],a[236:239],v[192:195]\n"
"AR v202,a106\n"
"WL 3\n"
"SH v[232:235],a[240:243],v[196:199]\n"
"AR v206,a110\n"
"AR v203,a107\n"
"SH v[224:227],a[240:243],v[192:195]\n"
"AR v207,a111\n"
"WL 2\n"
"SH v[236:239],a[244:247],v[196:199]\n"
"v_permlane16_swap_b32_e64 v200, v204\n"
"SH v[228:231],a[244:247],v[192:195]\n"
"CB v129,v130,v131\n"
"WL 1\n"
"SH v[248:251],a[248:251],v[196:199]\n"
"v_permlane16_swap_b32 v201, v205\n"
"SH v[240:243],a[248:251],v[192:195]\n"
"CB v137,v138,v139\n"
"WL 0\n"
"SH v[252:255],a[252:255],v[196:199]\n"
"v_permlane16_swap_b32 v202, v206\n"
"SH v[244:247],a[252:255],v[192:195]\n"
"s_add_u32 s29, s31, 100352\n"
"buffer_load_dwordx4 a[40:43], v0, s[40:43], s29 offen offset:0\n"
"v_permlane16_swap_b32_e64 v203, v207\n"
"CB v130,v132,v133\n"
"CB v138,v140,v141\n"
"SH v[24:27],a[232:235],v[204:207]\n"
"AR v192,a112\n"
"AR v196,a116\n"
"SH v[16:19],a[232:235],v[200:203]\n"
"LD a[232:235],v7,51200\n"
"CB v131,v134,v135\n"
"SH v[28:31],a[236:239],v[204:207]\n"
"AR v193,a113\n"
"AR v197,a117\n"
"SH v[20:23],a[236:239],v[200:203]\n"
"LD a[236:239],v7,51456\n"
"CB v139,v142,v143\n"
"SH v[72:75],a[240:243],v[204:207]\n"
"AR v194,a114\n"
"AR v198,a118\n"
"SH v[64:67],a[240:243],v[200:203]\n"
"LD a[240:243],v7,53248\n"
"AR v195,a115\n"
"SH v[76:79],a[244:247],v[204:207]\n"
"AR v199,a119\n"
"v_permlane16_swap_b32_e64 v192, v196\n"
"SH v[68:71],a[244:247],v[200:203]\n"
"LD a[244:247],v7,53504\n"
"v_permlane16_swap_b32_e64 v193, v197\n"
"SH v[104:107],a[248:251],v[204:207]\n"
"v_permlane16_swap_b32_e64 v194, v198\n"
"SH v[96:99],a[248:251],v[200:203]\n"
"LD a[248:251],v7,55296\n"
"CB v144,v144,v145\n"
"SH v[108:111],a[252:255],v[204:207]\n"
"v_permlane16_swap_b32 v195, v199\n"
"SH v[100:103],a[252:255],v[200:203]\n"
"LD a[252:255],v7,55552\n"
"WL 5\n"
"SH v[216:219],a[232:235],v[196:199]\n"
"AR v200,a120\n"
"AR v204,a124\n"
"SH v[208:211],a[232:235],v[192:195]\n"
"AR v201,a121\n"
"WL 4\n"
"SH v[220:223],a[236:239],v[196:199]\n"
"CB v152,v152,v153\n"
"AR v205,a125\n"
"SH v[212:215],a[236:239],v[192:195]\n"
"AR v202,a122\n"
"WL 3\n"
"SH v[232:235],a[240:243],v[196:199]\n"
"buffer_load_dwordx4 a[44:47], v0, s[40:43], s29 offen offset:1024\n"
"SH v[224:227],a[240:243],v[192:195]\n"
"CB v145,v146,v147\n"
"WL 2\n"
"SH v[236:239],a[244:247],v[196:199]\n"
"AR v206,a126\n"
"AR v203,a123\n"
"SH v[228:231],a[244:247],v[192:195]\n"
"CB v153,v154,v155\n"
"WL 1\n"
"SH v[248:251],a[248:251],v[196:199]\n"
"AR v207,a127\n"
"CB v146,v148,v149\n"
"SH v[240:243],a[248:251],v[192:195]\n"
"v_permlane16_swap_b32 v200, v204\n"
"WL 0\n"
"SH v[252:255],a[252:255],v[196:199]\n"
"v_permlane16_swap_b32_e64 v201, v205\n"
"SH v[244:247],a[252:255],v[192:195]\n"
"CB v154,v156,v157\n"
"v_permlane16_swap_b32_e64 v202, v206\n"
"CB v147,v150,v151\n"
"v_permlane16_swap_b32_e64 v203, v207\n"
"CB v155,v158,v159\n"
"AR v192,a128\n"
"SH v[24:27],a[232:235],v[204:207]\n"
"AR v196,a132\n"
"AR v193,a129\n"
"SH v[16:19],a[232:235],v[200:203]\n"
"LD a[232:235],v7,57344\n"
"AR v197,a133\n"
"SH v[28:31],a[236:239],v[204:207]\n"
"AR v194,a130\n"
"AR v198,a134\n"
"SH v[20:23],a[236:239],v[200:203]\n"
"LD a[236:239],v7,57600\n"
"AR v195,a131\n"
"SH v[72:75],a[240:243],v[204:207]\n"
"AR v199,a135\n"
"CB v48,v48,v49\n"
"SH v[64:67],a[240:243],v[200:203]\n"
"LD a[240:243],v7,59392\n"
"v_permlane16_swap_b32_e64 v192, v196\n"
"SH v[76:79],a[244:247],v[204:207]\n"
"v_permlane16_swap_b32_e64 v193, v197\n"
"SH v[68:71],a[244:247],v[200:203]\n"
"LD a[244:247],v7,59648\n"
"CB v56,v56,v57\n"
"SH v[104:107],a[248:251],v[204:207]\n"
"v_permlane16_swap_b32_e64 v194, v198\n"
"SH v[96:99],a[248:251],v[200:203]\n"
"LD a[248:251],v7,61440\n"
"CB v49,v50,v51\n"
"SH v[108:111],a[252:255],v[204:207]\n"
"v_permlane16_swap_b32 v195, v199\n"
"SH v[100:103],a[252:255],v[200:203]\n"
"LD a[252:255],v7,61696\n"
"WL 5\n"
"SH v[216:219],a[232:235],v[196:199]\n"
"CB v57,v58,v59\n"
"AR v200,a136\n"
"SH v[208:211],a[232:235],v[192:195]\n"
"AR v204,a140\n"
"WL 4\n"
"SH v[220:223],a[236:239],v[196:199]\n"
"s_add_u32 s29, s31, 102400\n"
"buffer_load_dwordx4 a[48:51], v0, s[40:43], s29 offen offset:0\n"
"SH v[212:215],a[236:239],v[192:195]\n"
"CB v50,v52,v53\n"
"WL 3\n"
"SH v[232:235],a[240:243],v[196:199]\n"
"AR v201,a137\n"
"AR v205,a141\n"
"SH v[224:227],a[240:243],v[192:195]\n"
"CB v58,v60,v61\n"
"AR v202,a138\n"
"WL 2\n"
"SH v[236:239],a[244:247],v[196:199]\n"
"AR v206,a142\n"
"AR v203,a139\n"
"SH v[228:231],a[244:247],v[192:195]\n"
"CB v51,v54,v55\n"
"WL 1\n"
"SH v[248:251],a[248:251],v[196:199]\n"
"AR v207,a143\n"
"CB v59,v62,v63\n"
"SH v[240:243],a[248:251],v[192:195]\n"
"v_permlane16_swap_b32 v200, v204\n"
"WL 0\n"
"SH v[252:255],a[252:255],v[196:199]\n"
"v_permlane16_swap_b32_e64 v201, v205\n"
"SH v[244:247],a[252:255],v[192:195]\n"
"v_permlane16_swap_b32 v202, v206\n"
"v_permlane16_swap_b32 v203, v207\n"
"AR v192,a144\n"
"AR v196,a148\n"
"SH v[24:27],a[232:235],v[204:207]\n"
"AR v193,a145\n"
"AR v197,a149\n"
"SH v[16:19],a[232:235],v[200:203]\n"
"LD a[232:235],v7,63488\n"
"buffer_store_dwordx4 v[128:131], v4, s[40:43], s89 offen offset:0\n"
"SH v[28:31],a[236:239],v[204:207]\n"
"buffer_store_dwordx4 v[144:147], v4, s[40:43], s89 offen offset:64\n"
"SH v[20:23],a[236:239],v[200:203]\n"
"LD a[236:239],v7,63744\n"
"buffer_store_dwordx4 v[48:51], v4, s[40:43], s89 offen offset:128\n"
"SH v[72:75],a[240:243],v[204:207]\n"
"AR v194,a146\n"
"AR v198,a150\n"
"SH v[64:67],a[240:243],v[200:203]\n"
"LD a[240:243],v2,8192\n"
"AR v195,a147\n"
"SH v[76:79],a[244:247],v[204:207]\n"
"AR v199,a151\n"
"v_permlane16_swap_b32_e64 v192, v196\n"
"SH v[68:71],a[244:247],v[200:203]\n"
"LD a[244:247],v2,8448\n"
"buffer_load_dwordx4 a[52:55], v0, s[40:43], s29 offen offset:1024\n"
"SH v[104:107],a[248:251],v[204:207]\n"
"v_permlane16_swap_b32_e64 v193, v197\n"
"SH v[96:99],a[248:251],v[200:203]\n"
"LD a[248:251],v2,10240\n"
"v_permlane16_swap_b32_e64 v194, v198\n"
"SH v[108:111],a[252:255],v[204:207]\n"
"v_permlane16_swap_b32 v195, v199\n"
"SH v[100:103],a[252:255],v[200:203]\n"
"LD a[252:255],v2,10496\n"
"WL 5\n"
"SH v[216:219],a[232:235],v[196:199]\n"
"LD v[32:35],v3,0\n"
"AR v200,a152\n"
"SH v[208:211],a[232:235],v[192:195]\n"
"AR v204,a156\n"
"AR v201,a153\n"
"s_waitcnt vmcnt(15)\n"
"v_smfmac_f32_32x32x32_bf16 v[16:31], v[8:11], v[160:167], v12\n"
"LD v[36:39],v3,16\n"
"AR v205,a157\n"
"AR v202,a154\n"
"AR v206,a158\n"
"AR v203,a155\n"
"WL 5\n"
"SH v[220:223],a[236:239],v[196:199]\n"
"AR v207,a159\n"
"v_permlane16_swap_b32_e64 v200, v204\n"
"SH v[212:215],a[236:239],v[192:195]\n"
"LD v[80:83],v3,128\n"
"SH v[232:235],a[240:243],v[196:199]\n"
"v_permlane16_swap_b32 v201, v205\n"
"s_waitcnt vmcnt(14)\n"
"v_smfmac_f32_32x32x32_bf16 v[64:79], v[8:11], v[168:175], v12\n"
"v_permlane16_swap_b32_e64 v202, v206\n"
"v_permlane16_swap_b32 v203, v207\n"
"SH v[224:227],a[240:243],v[192:195]\n"
"LD v[84:87],v3,144\n"
"WL 5\n"
"SH v[236:239],a[244:247],v[196:199]\n"
"SH v[228:231],a[244:247],v[192:195]\n"
"s_waitcnt vmcnt(13)\n"
"v_smfmac_f32_32x32x32_bf16 v[96:111], v[8:11], v[176:183], v12\n"
"SH v[248:251],a[248:251],v[196:199]\n"
"SH v[240:243],a[248:251],v[192:195]\n"
"WL 3\n"
"SH v[252:255],a[252:255],v[196:199]\n"
"AR v196,a164\n"
"AR v197,a165\n"
"SH v[244:247],a[252:255],v[192:195]\n"
"AR v192,a160\n"
"AR v193,a161\n"
"SH v[24:27],a[232:235],v[204:207]\n"
"AR v194,a162\n"
"AR v198,a166\n"
"SH v[16:19],a[232:235],v[200:203]\n"
"LD a[232:235],v2,12288\n"
"AR v195,a163\n"
"SH v[28:31],a[236:239],v[204:207]\n"
"AR v199,a167\n"
"v_permlane16_swap_b32_e64 v192, v196\n"
"SH v[20:23],a[236:239],v[200:203]\n"
"LD a[236:239],v2,12544\n"
"LD v[112:115],v3,256\n"
"SH v[72:75],a[240:243],v[204:207]\n"
"v_permlane16_swap_b32_e64 v193, v197\n"
"SH v[64:67],a[240:243],v[200:203]\n"
"LD a[240:243],v2,14336\n"
"LD v[116:119],v3,272\n"
"SH v[76:79],a[244:247],v[204:207]\n"
"s_add_u32 s22, s61, 0\n"
"buffer_load_dwordx4 a[56:59], v0, s[40:43], s22 offen offset:0\n"
"SH v[68:71],a[244:247],v[200:203]\n"
"LD a[244:247],v2,14592\n"
"v_permlane16_swap_b32 v194, v198\n"
"SH v[104:107],a[248:251],v[204:207]\n"
"v_permlane16_swap_b32_e64 v195, v199\n"
"SH v[96:99],a[248:251],v[200:203]\n"
"LD a[248:251],v2,16384\n"
"SH v[108:111],a[252:255],v[204:207]\n"
"AR v204,a172\n"
"AR v205,a173\n"
"SH v[100:103],a[252:255],v[200:203]\n"
"LD a[252:255],v2,16640\n"
"WL 7\n"
"SH v[216:219],a[232:235],v[196:199]\n"
"AR v200,a168\n"
"AR v201,a169\n"
"SH v[208:211],a[232:235],v[192:195]\n"
"AR v202,a170\n"
"WL 6\n"
"SH v[220:223],a[236:239],v[196:199]\n"
"AR v206,a174\n"
"AR v203,a171\n"
"SH v[212:215],a[236:239],v[192:195]\n"
"AR v207,a175\n"
"WL 4\n"
"SH v[232:235],a[240:243],v[196:199]\n"
"buffer_load_dwordx4 a[60:63], v0, s[40:43], s22 offen offset:1024\n"
"SH v[224:227],a[240:243],v[192:195]\n"
"v_permlane16_swap_b32 v200, v204\n"
"WL 2\n"
"SH v[236:239],a[244:247],v[196:199]\n"
"v_permlane16_swap_b32 v201, v205\n"
"SH v[228:231],a[244:247],v[192:195]\n"
"LD a[8:11],v1,0\n"
"WL 2\n"
"SH v[248:251],a[248:251],v[196:199]\n"
"v_permlane16_swap_b32 v202, v206\n"
"SH v[240:243],a[248:251],v[192:195]\n"
"v_permlane16_swap_b32 v203, v207\n"
"WL 1\n"
"SH v[252:255],a[252:255],v[196:199]\n"
"AR v196,a180\n"
"AR v197,a181\n"
"SH v[244:247],a[252:255],v[192:195]\n"
"AR v192,a176\n"
"AR v193,a177\n"
"SH v[24:27],a[232:235],v[204:207]\n"
"AR v194,a178\n"
"AR v198,a182\n"
"SH v[16:19],a[232:235],v[200:203]\n"
"LD a[232:235],v2,18432\n"
"LD a[12:15],v1,256\n"
"SH v[28:31],a[236:239],v[204:207]\n"
"AR v195,a179\n"
"AR v199,a183\n"
"SH v[20:23],a[236:239],v[200:203]\n"
"LD a[236:239],v2,18688\n"
"LD a[16:19],v1,2048\n"
"SH v[72:75],a[240:243],v[204:207]\n"
"v_permlane16_swap_b32_e64 v192, v196\n"
"SH v[64:67],a[240:243],v[200:203]\n"
"LD a[240:243],v2,20480\n"
"LD a[20:23],v1,2304\n"
"SH v[76:79],a[244:247],v[204:207]\n"
"v_permlane16_swap_b32_e64 v193, v197\n"
"SH v[68:71],a[244:247],v[200:203]\n"
"LD a[244:247],v2,20736\n"
"LD a[24:27],v1,4096\n"
"SH v[104:107],a[248:251],v[204:207]\n"
"v_permlane16_swap_b32_e64 v194, v198\n"
"SH v[96:99],a[248:251],v[200:203]\n"
"LD a[248:251],v2,22528\n"
"LD a[28:31],v1,4352\n"
"SH v[108:111],a[252:255],v[204:207]\n"
"s_add_u32 s22, s61, 2048\n"
"buffer_load_dwordx4 a[64:67], v0, s[40:43], s22 offen offset:0\n"
"SH v[100:103],a[252:255],v[200:203]\n"
"LD a[252:255],v2,22784\n"
"v_permlane16_swap_b32 v195, v199\n"
"AR v200,a184\n"
"WL 10\n"
"SH v[216:219],a[232:235],v[196:199]\n"
"buffer_store_dwordx4 v[136:139], v4, s[40:43], s89 offen offset:3072\n"
"SH v[208:211],a[232:235],v[192:195]\n"
"buffer_store_dwordx4 v[152:155], v4, s[40:43], s89 offen offset:3136\n"
"WL 8\n"
"SH v[220:223],a[236:239],v[196:199]\n"
"buffer_store_dwordx4 v[56:59], v4, s[40:43], s89 offen offset:3200\n"
"SH v[212:215],a[236:239],v[192:195]\n"
"AR v204,a188\n"
"WL 6\n"
"SH v[232:235],a[240:243],v[196:199]\n"
"AR v201,a185\n"
"AR v205,a189\n"
"SH v[224:227],a[240:243],v[192:195]\n"
"AR v202,a186\n"
"WL 4\n"
"SH v[236:239],a[244:247],v[196:199]\n"
"LD v[128:131],v3,0\n"
"AR v206,a190\n"
"SH v[228:231],a[244:247],v[192:195]\n"
"AR v203,a187\n"
"WL 3\n"
"SH v[248:251],a[248:251],v[196:199]\n"
"LD v[132:135],v3,16\n"
"AR v207,a191\n"
"SH v[240:243],a[248:251],v[192:195]\n"
"buffer_load_dwordx4 a[68:71], v0, s[40:43], s22 offen offset:1024\n"
"WL 2\n"
"SH v[252:255],a[252:255],v[196:199]\n"
"LD v[144:147],v3,128\n"
"v_permlane16_swap_b32 v200, v204\n"
"SH v[244:247],a[252:255],v[192:195]\n"
"v_permlane16_swap_b32_e64 v201, v205\n"
"SZ v[160:163],a[8:11],a[192:195]\n"
"LD v[148:151],v3,144\n"
"v_permlane16_swap_b32_e64 v202, v206\n"
"SZ v[168:171],a[8:11],a[196:199]\n"
"v_permlane16_swap_b32_e64 v203, v207\n"
"SZ v[164:167],a[12:15],a[192:195]\n"
"LD v[48:51],v3,256\n"
"SZ v[172:175],a[12:15],a[196:199]\n"
"SB v[160:163],a[16:19],a[200:203]\n"
"SB v[168:171],a[16:19],a[204:207]\n"
"SB v[164:167],a[20:23],a[200:203]\n"
"SB v[172:175],a[20:23],a[204:207]\n"
"SB v[160:163],a[24:27],a[208:211]\n"
"SB v[168:171],a[24:27],a[212:215]\n"
"SB v[164:167],a[28:31],a[208:211]\n"
"SB v[172:175],a[28:31],a[212:215]\n"
"SH v[24:27],a[232:235],v[204:207]\n"
"SH v[16:19],a[232:235],v[200:203]\n"
"LD a[232:235],v7,8192\n"
"SH v[28:31],a[236:239],v[204:207]\n"
"s_nop 0\n"
"s_nop 0\n"
"GA v160\n"
"SH v[20:23],a[236:239],v[200:203]\n"
"GL v160\n"
"GA v161\n"
"s_add_u32 s22, s61, 4096\n"
"GH v161\n"
"LD v[52:55],v3,272\n"
"SH v[72:75],a[240:243],v[204:207]\n"
"LD a[236:239],v7,8448\n"
"buffer_load_dwordx4 a[72:75], v0, s[40:43], s22 offen offset:0\n"
"CH v166,v166,v167\n"
"CH v174,v174,v175\n"
"SH v[64:67],a[240:243],v[200:203]\n"
"PF v167,v166,v166,s56\n"
"GA v162\n"
"GA v163\n"
"SH v[76:79],a[244:247],v[204:207]\n"
"GL v162\n"
"GH v163\n"
"LD a[240:243],v7,10240\n"
"SH v[68:71],a[244:247],v[200:203]\n"
"PF v175,v174,v174,s56\n"
"PM v167,v167,v166\n"
"PM v175,v175,v174\n"
"SH v[104:107],a[248:251],v[204:207]\n"
"GA v164\n"
"GA v165\n"
"EX v167,0\n"
"SH v[96:99],a[248:251],v[200:203]\n"
"GL v164\n"
"GH v165\n"
"LD a[244:247],v7,10496\n"
"EX v175,0\n"
"SH v[108:111],a[252:255],v[204:207]\n"
"GA v168\n"
"GA v169\n"
"SH v[100:103],a[252:255],v[200:203]\n"
"GL v168\n"
"GH v169\n"
"SZ v[176:179],a[8:11],a[216:219]\n"
"LD a[248:251],v7,12288\n"
"LD a[252:255],v7,12544\n"
"SZ v[184:187],a[8:11],a[220:223]\n"
"EX v167,1\n"
"GA v170\n"
"GA v171\n"
"EX v175,1\n"
"PF v167,v167,s57,s57\n"
"SZ v[180:183],a[12:15],a[216:219]\n"
"GL v170\n"
"GH v171\n"
"LD a[8:11],v1,6144\n"
"SZ v[188:191],a[12:15],a[220:223]\n"
"GA v172\n"
"GA v173\n"
"SB v[176:179],a[16:19],a[224:227]\n"
"GL v172\n"
"GH v173\n"
"SB v[184:187],a[16:19],a[228:231]\n"
"LD a[12:15],v1,6400\n"
"LD a[16:19],v1,8192\n"
"SB v[180:183],a[20:23],a[224:227]\n"
"buffer_load_dwordx4 a[76:79], v0, s[40:43], s22 offen offset:1024\n"
"SB v[188:191],a[20:23],a[228:231]\n"
"LD a[20:23],v1,8448\n"
"PF v175,v175,s57,s57\n"
"SB v[176:179],a[24:27],a[0:3]\n"
"RC v167,0\n"
"SB v[184:187],a[24:27],a[4:7]\n"
"LD a[24:27],v1,10240\n"
"RC v175,0\n"
"SB v[180:183],a[28:31],a[0:3]\n"
"RC v167,1\n"
"RC v175,1\n"
"SB v[188:191],a[28:31],a[4:7]\n"
"LD a[28:31],v1,10496\n"
"PM v175,v174,v175\n"
"PM v167,v166,v167\n"
"WL 5\n"
"v_or_b32 v174, v172, v173\n"
"v_or_b32 v173, v170, v171\n"
"v_or_b32 v172, v168, v169\n"
"v_or_b32 v166, v164, v165\n"
"v_or_b32 v165, v162, v163\n"
"v_mfma_f32_16x16x32_f16 v[40:43], a[232:235], v[172:175], v[32:35]\n"
"v_or_b32_e64 v164, v160, v161\n"
"GA v176\n"
"GA v177\n"
"SH v[32:35],a[232:235],v[164:167]\n"
"GL v176\n"
"GH v177\n"
"v_mfma_f32_16x16x32_f16 v[44:47], a[236:239], v[172:175], v[36:39]\n"
"CH v182,v182,v183\n"
"CH v190,v190,v191\n"
"SH v[36:39],a[236:239],v[164:167]\n"
"PF v183,v182,v182,s56\n"
"GA v178\n"
"v_mfma_f32_16x16x32_f16 v[88:91], a[240:243], v[172:175], v[80:83]\n"
"GL v178\n"
"GA v179\n"
"SH v[80:83],a[240:243],v[164:167]\n"
"GH v179\n"
"PF v191,v190,v190,s56\n"
"v_mfma_f32_16x16x32_f16 v[92:95], a[244:247], v[172:175], v[84:87]\n"
"PM v183,v183,v182\n"
"s_add_u32 s22, s61, 98304\n"
"SH v[84:87],a[244:247],v[164:167]\n"
"buffer_load_dwordx4 a[88:91], v0, s[40:43], s22 offen offset:0\n"
"v_mfma_f32_16x16x32_f16 v[120:123], a[248:251], v[172:175], v[112:115]\n"
"PM v191,v191,v190\n"
"GA v180\n"
"GA v181\n"
"SH v[112:115],a[248:251],v[164:167]\n"
"GL v180\n"
"GH v181\n"
"EX v183,0\n"
"v_mfma_f32_16x16x32_f16 v[124:127], a[252:255], v[172:175], v[116:119]\n"
"EX v191,0\n"
"SH v[116:119],a[252:255],v[164:167]\n"
"GA v184\n"
"GA v185\n"
"SZ v[160:163],a[8:11],a[192:195]\n"
"GL v184\n"
"GH v185\n"
"EX v183,1\n"
"SZ v[168:171],a[8:11],a[196:199]\n"
"GA v186\n"
"GA v187\n"
"EX v191,1\n"
"CB v208,v208,v209\n"
"WL 5\n"
"s_nop 0\n"
"SZ v[164:167],a[12:15],a[192:195]\n"
"GL v186\n"
"GH v187\n"
"PF v183,v183,s57,s57\n"
"SZ v[172:175],a[12:15],a[196:199]\n"
"GA v188\n"
"SB v[160:163],a[16:19],a[200:203]\n"
"GL v188\n"
"GA v189\n"
"SB v[168:171],a[16:19],a[204:207]\n"
"GH v189\n"
"SB v[164:167],a[20:23],a[200:203]\n"
"PF v191,v191,s57,s57\n"
"CB v216,v216,v217\n"
"SB v[172:175],a[20:23],a[204:207]\n"
"buffer_load_dword a81, v0, s[24:27], s28 offen\n"
"SB v[160:163],a[24:27],a[208:211]\n"
"buffer_load_dwordx4 a[92:95], v0, s[40:43], s22 offen offset:1024\n"
"SB v[168:171],a[24:27],a[212:215]\n"
"RC v183,0\n"
"RC v191,0\n"
"SB v[164:167],a[28:31],a[208:211]\n"
"RC v183,1\n"
"CB v209,v210,v211\n"
"RC v191,1\n"
"CB v217,v218,v219\n"
"SB v[172:175],a[28:31],a[212:215]\n"
"PM v191,v190,v191\n"
"PM v183,v182,v183\n"
"WL 0\n"
"v_or_b32 v190, v188, v189\n"
"v_or_b32 v189, v186, v187\n"
"v_or_b32 v188, v184, v185\n"
"v_or_b32 v182, v180, v181\n"
"v_or_b32 v181, v178, v179\n"
"v_mfma_f32_16x16x32_f16 v[136:139], a[232:235], v[188:191], v[128:131]\n"
"v_or_b32_e64 v180, v176, v177\n"
"CB v210,v212,v213\n"
"GA v160\n"
"SH v[128:131],a[232:235],v[180:183]\n"
"LD a[232:235],v7,14336\n"
"GL v160\n"
"v_mfma_f32_16x16x32_f16 v[140:143], a[236:239], v[188:191], v[132:135]\n"
"GA v161\n"
"CH v166,v166,v167\n"
"SH v[132:135],a[236:239],v[180:183]\n"
"GH v161\n"
"LD a[236:239],v7,14592\n"
"v_mfma_f32_16x16x32_f16 v[152:155], a[240:243], v[188:191], v[144:147]\n"
"CB v218,v220,v221\n"
"CH v174,v174,v175\n"
"SH v[144:147],a[240:243],v[180:183]\n"
"PF v167,v166,v166,s56\n"
"GA v162\n"
"GA v163\n"
"PF v175,v174,v174,s56\n"
"v_mfma_f32_16x16x32_f16 v[156:159], a[244:247], v[188:191], v[148:151]\n"
"GL v162\n"
"GH v163\n"
"LD a[240:243],v7,16384\n"
"SH v[148:151],a[244:247],v[180:183]\n"
"PM v167,v167,v166\n"
"CB v211,v214,v215\n"
"PM v175,v175,v174\n"
"v_mfma_f32_16x16x32_f16 v[56:59], a[248:251], v[188:191], v[48:51]\n"
"GA v164\n"
"GA v165\n"
"EX v167,0\n"
"CB v219,v222,v223\n"
"SH v[48:51],a[248:251],v[180:183]\n"
"GL v164\n"
"GH v165\n"
"LD a[244:247],v7,16640\n"
"EX v175,0\n"
"v_mfma_f32_16x16x32_f16 v[60:63], a[252:255], v[188:191], v[52:55]\n"
"GA v168\n"
"GA v169\n"
"SH v[52:55],a[252:255],v[180:183]\n"
"GL v168\n"
"GH v169\n"
"SZ v[176:179],a[8:11],a[216:219]\n"
"LD a[248:251],v7,18432\n"
"LD a[252:255],v7,18688\n"
"SZ v[184:187],a[8:11],a[220:223]\n"
"EX v167,1\n"
"GA v170\n"
"GA v171\n"
"EX v175,1\n"
"PF v167,v167,s57,s57\n"
"SZ v[180:183],a[12:15],a[216:219]\n"
"GL v170\n"
"GH v171\n"
"LD a[8:11],v1,12288\n"
"SZ v[188:191],a[12:15],a[220:223]\n"
"GA v172\n"
"GA v173\n"
"SB v[176:179],a[16:19],a[224:227]\n"
"GL v172\n"
"GH v173\n"
"SB v[184:187],a[16:19],a[228:231]\n"
"LD a[12:15],v1,12544\n"
"LD a[16:19],v1,14336\n"
"SB v[180:183],a[20:23],a[224:227]\n"
"PF v175,v175,s57,s57\n"
"RC v167,0\n"
"SB v[188:191],a[20:23],a[228:231]\n"
"LD a[20:23],v1,14592\n"
"RC v175,0\n"
"SB v[176:179],a[24:27],a[0:3]\n"
"RC v167,1\n"
"SB v[184:187],a[24:27],a[4:7]\n"
"LD a[24:27],v1,16384\n"
"PM v167,v166,v167\n"
"SB v[180:183],a[28:31],a[0:3]\n"
"RC v175,1\n"
"SB v[188:191],a[28:31],a[4:7]\n"
"LD a[28:31],v1,16640\n"
"PM v175,v174,v175\n"
"WL 5\n"
"v_or_b32 v174, v172, v173\n"
"v_or_b32 v173, v170, v171\n"
"v_or_b32 v172, v168, v169\n"
"v_or_b32 v166, v164, v165\n"
"v_or_b32 v165, v162, v163\n"
"SH v[40:43],a[232:235],v[172:175]\n"
"v_or_b32_e64 v164, v160, v161\n"
"GA v176\n"
"GA v177\n"
"SH v[32:35],a[232:235],v[164:167]\n"
"GL v176\n"
"GH v177\n"
"SH v[44:47],a[236:239],v[172:175]\n"
"CH v182,v182,v183\n"
"CH v190,v190,v191\n"
"SH v[36:39],a[236:239],v[164:167]\n"
"PF v183,v182,v182,s56\n"
"GA v178\n"
"SH v[88:91],a[240:243],v[172:175]\n"
"GL v178\n"
"GA v179\n"
"SH v[80:83],a[240:243],v[164:167]\n"
"GH v179\n"
"CB v224,v224,v225\n"
"SH v[92:95],a[244:247],v[172:175]\n"
"PF v191,v190,v190,s56\n"
"PM v183,v183,v182\n"
"SH v[84:87],a[244:247],v[164:167]\n"
"PM v191,v191,v190\n"
"GA v180\n"
"SH v[120:123],a[248:251],v[172:175]\n"
"GL v180\n"
"GA v181\n"
"SH v[112:115],a[248:251],v[164:167]\n"
"GH v181\n"
"CB v232,v232,v233\n"
"EX v183,0\n"
"EX v191,0\n"
"SH v[124:127],a[252:255],v[172:175]\n"
"GA v184\n"
"GA v185\n"
"SH v[116:119],a[252:255],v[164:167]\n"
"GL v184\n"
"GH v185\n"
"SZ v[160:163],a[8:11],a[192:195]\n"
"CB v225,v226,v227\n"
"EX v183,1\n"
"SZ v[168:171],a[8:11],a[196:199]\n"
"GA v186\n"
"GA v187\n"
"EX v191,1\n"
"CB v233,v234,v235\n"
"WL 5\n"
"s_nop 0\n"
"SZ v[164:167],a[12:15],a[192:195]\n"
"GL v186\n"
"GH v187\n"
"PF v183,v183,s57,s57\n"
"GA v188\n"
"SZ v[172:175],a[12:15],a[196:199]\n"
"GL v188\n"
"SB v[160:163],a[16:19],a[200:203]\n"
"buffer_load_dwordx4 a[96:99], v4, s[36:39], s92 offen offset:0\n"
"SB v[168:171],a[16:19],a[204:207]\n"
"buffer_load_dwordx4 a[104:107], v4, s[36:39], s92 offen offset:64\n"
"buffer_load_dwordx4 a[112:115], v4, s[36:39], s92 offen offset:128\n"
"GA v189\n"
"PF v191,v191,s57,s57\n"
"SB v[164:167],a[20:23],a[200:203]\n"
"GH v189\n"
"CB v226,v228,v229\n"
"SB v[172:175],a[20:23],a[204:207]\n"
"CB v234,v236,v237\n"
"SB v[160:163],a[24:27],a[208:211]\n"
"RC v183,0\n"
"SB v[168:171],a[24:27],a[212:215]\n"
"CB v227,v230,v231\n"
"SB v[164:167],a[28:31],a[208:211]\n"
"RC v191,0\n"
"RC v183,1\n"
"RC v191,1\n"
"SB v[172:175],a[28:31],a[212:215]\n"
"PM v191,v190,v191\n"
"PM v183,v182,v183\n"
"WL 0\n"
"v_or_b32 v190, v188, v189\n"
"v_or_b32 v189, v186, v187\n"
"v_or_b32 v188, v184, v185\n"
"v_or_b32 v182, v180, v181\n"
"v_or_b32 v181, v178, v179\n"
"SH v[136:139],a[232:235],v[188:191]\n"
"CB v235,v238,v239\n"
"v_or_b32_e64 v180, v176, v177\n"
"GA v160\n"
"GA v161\n"
"SH v[128:131],a[232:235],v[180:183]\n"
"LD a[232:235],v7,20480\n"
"GL v160\n"
"SH v[140:143],a[236:239],v[188:191]\n"
"GH v161\n"
"CH v166,v166,v167\n"
"SH v[132:135],a[236:239],v[180:183]\n"
"LD a[236:239],v7,20736\n"
"CH v174,v174,v175\n"
"SH v[152:155],a[240:243],v[188:191]\n"
"PF v167,v166,v166,s56\n"
"GA v162\n"
"SH v[144:147],a[240:243],v[180:183]\n"
"GL v162\n"
"GA v163\n"
"SH v[156:159],a[244:247],v[188:191]\n"
"GH v163\n"
"LD a[240:243],v7,22528\n"
"SH v[148:151],a[244:247],v[180:183]\n"
"PF v175,v174,v174,s56\n"
"PM v167,v167,v166\n"
"PM v175,v175,v174\n"
"SH v[56:59],a[248:251],v[188:191]\n"
"GA v164\n"
"GA v165\n"
"EX v167,0\n"
"SH v[48:51],a[248:251],v[180:183]\n"
"GL v164\n"
"GH v165\n"
"LD a[244:247],v7,22784\n"
"EX v175,0\n"
"SH v[60:63],a[252:255],v[188:191]\n"
"GA v168\n"
"GA v169\n"
"SH v[52:55],a[252:255],v[180:183]\n"
"GL v168\n"
"GH v169\n"
"SZ v[176:179],a[8:11],a[216:219]\n"
"LD a[248:251],v7,24576\n"
"LD a[252:255],v7,24832\n"
"SZ v[184:187],a[8:11],a[220:223]\n"
"EX v167,1\n"
"GA v170\n"
"GA v171\n"
"EX v175,1\n"
"PF v167,v167,s57,s57\n"
"SZ v[180:183],a[12:15],a[216:219]\n"
"GL v170\n"
"GH v171\n"
"LD a[8:11],v1,18432\n"
"SZ v[188:191],a[12:15],a[220:223]\n"
"GA v172\n"
"GA v173\n"
"SB v[176:179],a[16:19],a[224:227]\n"
"GL v172\n"
"GH v173\n"
"SB v[184:187],a[16:19],a[228:231]\n"
"LD a[12:15],v1,18688\n"
"LD a[16:19],v1,20480\n"
"SB v[180:183],a[20:23],a[224:227]\n"
"PF v175,v175,s57,s57\n"
"CB v240,v240,v241\n"
"SB v[188:191],a[20:23],a[228:231]\n"
"LD a[20:23],v1,20736\n"
"RC v167,0\n"
"SB v[176:179],a[24:27],a[0:3]\n"
"RC v175,0\n"
"SB v[184:187],a[24:27],a[4:7]\n"
"LD a[24:27],v1,22528\n"
"CB v248,v248,v249\n"
"SB v[180:183],a[28:31],a[0:3]\n"
"RC v167,1\n"
"RC v175,1\n"
"SB v[188:191],a[28:31],a[4:7]\n"
"LD a[28:31],v1,22784\n"
"PM v175,v174,v175\n"
"PM v167,v166,v167\n"
"WL 5\n"
"v_or_b32 v174, v172, v173\n"
"v_or_b32 v173, v170, v171\n"
"v_or_b32 v172, v168, v169\n"
"v_or_b32 v166, v164, v165\n"
"v_or_b32 v165, v162, v163\n"
"SH v[40:43],a[232:235],v[172:175]\n"
"v_or_b32_e64 v164, v160, v161\n"
"CB v241,v242,v243\n"
"GA v176\n"
"SH v[32:35],a[232:235],v[164:167]\n"
"GL v176\n"
"GA v177\n"
"SH v[44:47],a[236:239],v[172:175]\n"
"GH v177\n"
"CH v182,v182,v183\n"
"SH v[36:39],a[236:239],v[164:167]\n"
"CH v190,v190,v191\n"
"CB v249,v250,v251\n"
"SH v[88:91],a[240:243],v[172:175]\n"
"PF v183,v182,v182,s56\n"
"GA v178\n"
"SH v[80:83],a[240:243],v[164:167]\n"
"GL v178\n"
"GA v179\n"
"SH v[92:95],a[244:247],v[172:175]\n"
"GH v179\n"
"PF v191,v190,v190,s56\n"
"SH v[84:87],a[244:247],v[164:167]\n"
"PM v183,v183,v182\n"
"PM v191,v191,v190\n"
"SH v[120:123],a[248:251],v[172:175]\n"
"GA v180\n"
"GA v181\n"
"CB v242,v244,v245\n"
"EX v183,0\n"
"SH v[112:115],a[248:251],v[164:167]\n"
"GL v180\n"
"GH v181\n"
"EX v191,0\n"
"SH v[124:127],a[252:255],v[172:175]\n"
"GA v184\n"
"GA v185\n"
"SH v[116:119],a[252:255],v[164:167]\n"
"GL v184\n"
"GH v185\n"
"SZ v[160:163],a[8:11],a[192:195]\n"
"CB v250,v252,v253\n"
"EX v183,1\n"
"SZ v[168:171],a[8:11],a[196:199]\n"
"GA v186\n"
"GA v187\n"
"EX v191,1\n"
"CB v243,v246,v247\n"
"WL 5\n"
"s_nop 0\n"
"SZ v[164:167],a[12:15],a[192:195]\n"
"GL v186\n"
"GH v187\n"
"PF v183,v183,s57,s57\n"
"GA v188\n"
"SZ v[172:175],a[12:15],a[196:199]\n"
"GL v188\n"
"SB v[160:163],a[16:19],a[200:203]\n"
"GA v189\n"
"PF v191,v191,s57,s57\n"
"SB v[168:171],a[16:19],a[204:207]\n"
"GH v189\n"
"SB v[164:167],a[20:23],a[200:203]\n"
"CB v251,v254,v255\n"
"RC v183,0\n"
"SB v[172:175],a[20:23],a[204:207]\n"
"RC v191,0\n"
"SB v[160:163],a[24:27],a[208:211]\n"
"RC v183,1\n"
"SB v[168:171],a[24:27],a[212:215]\n"
"PM v183,v182,v183\n"
"SB v[164:167],a[28:31],a[208:211]\n"
"RC v191,1\n"
"SB v[172:175],a[28:31],a[212:215]\n"
"PM v191,v190,v191\n"
"WL 0\n"
"v_or_b32 v190, v188, v189\n"
"v_or_b32 v189, v186, v187\n"
"v_or_b32 v188, v184, v185\n"
"v_or_b32 v182, v180, v181\n"
"v_or_b32 v181, v178, v179\n"
"SH v[136:139],a[232:235],v[188:191]\n"
"v_or_b32_e64 v180, v176, v177\n"
"GA v160\n"
"GA v161\n"
"SH v[128:131],a[232:235],v[180:183]\n"
"LD a[232:235],v7,26624\n"
"GL v160\n"
"SH v[140:143],a[236:239],v[188:191]\n"
"GH v161\n"
"CH v166,v166,v167\n"
"SH v[132:135],a[236:239],v[180:183]\n"
"LD a[236:239],v7,26880\n"
"CH v174,v174,v175\n"
"SH v[152:155],a[240:243],v[188:191]\n"
"PF v167,v166,v166,s56\n"
"GA v162\n"
"SH v[144:147],a[240:243],v[180:183]\n"
"GL v162\n"
"GA v163\n"
"SH v[156:159],a[244:247],v[188:191]\n"
"GH v163\n"
"LD a[240:243],v7,28672\n"
"SH v[148:151],a[244:247],v[180:183]\n"
"buffer_load_dwordx4 a[100:103], v4, s[36:39], s92 offen offset:3072\n"
"buffer_load_dwordx4 a[108:111], v4, s[36:39], s92 offen offset:3136\n"
"buffer_load_dwordx4 a[116:119], v4, s[36:39], s92 offen offset:3200\n"
"PF v175,v174,v174,s56\n"
"PM v167,v167,v166\n"
"PM v175,v175,v174\n"
"SH v[56:59],a[248:251],v[188:191]\n"
"GA v164\n"
"GA v165\n"
"EX v167,0\n"
"SH v[48:51],a[248:251],v[180:183]\n"
"GL v164\n"
"GH v165\n"
"LD a[244:247],v7,28928\n"
"EX v175,0\n"
"SH v[60:63],a[252:255],v[188:191]\n"
"GA v168\n"
"GA v169\n"
"SH v[52:55],a[252:255],v[180:183]\n"
"GL v168\n"
"GH v169\n"
"SZ v[176:179],a[8:11],a[216:219]\n"
"LD a[248:251],v7,30720\n"
"LD a[252:255],v7,30976\n"
"SZ v[184:187],a[8:11],a[220:223]\n"
"buffer_store_dwordx4 v[208:211], v4, s[40:43], s90 offen offset:0 sc1\n"
"buffer_store_dwordx4 v[224:227], v4, s[40:43], s90 offen offset:64 sc1\n"
"buffer_store_dwordx4 v[240:243], v4, s[40:43], s90 offen offset:128 sc1\n"
"EX v167,1\n"
"GA v170\n"
"GA v171\n"
"EX v175,1\n"
"PF v167,v167,s57,s57\n"
"SZ v[180:183],a[12:15],a[216:219]\n"
"GL v170\n"
"GH v171\n"
"LD a[8:11],v1,24576\n"
"SZ v[188:191],a[12:15],a[220:223]\n"
"GA v172\n"
"GA v173\n"
"SB v[176:179],a[16:19],a[224:227]\n"
"GL v172\n"
"GH v173\n"
"SB v[184:187],a[16:19],a[228:231]\n"
"LD a[12:15],v1,24832\n"
"LD a[16:19],v1,26624\n"
"SB v[180:183],a[20:23],a[224:227]\n"
"PF v175,v175,s57,s57\n"
"RC v167,0\n"
"SB v[188:191],a[20:23],a[228:231]\n"
"LD a[20:23],v1,26880\n"
"RC v175,0\n"
"SB v[176:179],a[24:27],a[0:3]\n"
"RC v167,1\n"
"SB v[184:187],a[24:27],a[4:7]\n"
"LD a[24:27],v1,28672\n"
"PM v167,v166,v167\n"
"SB v[180:183],a[28:31],a[0:3]\n"
"RC v175,1\n"
"SB v[188:191],a[28:31],a[4:7]\n"
"LD a[28:31],v1,28928\n"
"PM v175,v174,v175\n"
"WL 5\n"
"v_or_b32 v174, v172, v173\n"
"v_or_b32 v173, v170, v171\n"
"v_or_b32 v172, v168, v169\n"
"v_or_b32 v166, v164, v165\n"
"v_or_b32 v165, v162, v163\n"
"SH v[40:43],a[232:235],v[172:175]\n"
"v_or_b32_e64 v164, v160, v161\n"
"GA v176\n"
"GA v177\n"
"SH v[32:35],a[232:235],v[164:167]\n"
"GL v176\n"
"GH v177\n"
"SH v[44:47],a[236:239],v[172:175]\n"
"CH v182,v182,v183\n"
"CH v190,v190,v191\n"
"SH v[36:39],a[236:239],v[164:167]\n"
"PF v183,v182,v182,s56\n"
"GA v178\n"
"SH v[88:91],a[240:243],v[172:175]\n"
"GL v178\n"
"GA v179\n"
"SH v[80:83],a[240:243],v[164:167]\n"
"GH v179\n"
"PF v191,v190,v190,s56\n"
"SH v[92:95],a[244:247],v[172:175]\n"
"PM v183,v183,v182\n"
"PM v191,v191,v190\n"
"SH v[84:87],a[244:247],v[164:167]\n"
"GA v180\n"
"GA v181\n"
"SH v[120:123],a[248:251],v[172:175]\n"
"GL v180\n"
"GH v181\n"
"SH v[112:115],a[248:251],v[164:167]\n"
"EX v183,0\n"
"SH v[124:127],a[252:255],v[172:175]\n"
"EX v191,0\n"
"SH v[116:119],a[252:255],v[164:167]\n"
"GA v184\n"
"GA v185\n"
"SZ v[160:163],a[8:11],a[192:195]\n"
"GL v184\n"
"GH v185\n"
"SZ v[168:171],a[8:11],a[196:199]\n"
"EX v183,1\n"
"GA v186\n"
"GA v187\n"
"EX v191,1\n"
"WL 6\n"
"s_nop 0\n"
"SZ v[164:167],a[12:15],a[192:195]\n"
"GL v186\n"
"GH v187\n"
"PF v183,v183,s57,s57\n"
"SZ v[172:175],a[12:15],a[196:199]\n"
"GA v188\n"
"SB v[160:163],a[16:19],a[200:203]\n"
"GL v188\n"
"GA v189\n"
"SB v[168:171],a[16:19],a[204:207]\n"
"GH v189\n"
"SB v[164:167],a[20:23],a[200:203]\n"
"PF v191,v191,s57,s57\n"
"RC v183,0\n"
"SB v[172:175],a[20:23],a[204:207]\n"
"RC v191,0\n"
"SB v[160:163],a[24:27],a[208:211]\n"
"RC v183,1\n"
"SB v[168:171],a[24:27],a[212:215]\n"
"PM v183,v182,v183\n"
"SB v[164:167],a[28:31],a[208:211]\n"
"RC v191,1\n"
"SB v[172:175],a[28:31],a[212:215]\n"
"PM v191,v190,v191\n"
"WL 0\n"
"v_or_b32 v190, v188, v189\n"
"v_or_b32 v189, v186, v187\n"
"v_or_b32 v188, v184, v185\n"
"v_or_b32 v182, v180, v181\n"
"v_or_b32 v181, v178, v179\n"
"SH v[136:139],a[232:235],v[188:191]\n"
"v_or_b32_e64 v180, v176, v177\n"
"GA v160\n"
"GA v161\n"
"SH v[128:131],a[232:235],v[180:183]\n"
"LD a[232:235],v7,32768\n"
"GL v160\n"
"SH v[140:143],a[236:239],v[188:191]\n"
"GH v161\n"
"CH v166,v166,v167\n"
"SH v[132:135],a[236:239],v[180:183]\n"
"LD a[236:239],v7,33024\n"
"CH v174,v174,v175\n"
"SH v[152:155],a[240:243],v[188:191]\n"
"PF v167,v166,v166,s56\n"
"GA v162\n"
"SH v[144:147],a[240:243],v[180:183]\n"
"GL v162\n"
"GA v163\n"
"SH v[156:159],a[244:247],v[188:191]\n"
"GH v163\n"
"LD a[240:243],v7,34816\n"
"SH v[148:151],a[244:247],v[180:183]\n"
"PF v175,v174,v174,s56\n"
"PM v167,v167,v166\n"
"PM v175,v175,v174\n"
"SH v[56:59],a[248:251],v[188:191]\n"
"GA v164\n"
"GA v165\n"
"EX v167,0\n"
"SH v[48:51],a[248:251],v[180:183]\n"
"GL v164\n"
"GH v165\n"
"LD a[244:247],v7,35072\n"
"EX v175,0\n"
"SH v[60:63],a[252:255],v[188:191]\n"
"GA v168\n"
"GA v169\n"
"SH v[52:55],a[252:255],v[180:183]\n"
"GL v168\n"
"GH v169\n"
"SZ v[176:179],a[8:11],a[216:219]\n"
"LD a[248:251],v7,36864\n"
"LD a[252:255],v7,37120\n"
"SZ v[184:187],a[8:11],a[220:223]\n"
"EX v167,1\n"
"GA v170\n"
"GA v171\n"
"EX v175,1\n"
"PF v167,v167,s57,s57\n"
"SZ v[180:183],a[12:15],a[216:219]\n"
"GL v170\n"
"GH v171\n"
"LD a[8:11],v1,30720\n"
"SZ v[188:191],a[12:15],a[220:223]\n"
"GA v172\n"
"GA v173\n"
"SB v[176:179],a[16:19],a[224:227]\n"
"GL v172\n"
"GH v173\n"
"SB v[184:187],a[16:19],a[228:231]\n"
"LD a[12:15],v1,30976\n"
"LD a[16:19],v1,32768\n"
"SB v[180:183],a[20:23],a[224:227]\n"
"PF v175,v175,s57,s57\n"
"RC v167,0\n"
"SB v[188:191],a[20:23],a[228:231]\n"
"LD a[20:23],v1,33024\n"
"RC v175,0\n"
"SB v[176:179],a[24:27],a[0:3]\n"
"RC v167,1\n"
"SB v[184:187],a[24:27],a[4:7]\n"
"LD a[24:27],v1,34816\n"
"buffer_load_dwordx4 a[120:123], v4, s[36:39], s93 offen offset:0\n"
"SB v[180:183],a[28:31],a[0:3]\n"
"buffer_load_dwordx4 a[128:131], v4, s[36:39], s93 offen offset:64\n"
"buffer_load_dwordx4 a[136:139], v4, s[36:39], s93 offen offset:128\n"
"RC v175,1\n"
"SB v[188:191],a[28:31],a[4:7]\n"
"LD a[28:31],v1,35072\n"
"PM v175,v174,v175\n"
"PM v167,v166,v167\n"
"WL 5\n"
"v_or_b32 v174, v172, v173\n"
"v_or_b32 v173, v170, v171\n"
"v_or_b32 v172, v168, v169\n"
"v_or_b32 v166, v164, v165\n"
"v_or_b32 v165, v162, v163\n"
"SH v[40:43],a[232:235],v[172:175]\n"
"v_or_b32_e64 v164, v160, v161\n"
"GA v176\n"
"GA v177\n"
"SH v[32:35],a[232:235],v[164:167]\n"
"GL v176\n"
"GH v177\n"
"SH v[44:47],a[236:239],v[172:175]\n"
"CH v182,v182,v183\n"
"CH v190,v190,v191\n"
"SH v[36:39],a[236:239],v[164:167]\n"
"PF v183,v182,v182,s56\n"
"GA v178\n"
"SH v[88:91],a[240:243],v[172:175]\n"
"GL v178\n"
"GA v179\n"
"SH v[80:83],a[240:243],v[164:167]\n"
"GH v179\n"
"PF v191,v190,v190,s56\n"
"SH v[92:95],a[244:247],v[172:175]\n"
"PM v183,v183,v182\n"
"PM v191,v191,v190\n"
"SH v[84:87],a[244:247],v[164:167]\n"
"GA v180\n"
"GA v181\n"
"SH v[120:123],a[248:251],v[172:175]\n"
"GL v180\n"
"GH v181\n"
"SH v[112:115],a[248:251],v[164:167]\n"
"EX v183,0\n"
"SH v[124:127],a[252:255],v[172:175]\n"
"EX v191,0\n"
"SH v[116:119],a[252:255],v[164:167]\n"
"GA v184\n"
"GA v185\n"
"SZ v[160:163],a[8:11],a[192:195]\n"
"GL v184\n"
"GH v185\n"
"SZ v[168:171],a[8:11],a[196:199]\n"
"EX v183,1\n"
"GA v186\n"
"GA v187\n"
"EX v191,1\n"
"WL 6\n"
"s_nop 0\n"
"SZ v[164:167],a[12:15],a[192:195]\n"
"GL v186\n"
"GH v187\n"
"PF v183,v183,s57,s57\n"
"SZ v[172:175],a[12:15],a[196:199]\n"
"GA v188\n"
"SB v[160:163],a[16:19],a[200:203]\n"
"GL v188\n"
"GA v189\n"
"SB v[168:171],a[16:19],a[204:207]\n"
"GH v189\n"
"SB v[164:167],a[20:23],a[200:203]\n"
"PF v191,v191,s57,s57\n"
"RC v183,0\n"
"SB v[172:175],a[20:23],a[204:207]\n"
"RC v191,0\n"
"SB v[160:163],a[24:27],a[208:211]\n"
"RC v183,1\n"
"SB v[168:171],a[24:27],a[212:215]\n"
"PM v183,v182,v183\n"
"SB v[164:167],a[28:31],a[208:211]\n"
"RC v191,1\n"
"SB v[172:175],a[28:31],a[212:215]\n"
"PM v191,v190,v191\n"
"WL 0\n"
"v_or_b32 v190, v188, v189\n"
"v_or_b32 v189, v186, v187\n"
"v_or_b32 v188, v184, v185\n"
"v_or_b32 v182, v180, v181\n"
"v_or_b32 v181, v178, v179\n"
"SH v[136:139],a[232:235],v[188:191]\n"
"v_or_b32_e64 v180, v176, v177\n"
"GA v160\n"
"GA v161\n"
"SH v[128:131],a[232:235],v[180:183]\n"
"LD a[232:235],v7,38912\n"
"GL v160\n"
"SH v[140:143],a[236:239],v[188:191]\n"
"GH v161\n"
"CH v166,v166,v167\n"
"SH v[132:135],a[236:239],v[180:183]\n"
"LD a[236:239],v7,39168\n"
"CH v174,v174,v175\n"
"SH v[152:155],a[240:243],v[188:191]\n"
"PF v167,v166,v166,s56\n"
"GA v162\n"
"SH v[144:147],a[240:243],v[180:183]\n"
"GL v162\n"
"GA v163\n"
"SH v[156:159],a[244:247],v[188:191]\n"
"GH v163\n"
"LD a[240:243],v7,40960\n"
"SH v[148:151],a[244:247],v[180:183]\n"
"PF v175,v174,v174,s56\n"
"PM v167,v167,v166\n"
"PM v175,v175,v174\n"
"SH v[56:59],a[248:251],v[188:191]\n"
"GA v164\n"
"GA v165\n"
"EX v167,0\n"
"SH v[48:51],a[248:251],v[180:183]\n"
"GL v164\n"
"GH v165\n"
"LD a[244:247],v7,41216\n"
"EX v175,0\n"
"SH v[60:63],a[252:255],v[188:191]\n"
"GA v168\n"
"GA v169\n"
"SH v[52:55],a[252:255],v[180:183]\n"
"GL v168\n"
"GH v169\n"
"SZ v[176:179],a[8:11],a[216:219]\n"
"LD a[248:251],v7,43008\n"
"LD a[252:255],v7,43264\n"
"SZ v[184:187],a[8:11],a[220:223]\n"
"EX v167,1\n"
"GA v170\n"
"GA v171\n"
"EX v175,1\n"
"PF v167,v167,s57,s57\n"
"SZ v[180:183],a[12:15],a[216:219]\n"
"GL v170\n"
"GH v171\n"
"LD a[8:11],v1,36864\n"
"SZ v[188:191],a[12:15],a[220:223]\n"
"GA v172\n"
"GA v173\n"
"SB v[176:179],a[16:19],a[224:227]\n"
"GL v172\n"
"GH v173\n"
"SB v[184:187],a[16:19],a[228:231]\n"
"LD a[12:15],v1,37120\n"
"LD a[16:19],v1,38912\n"
"SB v[180:183],a[20:23],a[224:227]\n"
"PF v175,v175,s57,s57\n"
"RC v167,0\n"
"SB v[188:191],a[20:23],a[228:231]\n"
"LD a[20:23],v1,39168\n"
"RC v175,0\n"
"SB v[176:179],a[24:27],a[0:3]\n"
"RC v167,1\n"
"SB v[184:187],a[24:27],a[4:7]\n"
"LD a[24:27],v1,40960\n"
"PM v167,v166,v167\n"
"SB v[180:183],a[28:31],a[0:3]\n"
"RC v175,1\n"
"SB v[188:191],a[28:31],a[4:7]\n"
"LD a[28:31],v1,41216\n"
"PM v175,v174,v175\n"
"WL 5\n"
"v_or_b32 v174, v172, v173\n"
"v_or_b32 v173, v170, v171\n"
"v_or_b32 v172, v168, v169\n"
"v_or_b32 v166, v164, v165\n"
"v_or_b32 v165, v162, v163\n"
"SH v[40:43],a[232:235],v[172:175]\n"
"v_or_b32_e64 v164, v160, v161\n"
"GA v176\n"
"GA v177\n"
"SH v[32:35],a[232:235],v[164:167]\n"
"GL v176\n"
"GH v177\n"
"SH v[44:47],a[236:239],v[172:175]\n"
"CH v182,v182,v183\n"
"CH v190,v190,v191\n"
"SH v[36:39],a[236:239],v[164:167]\n"
"PF v183,v182,v182,s56\n"
"GA v178\n"
"SH v[88:91],a[240:243],v[172:175]\n"
"GL v178\n"
"GA v179\n"
"SH v[80:83],a[240:243],v[164:167]\n"
"GH v179\n"
"PF v191,v190,v190,s56\n"
"SH v[92:95],a[244:247],v[172:175]\n"
"PM v183,v183,v182\n"
"PM v191,v191,v190\n"
"SH v[84:87],a[244:247],v[164:167]\n"
"GA v180\n"
"GA v181\n"
"SH v[120:123],a[248:251],v[172:175]\n"
"GL v180\n"
"GH v181\n"
"SH v[112:115],a[248:251],v[164:167]\n"
"buffer_store_dwordx4 v[216:219], v4, s[40:43], s90 offen offset:3072 sc1\n"
"buffer_store_dwordx4 v[232:235], v4, s[40:43], s90 offen offset:3136 sc1\n"
"buffer_store_dwordx4 v[248:251], v4, s[40:43], s90 offen offset:3200 sc1\n"
"EX v183,0\n"
"SH v[124:127],a[252:255],v[172:175]\n"
"EX v191,0\n"
"SH v[116:119],a[252:255],v[164:167]\n"
"GA v184\n"
"GA v185\n"
"SZ v[160:163],a[8:11],a[192:195]\n"
"GL v184\n"
"GH v185\n"
"SZ v[168:171],a[8:11],a[196:199]\n"
"EX v183,1\n"
"GA v186\n"
"GA v187\n"
"EX v191,1\n"
"WL 5\n"
"s_nop 0\n"
"SZ v[164:167],a[12:15],a[192:195]\n"
"GL v186\n"
"GH v187\n"
"buffer_load_dwordx4 a[124:127], v4, s[36:39], s93 offen offset:3072\n"
"buffer_load_dwordx4 a[132:135], v4, s[36:39], s93 offen offset:3136\n"
"buffer_load_dwordx4 a[140:143], v4, s[36:39], s93 offen offset:3200\n"
"PF v183,v183,s57,s57\n"
"GA v188\n"
"SZ v[172:175],a[12:15],a[196:199]\n"
"GL v188\n"
"SB v[160:163],a[16:19],a[200:203]\n"
"GA v189\n"
"PF v191,v191,s57,s57\n"
"SB v[168:171],a[16:19],a[204:207]\n"
"GH v189\n"
"SB v[164:167],a[20:23],a[200:203]\n"
"buffer_load_dword a82, v0, s[24:27], s28 offen\n"
"SB v[172:175],a[20:23],a[204:207]\n"
"CB v16,v16,v17\n"
"SB v[160:163],a[24:27],a[208:211]\n"
"RC v183,0\n"
"SB v[168:171],a[24:27],a[212:215]\n"
"RC v191,0\n"
"SB v[164:167],a[28:31],a[208:211]\n"
"CB v24,v24,v25\n"
"RC v183,1\n"
"RC v191,1\n"
"CB v17,v18,v19\n"
"PM v191,v190,v191\n"
"SB v[172:175],a[28:31],a[212:215]\n"
"PM v183,v182,v183\n"
"WL 0\n"
"v_or_b32 v190, v188, v189\n"
"v_or_b32 v189, v186, v187\n"
"v_or_b32 v188, v184, v185\n"
"v_or_b32_e64 v182, v180, v181\n"
"CB v25,v26,v27\n"
"SH v[136:139],a[232:235],v[188:191]\n"
"v_or_b32 v181, v178, v179\n"
"v_or_b32 v180, v176, v177\n"
"GA v160\n"
"GA v161\n"
"SH v[128:131],a[232:235],v[180:183]\n"
"LD a[232:235],v7,45056\n"
"GL v160\n"
"SH v[140:143],a[236:239],v[188:191]\n"
"GH v161\n"
"CB v18,v20,v21\n"
"SH v[132:135],a[236:239],v[180:183]\n"
"LD a[236:239],v7,45312\n"
"CH v166,v166,v167\n"
"SH v[152:155],a[240:243],v[188:191]\n"
"CH v174,v174,v175\n"
"PF v167,v166,v166,s56\n"
"SH v[144:147],a[240:243],v[180:183]\n"
"GA v162\n"
"GA v163\n"
"PF v175,v174,v174,s56\n"
"SH v[156:159],a[244:247],v[188:191]\n"
"GL v162\n"
"GH v163\n"
"LD a[240:243],v7,47104\n"
"SH v[148:151],a[244:247],v[180:183]\n"
"CB v26,v28,v29\n"
"PM v167,v167,v166\n"
"PM v175,v175,v174\n"
"GA v164\n"
"SH v[56:59],a[248:251],v[188:191]\n"
"GL v164\n"
"GA v165\n"
"EX v167,0\n"
"SH v[48:51],a[248:251],v[180:183]\n"
"GH v165\n"
"LD a[244:247],v7,47360\n"
"CB v19,v22,v23\n"
"EX v175,0\n"
"SH v[60:63],a[252:255],v[188:191]\n"
"GA v168\n"
"GA v169\n"
"SH v[52:55],a[252:255],v[180:183]\n"
"GL v168\n"
"GH v169\n"
"LD a[248:251],v7,49152\n"
"SZ v[176:179],a[8:11],a[216:219]\n"
"LD a[252:255],v7,49408\n"
"EX v167,1\n"
"SZ v[184:187],a[8:11],a[220:223]\n"
"CB v27,v30,v31\n"
"GA v170\n"
"GA v171\n"
"EX v175,1\n"
"PF v167,v167,s57,s57\n"
"SZ v[180:183],a[12:15],a[216:219]\n"
"GL v170\n"
"GH v171\n"
"LD a[8:11],v1,43008\n"
"SZ v[188:191],a[12:15],a[220:223]\n"
"GA v172\n"
"GA v173\n"
"SB v[176:179],a[16:19],a[224:227]\n"
"GL v172\n"
"GH v173\n"
"SB v[184:187],a[16:19],a[228:231]\n"
"LD a[12:15],v1,43264\n"
"LD a[16:19],v1,45056\n"
"SB v[180:183],a[20:23],a[224:227]\n"
"PF v175,v175,s57,s57\n"
"RC v167,0\n"
"SB v[188:191],a[20:23],a[228:231]\n"
"LD a[20:23],v1,45312\n"
"RC v175,0\n"
"SB v[176:179],a[24:27],a[0:3]\n"
"RC v167,1\n"
"SB v[184:187],a[24:27],a[4:7]\n"
"LD a[24:27],v1,47104\n"
"PM v167,v166,v167\n"
"SB v[180:183],a[28:31],a[0:3]\n"
"RC v175,1\n"
"SB v[188:191],a[28:31],a[4:7]\n"
"LD a[28:31],v1,47360\n"
"PM v175,v174,v175\n"
"WL 5\n"
"v_or_b32 v174, v172, v173\n"
"v_or_b32 v173, v170, v171\n"
"v_or_b32 v172, v168, v169\n"
"v_or_b32 v166, v164, v165\n"
"v_or_b32 v165, v162, v163\n"
"SH v[40:43],a[232:235],v[172:175]\n"
"v_or_b32_e64 v164, v160, v161\n"
"GA v176\n"
"GA v177\n"
"SH v[32:35],a[232:235],v[164:167]\n"
"GL v176\n"
"GH v177\n"
"SH v[44:47],a[236:239],v[172:175]\n"
"CH v182,v182,v183\n"
"CH v190,v190,v191\n"
"SH v[36:39],a[236:239],v[164:167]\n"
"PF v183,v182,v182,s56\n"
"GA v178\n"
"SH v[88:91],a[240:243],v[172:175]\n"
"GL v178\n"
"GA v179\n"
"SH v[80:83],a[240:243],v[164:167]\n"
"GH v179\n"
"PF v191,v190,v190,s56\n"
"SH v[92:95],a[244:247],v[172:175]\n"
"PM v183,v183,v182\n"
"CB v64,v64,v65\n"
"SH v[84:87],a[244:247],v[164:167]\n"
"PM v191,v191,v190\n"
"GA v180\n"
"SH v[120:123],a[248:251],v[172:175]\n"
"GL v180\n"
"GA v181\n"
"SH v[112:115],a[248:251],v[164:167]\n"
"GH v181\n"
"EX v183,0\n"
"EX v191,0\n"
"SH v[124:127],a[252:255],v[172:175]\n"
"GA v184\n"
"CB v72,v72,v73\n"
"SH v[116:119],a[252:255],v[164:167]\n"
"GL v184\n"
"GA v185\n"
"SZ v[160:163],a[8:11],a[192:195]\n"
"GH v185\n"
"EX v183,1\n"
"SZ v[168:171],a[8:11],a[196:199]\n"
"GA v186\n"
"GA v187\n"
"CB v65,v66,v67\n"
"EX v191,1\n"
"WL 5\n"
"s_nop 0\n"
"SZ v[164:167],a[12:15],a[192:195]\n"
"GL v186\n"
"GH v187\n"
"PF v183,v183,s57,s57\n"
"GA v188\n"
"SZ v[172:175],a[12:15],a[196:199]\n"
"GL v188\n"
"SB v[160:163],a[16:19],a[200:203]\n"
"GA v189\n"
"PF v191,v191,s57,s57\n"
"SB v[168:171],a[16:19],a[204:207]\n"
"GH v189\n"
"SB v[164:167],a[20:23],a[200:203]\n"
"CB v73,v74,v75\n"
"CB v66,v68,v69\n"
"SB v[172:175],a[20:23],a[204:207]\n"
"CB v74,v76,v77\n"
"SB v[160:163],a[24:27],a[208:211]\n"
"RC v183,0\n"
"SB v[168:171],a[24:27],a[212:215]\n"
"RC v191,0\n"
"SB v[164:167],a[28:31],a[208:211]\n"
"RC v183,1\n"
"RC v191,1\n"
"PM v183,v182,v183\n"
"SB v[172:175],a[28:31],a[212:215]\n"
"PM v191,v190,v191\n"
"WL 0\n"
"v_or_b32 v190, v188, v189\n"
"v_or_b32_e64 v189, v186, v187\n"
"CB v67,v70,v71\n"
"v_or_b32 v188, v184, v185\n"
"v_or_b32 v182, v180, v181\n"
"v_or_b32_e64 v181, v178, v179\n"
"SH v[136:139],a[232:235],v[188:191]\n"
"v_or_b32_e64 v180, v176, v177\n"
"GA v160\n"
"GA v161\n"
"SH v[128:131],a[232:235],v[180:183]\n"
"LD a[232:235],v7,51200\n"
"GL v160\n"
"SH v[140:143],a[236:239],v[188:191]\n"
"GH v161\n"
"buffer_load_dwordx4 a[144:147], v4, s[36:39], s94 offen offset:0\n"
"SH v[132:135],a[236:239],v[180:183]\n"
"LD a[236:239],v7,51456\n"
"buffer_load_dwordx4 a[152:155], v4, s[36:39], s94 offen offset:64\n"
"SH v[152:155],a[240:243],v[188:191]\n"
"buffer_load_dwordx4 a[160:163], v4, s[36:39], s94 offen offset:128\n"
"CB v75,v78,v79\n"
"CH v166,v166,v167\n"
"CH v174,v174,v175\n"
"SH v[144:147],a[240:243],v[180:183]\n"
"PF v167,v166,v166,s56\n"
"GA v162\n"
"GA v163\n"
"SH v[156:159],a[244:247],v[188:191]\n"
"GL v162\n"
"GH v163\n"
"LD a[240:243],v7,53248\n"
"SH v[148:151],a[244:247],v[180:183]\n"
"PF v175,v174,v174,s56\n"
"PM v167,v167,v166\n"
"PM v175,v175,v174\n"
"SH v[56:59],a[248:251],v[188:191]\n"
"GA v164\n"
"GA v165\n"
"EX v167,0\n"
"SH v[48:51],a[248:251],v[180:183]\n"
"GL v164\n"
"GH v165\n"
"LD a[244:247],v7,53504\n"
"EX v175,0\n"
"SH v[60:63],a[252:255],v[188:191]\n"
"GA v168\n"
"GA v169\n"
"SH v[52:55],a[252:255],v[180:183]\n"
"GL v168\n"
"GH v169\n"
"SZ v[176:179],a[8:11],a[216:219]\n"
"LD a[248:251],v7,55296\n"
"LD a[252:255],v7,55552\n"
"SZ v[184:187],a[8:11],a[220:223]\n"
"EX v167,1\n"
"GA v170\n"
"GA v171\n"
"EX v175,1\n"
"PF v167,v167,s57,s57\n"
"SZ v[180:183],a[12:15],a[216:219]\n"
"GL v170\n"
"GH v171\n"
"LD a[8:11],v1,49152\n"
"SZ v[188:191],a[12:15],a[220:223]\n"
"GA v172\n"
"GA v173\n"
"SB v[176:179],a[16:19],a[224:227]\n"
"GL v172\n"
"GH v173\n"
"SB v[184:187],a[16:19],a[228:231]\n"
"LD a[12:15],v1,49408\n"
"LD a[16:19],v1,51200\n"
"SB v[180:183],a[20:23],a[224:227]\n"
"PF v175,v175,s57,s57\n"
"RC v167,0\n"
"SB v[188:191],a[20:23],a[228:231]\n"
"LD a[20:23],v1,51456\n"
"CB v96,v96,v97\n"
"SB v[176:179],a[24:27],a[0:3]\n"
"RC v175,0\n"
"SB v[184:187],a[24:27],a[4:7]\n"
"LD a[24:27],v1,53248\n"
"RC v167,1\n"
"SB v[180:183],a[28:31],a[0:3]\n"
"RC v175,1\n"
"SB v[188:191],a[28:31],a[4:7]\n"
"LD a[28:31],v1,53504\n"
"PM v175,v174,v175\n"
"PM v167,v166,v167\n"
"WL 5\n"
"v_or_b32 v174, v172, v173\n"
"v_or_b32 v173, v170, v171\n"
"v_or_b32 v172, v168, v169\n"
"v_or_b32 v166, v164, v165\n"
"v_or_b32 v165, v162, v163\n"
"SH v[40:43],a[232:235],v[172:175]\n"
"v_or_b32_e64 v164, v160, v161\n"
"CB v104,v104,v105\n"
"GA v176\n"
"SH v[32:35],a[232:235],v[164:167]\n"
"GL v176\n"
"GA v177\n"
"SH v[44:47],a[236:239],v[172:175]\n"
"GH v177\n"
"CH v182,v182,v183\n"
"SH v[36:39],a[236:239],v[164:167]\n"
"CB v97,v98,v99\n"
"CH v190,v190,v191\n"
"SH v[88:91],a[240:243],v[172:175]\n"
"PF v183,v182,v182,s56\n"
"GA v178\n"
"SH v[80:83],a[240:243],v[164:167]\n"
"GL v178\n"
"GA v179\n"
"SH v[92:95],a[244:247],v[172:175]\n"
"GH v179\n"
"PF v191,v190,v190,s56\n"
"SH v[84:87],a[244:247],v[164:167]\n"
"PM v183,v183,v182\n"
"CB v105,v106,v107\n"
"SH v[120:123],a[248:251],v[172:175]\n"
"PM v191,v191,v190\n"
"GA v180\n"
"GA v181\n"
"EX v183,0\n"
"SH v[112:115],a[248:251],v[164:167]\n"
"GL v180\n"
"GH v181\n"
"CB v98,v100,v101\n"
"SH v[124:127],a[252:255],v[172:175]\n"
"EX v191,0\n"
"SH v[116:119],a[252:255],v[164:167]\n"
"GA v184\n"
"GA v185\n"
"EX v183,1\n"
"SZ v[160:163],a[8:11],a[192:195]\n"
"GL v184\n"
"GH v185\n"
"SZ v[168:171],a[8:11],a[196:199]\n"
"CB v106,v108,v109\n"
"GA v186\n"
"GA v187\n"
"EX v191,1\n"
"PF v183,v183,s57,s57\n"
"WL 5\n"
"s_nop 0\n"
"SZ v[164:167],a[12:15],a[192:195]\n"
"GL v186\n"
"GH v187\n"
"GA v188\n"
"SZ v[172:175],a[12:15],a[196:199]\n"
"GL v188\n"
"SB v[160:163],a[16:19],a[200:203]\n"
"GA v189\n"
"CB v99,v102,v103\n"
"SB v[168:171],a[16:19],a[204:207]\n"
"GH v189\n"
"SB v[164:167],a[20:23],a[200:203]\n"
"PF v191,v191,s57,s57\n"
"CB v107,v110,v111\n"
"SB v[172:175],a[20:23],a[204:207]\n"
"RC v183,0\n"
"SB v[160:163],a[24:27],a[208:211]\n"
"RC v191,0\n"
"SB v[168:171],a[24:27],a[212:215]\n"
"RC v183,1\n"
"SB v[164:167],a[28:31],a[208:211]\n"
"RC v191,1\n"
"SB v[172:175],a[28:31],a[212:215]\n"
"PM v191,v190,v191\n"
"PM v183,v182,v183\n"
"WL 0\n"
"v_or_b32 v190, v188, v189\n"
"v_or_b32 v189, v186, v187\n"
"v_or_b32 v188, v184, v185\n"
"v_or_b32 v182, v180, v181\n"
"v_or_b32 v181, v178, v179\n"
"SH v[136:139],a[232:235],v[188:191]\n"
"v_or_b32_e64 v180, v176, v177\n"
"GA v160\n"
"GA v161\n"
"SH v[128:131],a[232:235],v[180:183]\n"
"LD a[232:235],v7,57344\n"
"GL v160\n"
"SH v[140:143],a[236:239],v[188:191]\n"
"GH v161\n"
"CH v166,v166,v167\n"
"SH v[132:135],a[236:239],v[180:183]\n"
"LD a[236:239],v7,57600\n"
"CH v174,v174,v175\n"
"SH v[152:155],a[240:243],v[188:191]\n"
"PF v167,v166,v166,s56\n"
"GA v162\n"
"SH v[144:147],a[240:243],v[180:183]\n"
"GL v162\n"
"GA v163\n"
"SH v[156:159],a[244:247],v[188:191]\n"
"GH v163\n"
"LD a[240:243],v7,59392\n"
"SH v[148:151],a[244:247],v[180:183]\n"
"PF v175,v174,v174,s56\n"
"PM v167,v167,v166\n"
"PM v175,v175,v174\n"
"SH v[56:59],a[248:251],v[188:191]\n"
"GA v164\n"
"GA v165\n"
"EX v167,0\n"
"SH v[48:51],a[248:251],v[180:183]\n"
"GL v164\n"
"GH v165\n"
"LD a[244:247],v7,59648\n"
"EX v175,0\n"
"SH v[60:63],a[252:255],v[188:191]\n"
"GA v168\n"
"GA v169\n"
"SH v[52:55],a[252:255],v[180:183]\n"
"GL v168\n"
"GH v169\n"
"SZ v[176:179],a[8:11],a[216:219]\n"
"LD a[248:251],v7,61440\n"
"LD a[252:255],v7,61696\n"
"SZ v[184:187],a[8:11],a[220:223]\n"
"EX v167,1\n"
"GA v170\n"
"GA v171\n"
"EX v175,1\n"
"PF v167,v167,s57,s57\n"
"SZ v[180:183],a[12:15],a[216:219]\n"
"GL v170\n"
"GH v171\n"
"LD a[8:11],v1,55296\n"
"SZ v[188:191],a[12:15],a[220:223]\n"
"s_waitcnt vmcnt(34)\n"
"s_nop 0\n"
"buffer_store_dwordx4 v[16:19], v4, s[40:43], s91 offen offset:0 sc1\n"
"buffer_store_dwordx4 v[64:67], v4, s[40:43], s91 offen offset:64 sc1\n"
"buffer_store_dwordx4 v[96:99], v4, s[40:43], s91 offen offset:128 sc1\n"
"GA v172\n"
"SB v[176:179],a[16:19],a[224:227]\n"
"GL v172\n"
"GA v173\n"
"SB v[184:187],a[16:19],a[228:231]\n"
"GH v173\n"
"LD a[12:15],v1,55552\n"
"SB v[180:183],a[20:23],a[224:227]\n"
"LD a[16:19],v1,57344\n"
"PF v175,v175,s57,s57\n"
"SB v[188:191],a[20:23],a[228:231]\n"
"LD a[20:23],v1,57600\n"
"RC v167,0\n"
"SB v[176:179],a[24:27],a[0:3]\n"
"RC v175,0\n"
"SB v[184:187],a[24:27],a[4:7]\n"
"LD a[24:27],v1,59392\n"
"buffer_load_dwordx4 a[148:151], v4, s[36:39], s94 offen offset:3072\n"
"SB v[180:183],a[28:31],a[0:3]\n"
"buffer_load_dwordx4 a[156:159], v4, s[36:39], s94 offen offset:3136\n"
"buffer_load_dwordx4 a[164:167], v4, s[36:39], s94 offen offset:3200\n"
"RC v167,1\n"
"RC v175,1\n"
"SB v[188:191],a[28:31],a[4:7]\n"
"LD a[28:31],v1,59648\n"
"PM v175,v174,v175\n"
"PM v167,v166,v167\n"
"WL 5\n"
"v_or_b32 v174, v172, v173\n"
"v_or_b32 v173, v170, v171\n"
"v_or_b32 v172, v168, v169\n"
"v_or_b32 v166, v164, v165\n"
"v_or_b32 v165, v162, v163\n"
"SH v[40:43],a[232:235],v[172:175]\n"
"v_or_b32_e64 v164, v160, v161\n"
"GA v176\n"
"GA v177\n"
"SH v[32:35],a[232:235],v[164:167]\n"
"GL v176\n"
"GH v177\n"
"SH v[44:47],a[236:239],v[172:175]\n"
"CH v182,v182,v183\n"
"CH v190,v190,v191\n"
"SH v[36:39],a[236:239],v[164:167]\n"
"PF v183,v182,v182,s56\n"
"GA v178\n"
"SH v[88:91],a[240:243],v[172:175]\n"
"GL v178\n"
"GA v179\n"
"SH v[80:83],a[240:243],v[164:167]\n"
"GH v179\n"
"PF v191,v190,v190,s56\n"
"SH v[92:95],a[244:247],v[172:175]\n"
"PM v183,v183,v182\n"
"PM v191,v191,v190\n"
"SH v[84:87],a[244:247],v[164:167]\n"
"GA v180\n"
"GA v181\n"
"SH v[120:123],a[248:251],v[172:175]\n"
"GL v180\n"
"GH v181\n"
"SH v[112:115],a[248:251],v[164:167]\n"
"EX v183,0\n"
"SH v[124:127],a[252:255],v[172:175]\n"
"EX v191,0\n"
"SH v[116:119],a[252:255],v[164:167]\n"
"GA v184\n"
"GA v185\n"
"SZ v[160:163],a[8:11],a[192:195]\n"
"GL v184\n"
"GH v185\n"
"SZ v[168:171],a[8:11],a[196:199]\n"
"EX v183,1\n"
"GA v186\n"
"GA v187\n"
"EX v191,1\n"
"WL 6\n"
"s_nop 0\n"
"SZ v[164:167],a[12:15],a[192:195]\n"
"GL v186\n"
"GH v187\n"
"PF v183,v183,s57,s57\n"
"SZ v[172:175],a[12:15],a[196:199]\n"
"GA v188\n"
"SB v[160:163],a[16:19],a[200:203]\n"
"GL v188\n"
"GA v189\n"
"SB v[168:171],a[16:19],a[204:207]\n"
"GH v189\n"
"SB v[164:167],a[20:23],a[200:203]\n"
"PF v191,v191,s57,s57\n"
"RC v183,0\n"
"SB v[172:175],a[20:23],a[204:207]\n"
"RC v191,0\n"
"SB v[160:163],a[24:27],a[208:211]\n"
"RC v183,1\n"
"SB v[168:171],a[24:27],a[212:215]\n"
"PM v183,v182,v183\n"
"SB v[164:167],a[28:31],a[208:211]\n"
"RC v191,1\n"
"SB v[172:175],a[28:31],a[212:215]\n"
"PM v191,v190,v191\n"
"WL 0\n"
"v_or_b32 v190, v188, v189\n"
"v_or_b32 v189, v186, v187\n"
"v_or_b32 v188, v184, v185\n"
"v_or_b32 v182, v180, v181\n"
"v_or_b32 v181, v178, v179\n"
"SH v[136:139],a[232:235],v[188:191]\n"
"v_or_b32_e64 v180, v176, v177\n"
"GA v160\n"
"GA v161\n"
"SH v[128:131],a[232:235],v[180:183]\n"
"LD a[232:235],v7,63488\n"
"GL v160\n"
"SH v[140:143],a[236:239],v[188:191]\n"
"GH v161\n"
"CH v166,v166,v167\n"
"SH v[132:135],a[236:239],v[180:183]\n"
"LD a[236:239],v7,63744\n"
"CH v174,v174,v175\n"
"SH v[152:155],a[240:243],v[188:191]\n"
"PF v167,v166,v166,s56\n"
"GA v162\n"
"SH v[144:147],a[240:243],v[180:183]\n"
"GL v162\n"
"GA v163\n"
"SH v[156:159],a[244:247],v[188:191]\n"
"GH v163\n"
"LD a[240:243],v2,8192\n"
"SH v[148:151],a[244:247],v[180:183]\n"
"PF v175,v174,v174,s56\n"
"PM v167,v167,v166\n"
"PM v175,v175,v174\n"
"SH v[56:59],a[248:251],v[188:191]\n"
"GA v164\n"
"GA v165\n"
"EX v167,0\n"
"SH v[48:51],a[248:251],v[180:183]\n"
"GL v164\n"
"GH v165\n"
"LD a[244:247],v2,8448\n"
"EX v175,0\n"
"SH v[60:63],a[252:255],v[188:191]\n"
"GA v168\n"
"GA v169\n"
"SH v[52:55],a[252:255],v[180:183]\n"
"GL v168\n"
"GH v169\n"
"SZ v[176:179],a[8:11],a[216:219]\n"
"LD a[248:251],v2,10240\n"
"LD a[252:255],v2,10496\n"
"SZ v[184:187],a[8:11],a[220:223]\n"
"EX v167,1\n"
"GA v170\n"
"GA v171\n"
"EX v175,1\n"
"PF v167,v167,s57,s57\n"
"SZ v[180:183],a[12:15],a[216:219]\n"
"GL v170\n"
"GH v171\n"
"LD a[8:11],v1,61440\n"
"SZ v[188:191],a[12:15],a[220:223]\n"
"GA v172\n"
"GA v173\n"
"SB v[176:179],a[16:19],a[224:227]\n"
"GL v172\n"
"GH v173\n"
"SB v[184:187],a[16:19],a[228:231]\n"
"LD a[12:15],v1,61696\n"
"LD a[16:19],v1,63488\n"
"SB v[180:183],a[20:23],a[224:227]\n"
"PF v175,v175,s57,s57\n"
"RC v167,0\n"
"SB v[188:191],a[20:23],a[228:231]\n"
"LD a[20:23],v1,63744\n"
"RC v175,0\n"
"SB v[176:179],a[24:27],a[0:3]\n"
"RC v167,1\n"
"SB v[184:187],a[24:27],a[4:7]\n"
"LD a[24:27],v2,0\n"
"PM v167,v166,v167\n"
"SB v[180:183],a[28:31],a[0:3]\n"
"RC v175,1\n"
"SB v[188:191],a[28:31],a[4:7]\n"
"LD a[28:31],v2,256\n"
"PM v175,v174,v175\n"
"WL 5\n"
"v_or_b32 v174, v172, v173\n"
"v_or_b32 v173, v170, v171\n"
"v_or_b32 v172, v168, v169\n"
"v_or_b32 v166, v164, v165\n"
"v_or_b32 v165, v162, v163\n"
"SH v[40:43],a[232:235],v[172:175]\n"
"v_or_b32_e64 v164, v160, v161\n"
"GA v176\n"
"GA v177\n"
"SH v[32:35],a[232:235],v[164:167]\n"
"GL v176\n"
"GH v177\n"
"SH v[44:47],a[236:239],v[172:175]\n"
"CH v182,v182,v183\n"
"CH v190,v190,v191\n"
"SH v[36:39],a[236:239],v[164:167]\n"
"PF v183,v182,v182,s56\n"
"GA v178\n"
"SH v[88:91],a[240:243],v[172:175]\n"
"GL v178\n"
"GA v179\n"
"SH v[80:83],a[240:243],v[164:167]\n"
"GH v179\n"
"PF v191,v190,v190,s56\n"
"SH v[92:95],a[244:247],v[172:175]\n"
"PM v183,v183,v182\n"
"PM v191,v191,v190\n"
"SH v[84:87],a[244:247],v[164:167]\n"
"GA v180\n"
"GA v181\n"
"SH v[120:123],a[248:251],v[172:175]\n"
"GL v180\n"
"GH v181\n"
"SH v[112:115],a[248:251],v[164:167]\n"
"EX v183,0\n"
"SH v[124:127],a[252:255],v[172:175]\n"
"EX v191,0\n"
"SH v[116:119],a[252:255],v[164:167]\n"
"GA v184\n"
"GA v185\n"
"SZ v[160:163],a[8:11],a[192:195]\n"
"GL v184\n"
"GH v185\n"
"SZ v[168:171],a[8:11],a[196:199]\n"
"EX v183,1\n"
"GA v186\n"
"GA v187\n"
"EX v191,1\n"
"WL 6\n"
"SZ v[164:167],a[12:15],a[192:195]\n"
"GL v186\n"
"GH v187\n"
"PF v183,v183,s57,s57\n"
"SZ v[172:175],a[12:15],a[196:199]\n"
"GA v188\n"
"SB v[160:163],a[16:19],a[200:203]\n"
"GL v188\n"
"buffer_load_dwordx4 a[168:171], v4, s[36:39], s95 offen offset:0\n"
"SB v[168:171],a[16:19],a[204:207]\n"
"buffer_load_dwordx4 a[176:179], v4, s[36:39], s95 offen offset:64\n"
"SB v[164:167],a[20:23],a[200:203]\n"
"buffer_load_dwordx4 a[184:187], v4, s[36:39], s95 offen offset:128\n"
"SB v[172:175],a[20:23],a[204:207]\n"
"GA v189\n"
"PF v191,v191,s57,s57\n"
"s_waitcnt vmcnt(27)\n"
"v_smfmac_f32_32x32x32_bf16 v[32:47], v[8:11], a[96:103], v12\n"
"GH v189\n"
"RC v183,0\n"
"RC v191,0\n"
"SB v[160:163],a[24:27],a[208:211]\n"
"RC v183,1\n"
"SB v[168:171],a[24:27],a[212:215]\n"
"PM v183,v182,v183\n"
"SB v[164:167],a[28:31],a[208:211]\n"
"RC v191,1\n"
"SB v[172:175],a[28:31],a[212:215]\n"
"PM v191,v190,v191\n"
"WL 4\n"
"v_or_b32 v182, v180, v181\n"
"v_or_b32 v181, v178, v179\n"
"v_or_b32 v180, v176, v177\n"
"GA v160\n"
"WL 0\n"
"v_or_b32 v190, v188, v189\n"
"v_or_b32 v189, v186, v187\n"
"v_or_b32 v188, v184, v185\n"
"GA v161\n"
"CH v166,v166,v167\n"
"SH v[136:139],a[232:235],v[188:191]\n"
"CH v174,v174,v175\n"
"PF v167,v166,v166,s56\n"
"SH v[128:131],a[232:235],v[180:183]\n"
"LD a[232:235],v2,12288\n"
"GL v160\n"
"SH v[140:143],a[236:239],v[188:191]\n"
"GH v161\n"
"GA v162\n"
"SH v[132:135],a[236:239],v[180:183]\n"
"LD a[236:239],v2,12544\n"
"GL v162\n"
"SH v[152:155],a[240:243],v[188:191]\n"
"GA v163\n"
"PF v175,v174,v174,s56\n"
"s_waitcnt vmcnt(26)\n"
"s_nop 0\n"
"v_smfmac_f32_32x32x32_bf16 v[80:95], v[8:11], a[104:111], v12\n"
"GH v163\n"
"PM v167,v167,v166\n"
"PM v175,v175,v174\n"
"GA v164\n"
"GA v165\n"
"EX v167,0\n"
"SH v[144:147],a[240:243],v[180:183]\n"
"LD a[240:243],v2,14336\n"
"GL v164\n"
"SH v[156:159],a[244:247],v[188:191]\n"
"GH v165\n"
"GA v168\n"
"SH v[148:151],a[244:247],v[180:183]\n"
"LD a[244:247],v2,14592\n"
"GL v168\n"
"SH v[56:59],a[248:251],v[188:191]\n"
"EX v175,0\n"
"SH v[48:51],a[248:251],v[180:183]\n"
"GA v169\n"
"GA v170\n"
"SH v[60:63],a[252:255],v[188:191]\n"
"GH v169\n"
"LD a[248:251],v2,16384\n"
"SH v[52:55],a[252:255],v[180:183]\n"
"LD a[252:255],v2,16640\n"
"GL v170\n"
"SZ v[176:179],a[8:11],a[216:219]\n"
"EX v167,1\n"
"SZ v[184:187],a[8:11],a[220:223]\n"
"GA v171\n"
"PF v167,v167,s57,s57\n"
"s_waitcnt vmcnt(25)\n"
"s_nop 0\n"
"v_smfmac_f32_32x32x32_bf16 v[112:127], v[8:11], a[112:119], v12\n"
"GH v171\n"
"LD a[8:11],v2,2048\n"
"EX v175,1\n"
"GA v172\n"
"GA v173\n"
"SZ v[180:183],a[12:15],a[216:219]\n"
"GL v172\n"
"GH v173\n"
"SZ v[188:191],a[12:15],a[220:223]\n"
"LD a[12:15],v2,2304\n"
"PF v175,v175,s57,s57\n"
"SB v[176:179],a[16:19],a[224:227]\n"
"RC v167,0\n"
"SB v[184:187],a[16:19],a[228:231]\n"
"LD a[16:19],v2,4096\n"
"RC v175,0\n"
"SB v[180:183],a[20:23],a[224:227]\n"
"RC v167,1\n"
"SB v[188:191],a[20:23],a[228:231]\n"
"LD a[20:23],v2,4352\n"
"PM v167,v166,v167\n"
"s_waitcnt vmcnt(15)\n"
"v_smfmac_f32_32x32x32_bf16 v[128:143], v[8:11], a[120:127], v12\n"
"RC v175,1\n"
"WL 7\n"
"v_or_b32_e64 v166, v164, v165\n"
"PM v175,v174,v175\n"
"v_or_b32_e64 v165, v162, v163\n"
"SB v[176:179],a[24:27],a[0:3]\n"
"WL 3\n"
"v_or_b32 v174, v172, v173\n"
"SB v[184:187],a[24:27],a[4:7]\n"
"LD a[24:27],v2,6144\n"
"v_or_b32_e64 v173, v170, v171\n"
"SB v[180:183],a[28:31],a[0:3]\n"
"v_or_b32 v172, v168, v169\n"
"v_or_b32 v164, v160, v161\n"
"SB v[188:191],a[28:31],a[4:7]\n"
"LD a[28:31],v2,6400\n"
"GA v176\n"
"SH v[40:43],a[232:235],v[172:175]\n"
"GL v176\n"
"GA v177\n"
"SH v[32:35],a[232:235],v[164:167]\n"
"GH v177\n"
"CH v182,v182,v183\n"
"SH v[44:47],a[236:239],v[172:175]\n"
"CH v190,v190,v191\n"
"PF v183,v182,v182,s56\n"
"SH v[36:39],a[236:239],v[164:167]\n"
"GA v178\n"
"GA v179\n"
"SH v[88:91],a[240:243],v[172:175]\n"
"GL v178\n"
"GH v179\n"
"s_waitcnt vmcnt(14)\n"
"s_nop 0\n"
"v_smfmac_f32_32x32x32_bf16 v[144:159], v[8:11], a[128:135], v12\n"
"PF v191,v190,v190,s56\n"
"PM v183,v183,v182\n"
"PM v191,v191,v190\n"
"GA v180\n"
"GA v181\n"
"GA v184\n"
"SH v[80:83],a[240:243],v[164:167]\n"
"GL v180\n"
"GH v181\n"
"SH v[92:95],a[244:247],v[172:175]\n"
"GL v184\n"
"GA v185\n"
"SH v[84:87],a[244:247],v[164:167]\n"
"GH v185\n"
"EX v183,0\n"
"SH v[120:123],a[248:251],v[172:175]\n"
"EX v191,0\n"
"SH v[112:115],a[248:251],v[164:167]\n"
"buffer_store_dwordx4 v[24:27], v4, s[40:43], s91 offen offset:3072 sc1\n"
"SH v[124:127],a[252:255],v[172:175]\n"
"buffer_store_dwordx4 v[72:75], v4, s[40:43], s91 offen offset:3136 sc1\n"
"SH v[116:119],a[252:255],v[164:167]\n"
"buffer_store_dwordx4 v[104:107], v4, s[40:43], s91 offen offset:3200 sc1\n"
"SZ v[160:163],a[8:11],a[192:195]\n"
"EX v183,1\n"
"SZ v[168:171],a[8:11],a[196:199]\n"
"GA v186\n"
"GA v187\n"
"s_waitcnt vmcnt(16)\n"
"v_smfmac_f32_32x32x32_bf16 v[48:63], v[8:11], a[136:143], v12\n"
"GL v186\n"
"GH v187\n"
"LD v[208:211],v3,0\n"
"EX v191,1\n"
"WL 7\n"
"SZ v[164:167],a[12:15],a[192:195]\n"
"PF v183,v183,s57,s57\n"
"GA v188\n"
"SZ v[172:175],a[12:15],a[196:199]\n"
"GL v188\n"
"SB v[160:163],a[16:19],a[200:203]\n"
"GA v189\n"
"PF v191,v191,s57,s57\n"
"SB v[168:171],a[16:19],a[204:207]\n"
"GH v189\n"
"SB v[164:167],a[20:23],a[200:203]\n"
"LD v[212:215],v3,16\n"
"RC v183,0\n"
"SB v[172:175],a[20:23],a[204:207]\n"
"RC v191,0\n"
"SB v[160:163],a[24:27],a[208:211]\n"
"RC v183,1\n"
"SB v[168:171],a[24:27],a[212:215]\n"
"LD v[224:227],v3,128\n"
"SB v[164:167],a[28:31],a[208:211]\n"
"RC v191,1\n"
"SB v[172:175],a[28:31],a[212:215]\n"
"PM v191,v190,v191\n"
"PM v183,v182,v183\n"
"WL 2\n"
"v_or_b32 v190, v188, v189\n"
"v_or_b32 v189, v186, v187\n"
"v_or_b32 v188, v184, v185\n"
"v_or_b32 v182, v180, v181\n"
"v_or_b32 v181, v178, v179\n"
"SH v[136:139],a[232:235],v[188:191]\n"
"v_or_b32_e64 v180, v176, v177\n"
"GA v160\n"
"GA v161\n"
"SH v[128:131],a[232:235],v[180:183]\n"
"LD a[232:235],v2,18432\n"
"GL v160\n"
"SH v[140:143],a[236:239],v[188:191]\n"
"GH v161\n"
"CH v166,v166,v167\n"
"SH v[132:135],a[236:239],v[180:183]\n"
"LD a[236:239],v2,18688\n"
"LD v[228:231],v3,144\n"
"SH v[152:155],a[240:243],v[188:191]\n"
"CH v174,v174,v175\n"
"PF v167,v166,v166,s56\n"
"SH v[144:147],a[240:243],v[180:183]\n"
"GA v162\n"
"GA v163\n"
"SH v[156:159],a[244:247],v[188:191]\n"
"GL v162\n"
"GH v163\n"
"LD a[240:243],v2,20480\n"
"SH v[148:151],a[244:247],v[180:183]\n"
"PF v175,v174,v174,s56\n"
"PM v167,v167,v166\n"
"PM v175,v175,v174\n"
"SH v[56:59],a[248:251],v[188:191]\n"
"GA v164\n"
"GA v165\n"
"EX v167,0\n"
"EX v175,0\n"
"SH v[48:51],a[248:251],v[180:183]\n"
"GL v164\n"
"GH v165\n"
"LD a[244:247],v2,20736\n"
"LD v[240:243],v3,256\n"
"SH v[60:63],a[252:255],v[188:191]\n"
"GA v168\n"
"GA v169\n"
"SH v[52:55],a[252:255],v[180:183]\n"
"GL v168\n"
"GH v169\n"
"SZ v[176:179],a[8:11],a[216:219]\n"
"LD a[248:251],v2,22528\n"
"LD a[252:255],v2,22784\n"
"SZ v[184:187],a[8:11],a[220:223]\n"
"EX v167,1\n"
"GA v170\n"
"GA v171\n"
"EX v175,1\n"
"PF v167,v167,s57,s57\n"
"GA v172\n"
"SZ v[180:183],a[12:15],a[216:219]\n"
"GL v170\n"
"GH v171\n"
"LD v[244:247],v3,272\n"
"LD a[8:11],v1,0\n"
"buffer_load_dwordx4 a[172:175], v4, s[36:39], s95 offen offset:3072\n"
"GL v172\n"
"SZ v[188:191],a[12:15],a[220:223]\n"
"buffer_load_dwordx4 a[180:183], v4, s[36:39], s95 offen offset:3136\n"
"SB v[176:179],a[16:19],a[224:227]\n"
"buffer_load_dwordx4 a[188:191], v4, s[36:39], s95 offen offset:3200\n"
"SB v[184:187],a[16:19],a[228:231]\n"
"buffer_load_dword a83, v0, s[24:27], s28 offen\n"
"GA v173\n"
"PF v175,v175,s57,s57\n"
"SB v[180:183],a[20:23],a[224:227]\n"
"GH v173\n"
"LD a[12:15],v1,256\n"
"SB v[188:191],a[20:23],a[228:231]\n"
"LD a[16:19],v1,2048\n"
"LD a[20:23],v1,2304\n"
"SB v[176:179],a[24:27],a[0:3]\n"
"LD v[16:19],v3,0\n"
"RC v167,0\n"
"SB v[184:187],a[24:27],a[4:7]\n"
"LD a[24:27],v1,4096\n"
"RC v175,0\n"
"SB v[180:183],a[28:31],a[0:3]\n"
"RC v167,1\n"
"RC v175,1\n"
"SB v[188:191],a[28:31],a[4:7]\n"
"LD a[28:31],v1,4352\n"
"PM v175,v174,v175\n"
"PM v167,v166,v167\n"
"WL 6\n"
"v_or_b32 v174, v172, v173\n"
"v_or_b32 v173, v170, v171\n"
"v_or_b32 v172, v168, v169\n"
"v_or_b32 v166, v164, v165\n"
"v_or_b32 v165, v162, v163\n"
"SH v[40:43],a[232:235],v[172:175]\n"
"v_or_b32_e64 v164, v160, v161\n"
"GA v176\n"
"GA v177\n"
"SH v[32:35],a[232:235],v[164:167]\n"
"GL v176\n"
"GH v177\n"
"SH v[44:47],a[236:239],v[172:175]\n"
"LD v[20:23],v3,16\n"
"CH v182,v182,v183\n"
"SH v[36:39],a[236:239],v[164:167]\n"
"CH v190,v190,v191\n"
"PF v183,v182,v182,s56\n"
"SH v[88:91],a[240:243],v[172:175]\n"
"GA v178\n"
"GA v179\n"
"SH v[80:83],a[240:243],v[164:167]\n"
"GL v178\n"
"GH v179\n"
"SH v[92:95],a[244:247],v[172:175]\n"
"PF v191,v190,v190,s56\n"
"PM v183,v183,v182\n"
"SH v[84:87],a[244:247],v[164:167]\n"
"PM v191,v191,v190\n"
"GA v180\n"
"SH v[120:123],a[248:251],v[172:175]\n"
"GL v180\n"
"GA v181\n"
"SH v[112:115],a[248:251],v[164:167]\n"
"GH v181\n"
"LD v[64:67],v3,128\n"
"EX v183,0\n"
"SH v[124:127],a[252:255],v[172:175]\n"
"EX v191,0\n"
"SH v[116:119],a[252:255],v[164:167]\n"
"GA v184\n"
"GA v185\n"
"EX v183,1\n"
"SZ v[160:163],a[8:11],a[32:35]\n"
"GL v184\n"
"GH v185\n"
"GA v186\n"
"SZ v[168:171],a[8:11],a[36:39]\n"
"GL v186\n"
"GA v187\n"
"EX v191,1\n"
"s_add_u32 s22, s61, 100352\n"
"GH v187\n"
"WL 8\n"
"s_nop 0\n"
"SZ v[164:167],a[12:15],a[32:35]\n"
"LD v[68:71],v3,144\n"
"LD a[8:11],v1,6144\n"
"buffer_load_dwordx4 a[192:195], v0, s[40:43], s22 offen offset:0\n"
"PF v183,v183,s57,s57\n"
"SZ v[172:175],a[12:15],a[36:39]\n"
"GA v188\n"
"SB v[160:163],a[16:19],a[40:43]\n"
"GL v188\n"
"GA v189\n"
"SB v[168:171],a[16:19],a[44:47]\n"
"GH v189\n"
"LD a[12:15],v1,6400\n"
"SB v[164:167],a[20:23],a[40:43]\n"
"LD v[96:99],v3,256\n"
"LD a[16:19],v1,8192\n"
"SB v[172:175],a[20:23],a[44:47]\n"
"LD a[20:23],v1,8448\n"
"SB v[160:163],a[24:27],a[48:51]\n"
"LD v[100:103],v3,272\n"
"PF v191,v191,s57,s57\n"
"SB v[168:171],a[24:27],a[52:55]\n"
"LD a[24:27],v1,10240\n"
"SB v[164:167],a[28:31],a[48:51]\n"
"RC v183,0\n"
"RC v191,0\n"
"RC v183,1\n"
"RC v191,1\n"
"SB v[172:175],a[28:31],a[52:55]\n"
"buffer_load_dwordx4 a[196:199], v0, s[40:43], s22 offen offset:1024\n"
"LD a[28:31],v1,10496\n"
"PM v191,v190,v191\n"
"PM v183,v182,v183\n"
"WL 7\n"
"v_or_b32 v190, v188, v189\n"
"v_or_b32 v189, v186, v187\n"
"v_or_b32 v188, v184, v185\n"
"v_or_b32 v182, v180, v181\n"
"v_or_b32 v181, v178, v179\n"
"SH v[136:139],a[232:235],v[188:191]\n"
"v_or_b32_e64 v180, v176, v177\n"
"GA v160\n"
"GA v161\n"
"SH v[128:131],a[232:235],v[180:183]\n"
"LD a[232:235],v7,8192\n"
"GL v160\n"
"SH v[140:143],a[236:239],v[188:191]\n"
"GH v161\n"
"CH v166,v166,v167\n"
"SH v[132:135],a[236:239],v[180:183]\n"
"LD a[236:239],v7,8448\n"
"CH v174,v174,v175\n"
"SH v[152:155],a[240:243],v[188:191]\n"
"PF v167,v166,v166,s56\n"
"GA v162\n"
"SH v[144:147],a[240:243],v[180:183]\n"
"GL v162\n"
"GA v163\n"
"SH v[156:159],a[244:247],v[188:191]\n"
"GH v163\n"
"LD a[240:243],v7,10240\n"
"PF v175,v174,v174,s56\n"
"SH v[148:151],a[244:247],v[180:183]\n"
"PM v167,v167,v166\n"
"s_add_u32 s22, s61, 102400\n"
"buffer_load_dwordx4 a[200:203], v0, s[40:43], s22 offen offset:0\n"
"PM v175,v175,v174\n"
"SH v[56:59],a[248:251],v[188:191]\n"
"GA v164\n"
"GA v165\n"
"EX v167,0\n"
"SH v[48:51],a[248:251],v[180:183]\n"
"GL v164\n"
"GH v165\n"
"LD a[244:247],v7,10496\n"
"EX v175,0\n"
"SH v[60:63],a[252:255],v[188:191]\n"
"GA v168\n"
"GA v169\n"
"SH v[52:55],a[252:255],v[180:183]\n"
"GL v168\n"
"GH v169\n"
"SZ v[176:179],a[8:11],a[32:35]\n"
"LD a[248:251],v7,12288\n"
"LD a[252:255],v7,12544\n"
"SZ v[184:187],a[8:11],a[36:39]\n"
"EX v167,1\n"
"GA v170\n"
"GA v171\n"
"EX v175,1\n"
"PF v167,v167,s57,s57\n"
"WL 7\n"
"s_nop 0\n"
"SZ v[180:183],a[12:15],a[32:35]\n"
"GL v170\n"
"GH v171\n"
"LD a[8:11],v1,12288\n"
"SZ v[188:191],a[12:15],a[36:39]\n"
"buffer_load_dwordx4 a[204:207], v0, s[40:43], s22 offen offset:1024\n"
"GA v172\n"
"SB v[176:179],a[16:19],a[40:43]\n"
"GL v172\n"
"GA v173\n"
"SB v[184:187],a[16:19],a[44:47]\n"
"GH v173\n"
"SB v[180:183],a[20:23],a[40:43]\n"
"LD a[12:15],v1,12544\n"
"LD a[16:19],v1,14336\n"
"SB v[188:191],a[20:23],a[44:47]\n"
"LD a[20:23],v1,14592\n"
"SB v[176:179],a[24:27],a[48:51]\n"
"PF v175,v175,s57,s57\n"
"RC v167,0\n"
"SB v[184:187],a[24:27],a[52:55]\n"
"LD a[24:27],v1,16384\n"
"SB v[180:183],a[28:31],a[48:51]\n"
"RC v175,0\n"
"RC v167,1\n"
"RC v175,1\n"
"SB v[188:191],a[28:31],a[52:55]\n"
"LD a[28:31],v1,16640\n"
"PM v175,v174,v175\n"
"PM v167,v166,v167\n"
"WL 5\n"
"v_or_b32 v174, v172, v173\n"
"v_or_b32 v173, v170, v171\n"
"v_or_b32 v172, v168, v169\n"
"v_or_b32 v166, v164, v165\n"
"v_or_b32 v165, v162, v163\n"
"v_mfma_f32_16x16x32_f16 v[216:219], a[232:235], v[172:175], v[208:211]\n"
"v_or_b32_e64 v164, v160, v161\n"
"s_add_u32 s22, s61, 294912\n"
"buffer_load_dwordx4 a[208:211], v0, s[40:43], s22 offen offset:0\n"
"SH v[208:211],a[232:235],v[164:167]\n"
"GA v176\n"
"GA v177\n"
"v_mfma_f32_16x16x32_f16 v[220:223], a[236:239], v[172:175], v[212:215]\n"
"GL v176\n"
"GH v177\n"
"SH v[212:215],a[236:239],v[164:167]\n"
"CH v182,v182,v183\n"
"CH v190,v190,v191\n"
"v_mfma_f32_16x16x32_f16 v[232:235], a[240:243], v[172:175], v[224:227]\n"
"PF v183,v182,v182,s56\n"
"GA v178\n"
"SH v[224:227],a[240:243],v[164:167]\n"
"GL v178\n"
"GA v179\n"
"v_mfma_f32_16x16x32_f16 v[236:239], a[244:247], v[172:175], v[228:231]\n"
"GH v179\n"
"PF v191,v190,v190,s56\n"
"SH v[228:231],a[244:247],v[164:167]\n"
"PM v183,v183,v182\n"
"PM v191,v191,v190\n"
"v_mfma_f32_16x16x32_f16 v[248:251], a[248:251], v[172:175], v[240:243]\n"
"buffer_load_dwordx4 a[212:215], v0, s[40:43], s22 offen offset:1024\n"
"SH v[240:243],a[248:251],v[164:167]\n"
"GA v180\n"
"GA v181\n"
"v_mfma_f32_16x16x32_f16 v[252:255], a[252:255], v[172:175], v[244:247]\n"
"GL v180\n"
"GH v181\n"
"SH v[244:247],a[252:255],v[164:167]\n"
"EX v183,0\n"
"v_mfma_f32_16x16x32_f16 v[24:27], a[232:235], a[60:63], v[16:19]\n"
"EX v191,0\n"
"SH v[16:19],a[232:235],a[56:59]\n"
"GA v184\n"
"GA v185\n"
"v_mfma_f32_16x16x32_f16 v[28:31], a[236:239], a[60:63], v[20:23]\n"
"GL v184\n"
"GH v185\n"
"SH v[20:23],a[236:239],a[56:59]\n"
"LD a[232:235],v7,14336\n"
"LD a[236:239],v7,14592\n"
"v_mfma_f32_16x16x32_f16 v[72:75], a[240:243], a[60:63], v[64:67]\n"
"EX v183,1\n"
"SH v[64:67],a[240:243],a[56:59]\n"
"GA v186\n"
"GA v187\n"
"v_mfma_f32_16x16x32_f16 v[76:79], a[244:247], a[60:63], v[68:71]\n"
"GL v186\n"
"GH v187\n"
"SH v[68:71],a[244:247],a[56:59]\n"
"LD a[240:243],v7,16384\n"
"LD a[244:247],v7,16640\n"
"v_mfma_f32_16x16x32_f16 v[104:107], a[248:251], a[60:63], v[96:99]\n"
"EX v191,1\n"
"SH v[96:99],a[248:251],a[56:59]\n"
"s_add_u32 s22, s61, 296960\n"
"buffer_load_dwordx4 a[216:219], v0, s[40:43], s22 offen offset:0\n"
"PF v183,v183,s57,s57\n"
"GA v188\n"
"v_mfma_f32_16x16x32_f16 v[108:111], a[252:255], a[60:63], v[100:103]\n"
"GL v188\n"
"GA v189\n"
"SH v[100:103],a[252:255],a[56:59]\n"
"GH v189\n"
"LD a[248:251],v7,18432\n"
"SZ v[160:163],a[8:11],a[32:35]\n"
"LD a[252:255],v7,18688\n"
"PF v191,v191,s57,s57\n"
"SZ v[168:171],a[8:11],a[36:39]\n"
"LD a[8:11],v1,18432\n"
"WL 10\n"
"s_nop 0\n"
"SZ v[164:167],a[12:15],a[32:35]\n"
"buffer_load_dwordx4 a[220:223], v0, s[40:43], s22 offen offset:1024\n"
"SZ v[172:175],a[12:15],a[36:39]\n"
"LD a[12:15],v1,18688\n"
"SB v[160:163],a[16:19],a[40:43]\n"
"RC v183,0\n"
"SB v[168:171],a[16:19],a[44:47]\n"
"LD a[16:19],v1,20480\n"
"SB v[164:167],a[20:23],a[40:43]\n"
"RC v191,0\n"
"SB v[172:175],a[20:23],a[44:47]\n"
"LD a[20:23],v1,20736\n"
"SB v[160:163],a[24:27],a[48:51]\n"
"RC v183,1\n"
"SB v[168:171],a[24:27],a[52:55]\n"
"LD a[24:27],v1,22528\n"
"SB v[164:167],a[28:31],a[48:51]\n"
"RC v191,1\n"
"PM v183,v182,v183\n"
"SB v[172:175],a[28:31],a[52:55]\n"
"LD a[28:31],v1,22784\n"
"PM v191,v190,v191\n"
"WL 8\n"
"v_or_b32 v190, v188, v189\n"
"v_or_b32 v189, v186, v187\n"
"v_or_b32 v188, v184, v185\n"
"v_or_b32 v182, v180, v181\n"
"v_or_b32 v181, v178, v179\n"
"SH v[216:219],a[232:235],v[188:191]\n"
"v_or_b32_e64 v180, v176, v177\n"
"s_add_u32 s22, s61, 299008\n"
"buffer_load_dwordx4 a[224:227], v0, s[40:43], s22 offen offset:0\n"
"SH v[208:211],a[232:235],v[180:183]\n"
"GA v160\n"
"GA v161\n"
"SH v[220:223],a[236:239],v[188:191]\n"
"GL v160\n"
"GH v161\n"
"SH v[212:215],a[236:239],v[180:183]\n"
"CH v166,v166,v167\n"
"CH v174,v174,v175\n"
"SH v[232:235],a[240:243],v[188:191]\n"
"PF v167,v166,v166,s56\n"
"GA v162\n"
"SH v[224:227],a[240:243],v[180:183]\n"
"GL v162\n"
"GA v163\n"
"SH v[236:239],a[244:247],v[188:191]\n"
"GH v163\n"
"PF v175,v174,v174,s56\n"
"SH v[228:231],a[244:247],v[180:183]\n"
"PM v167,v167,v166\n"
"WL 4\n"
"s_nop 0\n"
"SH v[248:251],a[248:251],v[188:191]\n"
"buffer_load_dwordx4 a[228:231], v0, s[40:43], s22 offen offset:1024\n"
"SH v[240:243],a[248:251],v[180:183]\n"
"PM v175,v175,v174\n"
"SH v[252:255],a[252:255],v[188:191]\n"
"GA v164\n"
"GA v165\n"
"SH v[244:247],a[252:255],v[180:183]\n"
"GL v164\n"
"GH v165\n"
"SH v[24:27],a[232:235],a[68:71]\n"
"EX v167,0\n"
"SH v[16:19],a[232:235],a[64:67]\n"
"EX v175,0\n"
"SH v[28:31],a[236:239],a[68:71]\n"
"GA v168\n"
"GA v169\n"
"SH v[20:23],a[236:239],a[64:67]\n"
"GL v168\n"
"GH v169\n"
"SH v[72:75],a[240:243],a[68:71]\n"
"LD a[232:235],v7,20480\n"
"LD a[236:239],v7,20736\n"
"SH v[64:67],a[240:243],a[64:67]\n"
"EX v167,1\n"
"GA v170\n"
"SH v[76:79],a[244:247],a[68:71]\n"
"GL v170\n"
"GA v171\n"
"SH v[68:71],a[244:247],a[64:67]\n"
"GH v171\n"
"LD a[240:243],v7,22528\n"
"SH v[104:107],a[248:251],a[68:71]\n"
"LD a[244:247],v7,22784\n"
"EX v175,1\n"
"SH v[96:99],a[248:251],a[64:67]\n"
"s_add_u32 s22, s61, 196608\n"
"buffer_load_dwordx4 a[56:59], v0, s[40:43], s22 offen offset:0\n"
"PF v167,v167,s57,s57\n"
"GA v172\n"
"SH v[108:111],a[252:255],a[68:71]\n"
"GL v172\n"
"GA v173\n"
"SH v[100:103],a[252:255],a[64:67]\n"
"GH v173\n"
"SZ v[176:179],a[8:11],a[32:35]\n"
"LD a[248:251],v7,24576\n"
"LD a[252:255],v7,24832\n"
"SZ v[184:187],a[8:11],a[36:39]\n"
"LD a[8:11],v1,24576\n"
"SZ v[180:183],a[12:15],a[32:35]\n"
"buffer_load_dwordx4 a[60:63], v0, s[40:43], s22 offen offset:1024\n"
"SZ v[188:191],a[12:15],a[36:39]\n"
"LD a[12:15],v1,24832\n"
"SB v[176:179],a[16:19],a[40:43]\n"
"PF v175,v175,s57,s57\n"
"CB v32,v32,v33\n"
"SB v[184:187],a[16:19],a[44:47]\n"
"LD a[16:19],v1,26624\n"
"SB v[180:183],a[20:23],a[40:43]\n"
"RC v167,0\n"
"SB v[188:191],a[20:23],a[44:47]\n"
"LD a[20:23],v1,26880\n"
"SB v[176:179],a[24:27],a[48:51]\n"
"RC v175,0\n"
"SB v[184:187],a[24:27],a[52:55]\n"
"LD a[24:27],v1,28672\n"
"RC v167,1\n"
"RC v175,1\n"
"CB v40,v40,v41\n"
"SB v[180:183],a[28:31],a[48:51]\n"
"CB v33,v34,v35\n"
"PM v175,v174,v175\n"
"PM v167,v166,v167\n"
"SB v[188:191],a[28:31],a[52:55]\n"
"LD a[28:31],v1,28928\n"
"WL 8\n"
"v_or_b32 v174, v172, v173\n"
"CB v41,v42,v43\n"
"v_or_b32 v173, v170, v171\n"
"v_or_b32 v172, v168, v169\n"
"v_or_b32 v166, v164, v165\n"
"v_or_b32 v165, v162, v163\n"
"SH v[216:219],a[232:235],v[172:175]\n"
"CB v34,v36,v37\n"
"v_or_b32_e64 v164, v160, v161\n"
"s_add_u32 s22, s61, 198656\n"
"buffer_load_dwordx4 a[64:67], v0, s[40:43], s22 offen offset:0\n"
"SH v[208:211],a[232:235],v[164:167]\n"
"CB v42,v44,v45\n"
"GA v176\n"
"SH v[220:223],a[236:239],v[172:175]\n"
"GL v176\n"
"GA v177\n"
"SH v[212:215],a[236:239],v[164:167]\n"
"GH v177\n"
"CH v182,v182,v183\n"
"SH v[232:235],a[240:243],v[172:175]\n"
"CB v35,v38,v39\n"
"CH v190,v190,v191\n"
"SH v[224:227],a[240:243],v[164:167]\n"
"CB v43,v46,v47\n"
"PF v183,v182,v182,s56\n"
"SH v[236:239],a[244:247],v[172:175]\n"
"GA v178\n"
"GA v179\n"
"SH v[228:231],a[244:247],v[164:167]\n"
"GL v178\n"
"WL 3\n"
"s_nop 0\n"
"SH v[248:251],a[248:251],v[172:175]\n"
"GH v179\n"
"PF v191,v190,v190,s56\n"
"SH v[240:243],a[248:251],v[164:167]\n"
"buffer_load_dwordx4 a[68:71], v0, s[40:43], s22 offen offset:1024\n"
"PM v183,v183,v182\n"
"SH v[252:255],a[252:255],v[172:175]\n"
"PM v191,v191,v190\n"
"GA v180\n"
"SH v[244:247],a[252:255],v[164:167]\n"
"GL v180\n"
"GA v181\n"
"SH v[24:27],a[232:235],a[76:79]\n"
"GH v181\n"
"GA v184\n"
"SH v[16:19],a[232:235],a[72:75]\n"
"GL v184\n"
"GA v185\n"
"SH v[28:31],a[236:239],a[76:79]\n"
"GH v185\n"
"LD a[232:235],v7,26624\n"
"SH v[20:23],a[236:239],a[72:75]\n"
"LD a[236:239],v7,26880\n"
"CB v80,v80,v81\n"
"SH v[72:75],a[240:243],a[76:79]\n"
"EX v183,0\n"
"SH v[64:67],a[240:243],a[72:75]\n"
"EX v191,0\n"
"EX v183,1\n"
"GA v186\n"
"SH v[76:79],a[244:247],a[76:79]\n"
"GL v186\n"
"GA v187\n"
"SH v[68:71],a[244:247],a[72:75]\n"
"GH v187\n"
"LD a[240:243],v7,28672\n"
"SH v[104:107],a[248:251],a[76:79]\n"
"LD a[244:247],v7,28928\n"
"EX v191,1\n"
"CB v88,v88,v89\n"
"SH v[96:99],a[248:251],a[72:75]\n"
"CB v81,v82,v83\n"
"PF v183,v183,s57,s57\n"
"GA v188\n"
"GA v189\n"
"CB v89,v90,v91\n"
"SH v[108:111],a[252:255],a[76:79]\n"
"GL v188\n"
"GH v189\n"
"SH v[100:103],a[252:255],a[72:75]\n"
"LD a[248:251],v7,30720\n"
"SZ v[160:163],a[8:11],a[32:35]\n"
"LD a[252:255],v7,30976\n"
"PF v191,v191,s57,s57\n"
"SZ v[168:171],a[8:11],a[36:39]\n"
"LD a[8:11],v1,30720\n"
"s_add_u32 s22, s61, 200704\n"
"SZ v[164:167],a[12:15],a[32:35]\n"
"buffer_load_dwordx4 a[72:75], v0, s[40:43], s22 offen offset:0\n"
"SZ v[172:175],a[12:15],a[36:39]\n"
"LD a[12:15],v1,30976\n"
"RC v183,0\n"
"SB v[160:163],a[16:19],a[40:43]\n"
"CB v82,v84,v85\n"
"CB v90,v92,v93\n"
"SB v[168:171],a[16:19],a[44:47]\n"
"LD a[16:19],v1,32768\n"
"SB v[164:167],a[20:23],a[40:43]\n"
"RC v191,0\n"
"SB v[172:175],a[20:23],a[44:47]\n"
"LD a[20:23],v1,33024\n"
"SB v[160:163],a[24:27],a[48:51]\n"
"CB v83,v86,v87\n"
"CB v91,v94,v95\n"
"SB v[168:171],a[24:27],a[52:55]\n"
"LD a[24:27],v1,34816\n"
"SB v[164:167],a[28:31],a[48:51]\n"
"buffer_load_dwordx4 a[76:79], v0, s[40:43], s22 offen offset:1024\n"
"RC v183,1\n"
"RC v191,1\n"
"PM v183,v182,v183\n"
"SB v[172:175],a[28:31],a[52:55]\n"
"LD a[28:31],v1,35072\n"
"PM v191,v190,v191\n"
"WL 6\n"
"v_or_b32 v190, v188, v189\n"
"v_or_b32 v189, v186, v187\n"
"v_or_b32 v188, v184, v185\n"
"v_or_b32 v182, v180, v181\n"
"v_or_b32 v181, v178, v179\n"
"SH v[216:219],a[232:235],v[188:191]\n"
"v_or_b32_e64 v180, v176, v177\n"
"GA v160\n"
"GA v161\n"
"SH v[208:211],a[232:235],v[180:183]\n"
"GL v160\n"
"GH v161\n"
"SH v[220:223],a[236:239],v[188:191]\n"
"CH v166,v166,v167\n"
"CB v112,v112,v113\n"
"SH v[212:215],a[236:239],v[180:183]\n"
"CH v174,v174,v175\n"
"CB v120,v120,v121\n"
"SH v[232:235],a[240:243],v[188:191]\n"
"PF v167,v166,v166,s56\n"
"GA v162\n"
"SH v[224:227],a[240:243],v[180:183]\n"
"GL v162\n"
"GA v163\n"
"SH v[236:239],a[244:247],v[188:191]\n"
"GH v163\n"
"PF v175,v174,v174,s56\n"
"SH v[228:231],a[244:247],v[180:183]\n"
"CB v113,v114,v115\n"
"SH v[248:251],a[248:251],v[188:191]\n"
"PM v167,v167,v166\n"
"CB v121,v122,v123\n"
"SH v[240:243],a[248:251],v[180:183]\n"
"PM v175,v175,v174\n"
"SH v[252:255],a[252:255],v[188:191]\n"
"GA v164\n"
"GA v165\n"
"SH v[244:247],a[252:255],v[180:183]\n"
"GL v164\n"
"GH v165\n"
"SH v[24:27],a[232:235],a[92:95]\n"
"CB v114,v116,v117\n"
"CB v122,v124,v125\n"
"SH v[16:19],a[232:235],a[88:91]\n"
"EX v167,0\n"
"EX v175,0\n"
"SH v[28:31],a[236:239],a[92:95]\n"
"GA v168\n"
"GA v169\n"
"SH v[20:23],a[236:239],a[88:91]\n"
"GL v168\n"
"GH v169\n"
"SH v[72:75],a[240:243],a[92:95]\n"
"LD a[232:235],v7,32768\n"
"LD a[236:239],v7,33024\n"
"SH v[64:67],a[240:243],a[88:91]\n"
"CB v115,v118,v119\n"
"CB v123,v126,v127\n"
"EX v167,1\n"
"SH v[76:79],a[244:247],a[92:95]\n"
"GA v170\n"
"GA v171\n"
"SH v[68:71],a[244:247],a[88:91]\n"
"GL v170\n"
"GH v171\n"
"SH v[104:107],a[248:251],a[92:95]\n"
"LD a[240:243],v7,34816\n"
"LD a[244:247],v7,35072\n"
"SH v[96:99],a[248:251],a[88:91]\n"
"EX v175,1\n"
"PF v167,v167,s57,s57\n"
"GA v172\n"
"SH v[108:111],a[252:255],a[92:95]\n"
"GL v172\n"
"GA v173\n"
"SH v[100:103],a[252:255],a[88:91]\n"
"GH v173\n"
"WL 10\n"
"s_nop 0\n"
"SZ v[176:179],a[8:11],a[32:35]\n"
"LD a[248:251],v7,36864\n"
"LD a[252:255],v7,37120\n"
"SZ v[184:187],a[8:11],a[36:39]\n"
"LD a[8:11],v1,36864\n"
"SZ v[180:183],a[12:15],a[32:35]\n"
"buffer_store_dwordx4 v[32:35], v4, s[40:43], s92 offen offset:0 sc1\n"
"SZ v[188:191],a[12:15],a[36:39]\n"
"LD a[12:15],v1,37120\n"
"SB v[176:179],a[16:19],a[40:43]\n"
"buffer_store_dwordx4 v[80:83], v4, s[40:43], s92 offen offset:64 sc1\n"
"SB v[184:187],a[16:19],a[44:47]\n"
"buffer_store_dwordx4 v[112:115], v4, s[40:43], s92 offen offset:128 sc1\n"
"SB v[180:183],a[20:23],a[40:43]\n"
"LD a[16:19],v1,38912\n"
"PF v175,v175,s57,s57\n"
"SB v[188:191],a[20:23],a[44:47]\n"
"LD a[20:23],v1,39168\n"
"SB v[176:179],a[24:27],a[48:51]\n"
"RC v167,0\n"
"SB v[184:187],a[24:27],a[52:55]\n"
"LD a[24:27],v1,40960\n"
"RC v175,0\n"
"SB v[180:183],a[28:31],a[48:51]\n"
"RC v167,1\n"
"RC v175,1\n"
"PM v167,v166,v167\n"
"SB v[188:191],a[28:31],a[52:55]\n"
"LD a[28:31],v1,41216\n"
"buffer_load_dword a84, v0, s[24:27], s28 offen\n"
"PM v175,v174,v175\n"
"WL 8\n"
"v_or_b32 v174, v172, v173\n"
"v_or_b32 v173, v170, v171\n"
"v_or_b32 v172, v168, v169\n"
"v_or_b32 v166, v164, v165\n"
"v_or_b32 v165, v162, v163\n"
"SH v[216:219],a[232:235],v[172:175]\n"
"v_or_b32_e64 v164, v160, v161\n"
"GA v176\n"
"GA v177\n"
"SH v[208:211],a[232:235],v[164:167]\n"
"GL v176\n"
"GH v177\n"
"SH v[220:223],a[236:239],v[172:175]\n"
"CH v182,v182,v183\n"
"CH v190,v190,v191\n"
"SH v[212:215],a[236:239],v[164:167]\n"
"PF v183,v182,v182,s56\n"
"GA v178\n"
"SH v[232:235],a[240:243],v[172:175]\n"
"GL v178\n"
"GA v179\n"
"SH v[224:227],a[240:243],v[164:167]\n"
"GH v179\n"
"PF v191,v190,v190,s56\n"
"SH v[236:239],a[244:247],v[172:175]\n"
"PM v183,v183,v182\n"
"PM v191,v191,v190\n"
"SH v[228:231],a[244:247],v[164:167]\n"
"GA v180\n"
"WL 4\n"
"SH v[248:251],a[248:251],v[172:175]\n"
"GL v180\n"
"GA v181\n"
"SH v[240:243],a[248:251],v[164:167]\n"
"GH v181\n"
"SH v[252:255],a[252:255],v[172:175]\n"
"EX v183,0\n"
"SH v[244:247],a[252:255],v[164:167]\n"
"EX v191,0\n"
"s_waitcnt vmcnt(18)\n"
"SH v[24:27],a[232:235],a[196:199]\n"
"GA v184\n"
"GA v185\n"
"SH v[16:19],a[232:235],a[192:195]\n"
"GL v184\n"
"GH v185\n"
"SH v[28:31],a[236:239],a[196:199]\n"
"LD a[232:235],v7,38912\n"
"GA v186\n"
"SH v[20:23],a[236:239],a[192:195]\n"
"LD a[236:239],v7,39168\n"
"GL v186\n"
"SH v[72:75],a[240:243],a[196:199]\n"
"EX v183,1\n"
"v_smfmac_f32_32x32x32_bf16 v[208:223], v[8:11], a[144:151], v12\n"
"GA v187\n"
"EX v191,1\n"
"PF v183,v183,s57,s57\n"
"GA v188\n"
"GA v189\n"
"SH v[64:67],a[240:243],a[192:195]\n"
"GH v187\n"
"LD a[240:243],v7,40960\n"
"SH v[76:79],a[244:247],a[196:199]\n"
"PF v191,v191,s57,s57\n"
"RC v183,0\n"
"SH v[68:71],a[244:247],a[192:195]\n"
"LD a[244:247],v7,41216\n"
"GL v188\n"
"SH v[104:107],a[248:251],a[196:199]\n"
"GH v189\n"
"RC v191,0\n"
"SH v[96:99],a[248:251],a[192:195]\n"
"LD a[248:251],v7,43008\n"
"RC v183,1\n"
"SH v[108:111],a[252:255],a[196:199]\n"
"RC v191,1\n"
"v_smfmac_f32_32x32x32_bf16 v[224:239], v[8:11], a[152:159], v12\n"
"PM v191,v190,v191\n"
"PM v183,v182,v183\n"
"WL 8\n"
"v_or_b32 v182, v180, v181\n"
"SH v[100:103],a[252:255],a[192:195]\n"
"LD a[252:255],v7,43264\n"
"v_or_b32_e64 v181, v178, v179\n"
"SZ v[160:163],a[8:11],a[32:35]\n"
"WL 2\n"
"v_or_b32 v190, v188, v189\n"
"SZ v[168:171],a[8:11],a[36:39]\n"
"LD a[8:11],v1,43008\n"
"v_or_b32_e64 v189, v186, v187\n"
"SZ v[164:167],a[12:15],a[32:35]\n"
"v_or_b32 v188, v184, v185\n"
"v_or_b32 v180, v176, v177\n"
"SZ v[172:175],a[12:15],a[36:39]\n"
"LD a[12:15],v1,43264\n"
"CB v128,v128,v129\n"
"SB v[160:163],a[16:19],a[40:43]\n"
"v_smfmac_f32_32x32x32_bf16 v[240:255], v[8:11], a[160:167], v12\n"
"CB v136,v136,v137\n"
"v_smfmac_f32_32x32x32_bf16 v[16:31], v[8:11], a[168:175], v12\n"
"SB v[168:171],a[16:19],a[44:47]\n"
"LD a[16:19],v1,45056\n"
"SB v[164:167],a[20:23],a[40:43]\n"
"SB v[172:175],a[20:23],a[44:47]\n"
"LD a[20:23],v1,45312\n"
"CB v129,v130,v131\n"
"SB v[160:163],a[24:27],a[48:51]\n"
"SB v[168:171],a[24:27],a[52:55]\n"
"LD a[24:27],v1,47104\n"
"SB v[164:167],a[28:31],a[48:51]\n"
"CB v137,v138,v139\n"
"v_smfmac_f32_32x32x32_bf16 v[64:79], v[8:11], a[176:183], v12\n"
"s_nop 2\n"
"s_nop 0\n"
"GA v160\n"
"SB v[172:175],a[28:31],a[52:55]\n"
"LD a[28:31],v1,47360\n"
"GL v160\n"
"SH v[216:219],a[232:235],v[188:191]\n"
"GA v161\n"
"CH v166,v166,v167\n"
"SH v[208:211],a[232:235],v[180:183]\n"
"GH v161\n"
"PF v167,v166,v166,s56\n"
"SH v[220:223],a[236:239],v[188:191]\n"
"CH v174,v174,v175\n"
"GA v162\n"
"SH v[212:215],a[236:239],v[180:183]\n"
"GL v162\n"
"GA v163\n"
"SH v[232:235],a[240:243],v[188:191]\n"
"GH v163\n"
"PF v175,v174,v174,s56\n"
"v_smfmac_f32_32x32x32_bf16 v[96:111], v[8:11], a[184:191], v12\n"
"PM v167,v167,v166\n"
"PM v175,v175,v174\n"
"CB v130,v132,v133\n"
"GA v164\n"
"GA v165\n"
"CB v138,v140,v141\n"
"SH v[224:227],a[240:243],v[180:183]\n"
"GL v164\n"
"GH v165\n"
"SH v[236:239],a[244:247],v[188:191]\n"
"EX v167,0\n"
"SH v[228:231],a[244:247],v[180:183]\n"
"CB v131,v134,v135\n"
"WL 6\n"
"SH v[248:251],a[248:251],v[188:191]\n"
"EX v175,0\n"
"SH v[240:243],a[248:251],v[180:183]\n"
"GA v168\n"
"SH v[252:255],a[252:255],v[188:191]\n"
"GL v168\n"
"GA v169\n"
"SH v[244:247],a[252:255],v[180:183]\n"
"GH v169\n"
"CB v139,v142,v143\n"
"s_waitcnt vmcnt(16)\n"
"SH v[24:27],a[232:235],a[204:207]\n"
"EX v167,1\n"
"SH v[16:19],a[232:235],a[200:203]\n"
"LD a[232:235],v7,45056\n"
"GA v170\n"
"SH v[28:31],a[236:239],a[204:207]\n"
"GA v171\n"
"EX v175,1\n"
"SH v[20:23],a[236:239],a[200:203]\n"
"LD a[236:239],v7,45312\n"
"GL v170\n"
"SH v[72:75],a[240:243],a[204:207]\n"
"GH v171\n"
"PF v167,v167,s57,s57\n"
"SH v[64:67],a[240:243],a[200:203]\n"
"LD a[240:243],v7,47104\n"
"GA v172\n"
"SH v[76:79],a[244:247],a[204:207]\n"
"GA v173\n"
"PF v175,v175,s57,s57\n"
"SH v[68:71],a[244:247],a[200:203]\n"
"LD a[244:247],v7,47360\n"
"GL v172\n"
"SH v[104:107],a[248:251],a[204:207]\n"
"GH v173\n"
"buffer_store_dwordx4 v[40:43], v4, s[40:43], s92 offen offset:3072 sc1\n"
"SH v[96:99],a[248:251],a[200:203]\n"
"LD a[248:251],v7,49152\n"
"buffer_store_dwordx4 v[88:91], v4, s[40:43], s92 offen offset:3136 sc1\n"
"SH v[108:111],a[252:255],a[204:207]\n"
"buffer_store_dwordx4 v[120:123], v4, s[40:43], s92 offen offset:3200 sc1\n"
"SH v[100:103],a[252:255],a[200:203]\n"
"LD a[252:255],v7,49408\n"
"SZ v[176:179],a[8:11],a[32:35]\n"
"RC v167,0\n"
"SZ v[184:187],a[8:11],a[36:39]\n"
"LD a[8:11],v1,49152\n"
"SZ v[180:183],a[12:15],a[32:35]\n"
"RC v175,0\n"
"SZ v[188:191],a[12:15],a[36:39]\n"
"LD a[12:15],v1,49408\n"
"SB v[176:179],a[16:19],a[40:43]\n"
"CB v144,v144,v145\n"
"CB v152,v152,v153\n"
"SB v[184:187],a[16:19],a[44:47]\n"
"LD a[16:19],v1,51200\n"
"SB v[180:183],a[20:23],a[40:43]\n"
"RC v167,1\n"
"SB v[188:191],a[20:23],a[44:47]\n"
"LD a[20:23],v1,51456\n"
"SB v[176:179],a[24:27],a[48:51]\n"
"CB v145,v146,v147\n"
"CB v153,v154,v155\n"
"SB v[184:187],a[24:27],a[52:55]\n"
"LD a[24:27],v1,53248\n"
"RC v175,1\n"
"SB v[180:183],a[28:31],a[48:51]\n"
"CB v146,v148,v149\n"
"PM v175,v174,v175\n"
"PM v167,v166,v167\n"
"WL 6\n"
"v_or_b32 v174, v172, v173\n"
"SB v[188:191],a[28:31],a[52:55]\n"
"LD a[28:31],v1,53504\n"
"CB v154,v156,v157\n"
"v_or_b32 v173, v170, v171\n"
"v_or_b32 v172, v168, v169\n"
"v_or_b32 v166, v164, v165\n"
"v_or_b32 v165, v162, v163\n"
"SH v[216:219],a[232:235],v[172:175]\n"
"CB v147,v150,v151\n"
"v_or_b32_e64 v164, v160, v161\n"
"CB v155,v158,v159\n"
"GA v176\n"
"SH v[208:211],a[232:235],v[164:167]\n"
"GL v176\n"
"GA v177\n"
"SH v[220:223],a[236:239],v[172:175]\n"
"GH v177\n"
"CH v182,v182,v183\n"
"SH v[212:215],a[236:239],v[164:167]\n"
"CH v190,v190,v191\n"
"PF v183,v182,v182,s56\n"
"SH v[232:235],a[240:243],v[172:175]\n"
"GA v178\n"
"GA v179\n"
"SH v[224:227],a[240:243],v[164:167]\n"
"GL v178\n"
"GH v179\n"
"SH v[236:239],a[244:247],v[172:175]\n"
"PF v191,v190,v190,s56\n"
"PM v183,v183,v182\n"
"SH v[228:231],a[244:247],v[164:167]\n"
"PM v191,v191,v190\n"
"SH v[248:251],a[248:251],v[172:175]\n"
"GA v180\n"
"GA v181\n"
"SH v[240:243],a[248:251],v[164:167]\n"
"GL v180\n"
"WL 5\n"
"SH v[252:255],a[252:255],v[172:175]\n"
"GH v181\n"
"CB v48,v48,v49\n"
"SH v[244:247],a[252:255],v[164:167]\n"
"EX v183,0\n"
"s_waitcnt vmcnt(17)\n"
"SH v[24:27],a[232:235],a[212:215]\n"
"EX v191,0\n"
"SH v[16:19],a[232:235],a[208:211]\n"
"GA v184\n"
"GA v185\n"
"SH v[28:31],a[236:239],a[212:215]\n"
"GL v184\n"
"GH v185\n"
"SH v[20:23],a[236:239],a[208:211]\n"
"LD a[232:235],v7,51200\n"
"LD a[236:239],v7,51456\n"
"SH v[72:75],a[240:243],a[212:215]\n"
"CB v56,v56,v57\n"
"CB v49,v50,v51\n"
"SH v[64:67],a[240:243],a[208:211]\n"
"EX v183,1\n"
"GA v186\n"
"SH v[76:79],a[244:247],a[212:215]\n"
"GL v186\n"
"GA v187\n"
"SH v[68:71],a[244:247],a[208:211]\n"
"GH v187\n"
"LD a[240:243],v7,53248\n"
"SH v[104:107],a[248:251],a[212:215]\n"
"LD a[244:247],v7,53504\n"
"EX v191,1\n"
"CB v57,v58,v59\n"
"SH v[96:99],a[248:251],a[208:211]\n"
"CB v50,v52,v53\n"
"PF v183,v183,s57,s57\n"
"GA v188\n"
"GA v189\n"
"SH v[108:111],a[252:255],a[212:215]\n"
"GL v188\n"
"GH v189\n"
"SH v[100:103],a[252:255],a[208:211]\n"
"LD a[248:251],v7,55296\n"
"SZ v[160:163],a[8:11],a[32:35]\n"
"LD a[252:255],v7,55552\n"
"CB v58,v60,v61\n"
"SZ v[168:171],a[8:11],a[36:39]\n"
"LD a[8:11],v1,55296\n"
"SZ v[164:167],a[12:15],a[32:35]\n"
"PF v191,v191,s57,s57\n"
"CB v51,v54,v55\n"
"SZ v[172:175],a[12:15],a[36:39]\n"
"LD a[12:15],v1,55552\n"
"SB v[160:163],a[16:19],a[40:43]\n"
"RC v183,0\n"
"SB v[168:171],a[16:19],a[44:47]\n"
"LD a[16:19],v1,57344\n"
"SB v[164:167],a[20:23],a[40:43]\n"
"CB v59,v62,v63\n"
"RC v191,0\n"
"SB v[172:175],a[20:23],a[44:47]\n"
"LD a[20:23],v1,57600\n"
"SB v[160:163],a[24:27],a[48:51]\n"
"RC v183,1\n"
"SB v[168:171],a[24:27],a[52:55]\n"
"LD a[24:27],v1,59392\n"
"SB v[164:167],a[28:31],a[48:51]\n"
"RC v191,1\n"
"PM v183,v182,v183\n"
"SB v[172:175],a[28:31],a[52:55]\n"
"LD a[28:31],v1,59648\n"
"buffer_store_dwordx4 v[128:131], v4, s[40:43], s93 offen offset:0 sc1\n"
"buffer_store_dwordx4 v[144:147], v4, s[40:43], s93 offen offset:64 sc1\n"
"buffer_store_dwordx4 v[48:51], v4, s[40:43], s93 offen offset:128 sc1\n"
"PM v191,v190,v191\n"
"WL 6\n"
"v_or_b32 v190, v188, v189\n"
"v_or_b32 v189, v186, v187\n"
"v_or_b32 v188, v184, v185\n"
"v_or_b32 v182, v180, v181\n"
"v_or_b32 v181, v178, v179\n"
"SH v[216:219],a[232:235],v[188:191]\n"
"v_or_b32_e64 v180, v176, v177\n"
"GA v160\n"
"GA v161\n"
"SH v[208:211],a[232:235],v[180:183]\n"
"GL v160\n"
"GH v161\n"
"SH v[220:223],a[236:239],v[188:191]\n"
"CH v166,v166,v167\n"
"CH v174,v174,v175\n"
"SH v[212:215],a[236:239],v[180:183]\n"
"PF v167,v166,v166,s56\n"
"GA v162\n"
"SH v[232:235],a[240:243],v[188:191]\n"
"GL v162\n"
"GA v163\n"
"SH v[224:227],a[240:243],v[180:183]\n"
"GH v163\n"
"PF v175,v174,v174,s56\n"
"SH v[236:239],a[244:247],v[188:191]\n"
"PM v167,v167,v166\n"
"PM v175,v175,v174\n"
"SH v[228:231],a[244:247],v[180:183]\n"
"GA v164\n"
"SH v[248:251],a[248:251],v[188:191]\n"
"GL v164\n"
"GA v165\n"
"SH v[240:243],a[248:251],v[180:183]\n"
"GH v165\n"
"SH v[252:255],a[252:255],v[188:191]\n"
"EX v167,0\n"
"SH v[244:247],a[252:255],v[180:183]\n"
"EX v175,0\n"
"s_waitcnt vmcnt(18)\n"
"s_nop 0\n"
"SH v[24:27],a[232:235],a[220:223]\n"
"GA v168\n"
"GA v169\n"
"SH v[16:19],a[232:235],a[216:219]\n"
"GL v168\n"
"GH v169\n"
"SH v[28:31],a[236:239],a[220:223]\n"
"LD a[232:235],v7,57344\n"
"GA v170\n"
"SH v[20:23],a[236:239],a[216:219]\n"
"LD a[236:239],v7,57600\n"
"GL v170\n"
"SH v[72:75],a[240:243],a[220:223]\n"
"EX v167,1\n"
"SH v[64:67],a[240:243],a[216:219]\n"
"GA v171\n"
"PF v167,v167,s57,s57\n"
"SH v[76:79],a[244:247],a[220:223]\n"
"GH v171\n"
"LD a[240:243],v7,59392\n"
"SH v[68:71],a[244:247],a[216:219]\n"
"LD a[244:247],v7,59648\n"
"GA v172\n"
"SH v[104:107],a[248:251],a[220:223]\n"
"GL v172\n"
"GA v173\n"
"SH v[96:99],a[248:251],a[216:219]\n"
"GH v173\n"
"LD a[248:251],v7,61440\n"
"SH v[108:111],a[252:255],a[220:223]\n"
"EX v175,1\n"
"SH v[100:103],a[252:255],a[216:219]\n"
"LD a[252:255],v7,61696\n"
"WL 12\n"
"s_nop 0\n"
"SZ v[176:179],a[8:11],a[32:35]\n"
"PF v175,v175,s57,s57\n"
"RC v167,0\n"
"SZ v[184:187],a[8:11],a[36:39]\n"
"LD a[8:11],v1,61440\n"
"SZ v[180:183],a[12:15],a[32:35]\n"
"RC v175,0\n"
"SZ v[188:191],a[12:15],a[36:39]\n"
"LD a[12:15],v1,61696\n"
"SB v[176:179],a[16:19],a[40:43]\n"
"RC v167,1\n"
"SB v[184:187],a[16:19],a[44:47]\n"
"LD a[16:19],v1,63488\n"
"SB v[180:183],a[20:23],a[40:43]\n"
"RC v175,1\n"
"SB v[188:191],a[20:23],a[44:47]\n"
"LD a[20:23],v1,63744\n"
"SB v[176:179],a[24:27],a[48:51]\n"
"PM v175,v174,v175\n"
"PM v167,v166,v167\n"
"SB v[184:187],a[24:27],a[52:55]\n"
"LD a[24:27],v2,0\n"
"SB v[180:183],a[28:31],a[48:51]\n"
"WL 7\n"
"v_or_b32 v174, v172, v173\n"
"SB v[188:191],a[28:31],a[52:55]\n"
"LD a[28:31],v2,256\n"
"v_or_b32 v173, v170, v171\n"
"v_or_b32 v172, v168, v169\n"
"v_or_b32 v166, v164, v165\n"
"v_or_b32 v165, v162, v163\n"
"SH v[216:219],a[232:235],v[172:175]\n"
"v_or_b32_e64 v164, v160, v161\n"
"GA v176\n"
"GA v177\n"
"SH v[208:211],a[232:235],v[164:167]\n"
"GL v176\n"
"GH v177\n"
"SH v[220:223],a[236:239],v[172:175]\n"
"CH v182,v182,v183\n"
"CH v190,v190,v191\n"
"SH v[212:215],a[236:239],v[164:167]\n"
"PF v183,v182,v182,s56\n"
"GA v178\n"
"SH v[232:235],a[240:243],v[172:175]\n"
"GL v178\n"
"GA v179\n"
"SH v[224:227],a[240:243],v[164:167]\n"
"GH v179\n"
"PF v191,v190,v190,s56\n"
"SH v[236:239],a[244:247],v[172:175]\n"
"PM v183,v183,v182\n"
"PM v191,v191,v190\n"
"SH v[228:231],a[244:247],v[164:167]\n"
"GA v180\n"
"WL 5\n"
"SH v[248:251],a[248:251],v[172:175]\n"
"GL v180\n"
"GA v181\n"
"SH v[240:243],a[248:251],v[164:167]\n"
"GH v181\n"
"SH v[252:255],a[252:255],v[172:175]\n"
"EX v183,0\n"
"SH v[244:247],a[252:255],v[164:167]\n"
"EX v191,0\n"
"s_waitcnt vmcnt(16)\n"
"SH v[24:27],a[232:235],a[228:231]\n"
"GA v184\n"
"GA v185\n"
"SH v[16:19],a[232:235],a[224:227]\n"
"LD a[232:235],v2,12288\n"
"GA v186\n"
"SH v[28:31],a[236:239],a[228:231]\n"
"EX v183,1\n"
"SH v[20:23],a[236:239],a[224:227]\n"
"LD a[236:239],v2,12544\n"
"GL v184\n"
"SH v[72:75],a[240:243],a[228:231]\n"
"GH v185\n"
"GA v187\n"
"SH v[64:67],a[240:243],a[224:227]\n"
"LD a[240:243],v2,14336\n"
"PF v183,v183,s57,s57\n"
"SH v[76:79],a[244:247],a[228:231]\n"
"EX v191,1\n"
"SH v[68:71],a[244:247],a[224:227]\n"
"LD a[244:247],v2,14592\n"
"GA v188\n"
"SH v[104:107],a[248:251],a[228:231]\n"
"GA v189\n"
"PF v191,v191,s57,s57\n"
"SH v[96:99],a[248:251],a[224:227]\n"
"LD a[248:251],v2,16384\n"
"GL v186\n"
"SH v[108:111],a[252:255],a[228:231]\n"
"GH v187\n"
"RC v183,0\n"
"SH v[100:103],a[252:255],a[224:227]\n"
"LD a[252:255],v2,16640\n"
"s_waitcnt vmcnt(14) lgkmcnt(9)\n"
"SH v[24:27],a[232:235],a[60:63]\n"
"RC v191,0\n"
"SH v[16:19],a[232:235],a[56:59]\n"
"LD a[232:235],v7,63488\n"
"WL 9\n"
"SH v[28:31],a[236:239],a[60:63]\n"
"GL v188\n"
"GH v189\n"
"SH v[20:23],a[236:239],a[56:59]\n"
"LD a[236:239],v7,63744\n"
"WL 9\n"
"SH v[72:75],a[240:243],a[60:63]\n"
"buffer_store_dwordx4 v[136:139], v4, s[40:43], s93 offen offset:3072 sc1\n"
"SH v[64:67],a[240:243],a[56:59]\n"
"LD a[240:243],v2,8192\n"
"WL 9\n"
"SH v[76:79],a[244:247],a[60:63]\n"
"buffer_store_dwordx4 v[152:155], v4, s[40:43], s93 offen offset:3136 sc1\n"
"SH v[68:71],a[244:247],a[56:59]\n"
"LD a[244:247],v2,8448\n"
"WL 9\n"
"SH v[104:107],a[248:251],a[60:63]\n"
"buffer_store_dwordx4 v[56:59], v4, s[40:43], s93 offen offset:3200 sc1\n"
"SH v[96:99],a[248:251],a[56:59]\n"
"LD a[248:251],v2,10240\n"
"WL 7\n"
"SH v[108:111],a[252:255],a[60:63]\n"
"RC v183,1\n"
"SH v[100:103],a[252:255],a[56:59]\n"
"LD a[252:255],v2,10496\n"
"RC v191,1\n"
"SZ v[160:163],a[8:11],a[32:35]\n"
"PM v191,v190,v191\n"
"PM v183,v182,v183\n"
"SZ v[168:171],a[8:11],a[36:39]\n"
"LD a[8:11],v2,2048\n"
"v_or_b32_e64 v182, v180, v181\n"
"SZ v[164:167],a[12:15],a[32:35]\n"
"WL 6\n"
"v_or_b32 v190, v188, v189\n"
"SZ v[172:175],a[12:15],a[36:39]\n"
"LD a[12:15],v2,2304\n"
"v_or_b32_e64 v189, v186, v187\n"
"SB v[160:163],a[16:19],a[40:43]\n"
"v_or_b32 v188, v184, v185\n"
"v_or_b32 v181, v178, v179\n"
"SB v[168:171],a[16:19],a[44:47]\n"
"LD a[16:19],v2,4096\n"
"v_or_b32_e64 v180, v176, v177\n"
"SB v[164:167],a[20:23],a[40:43]\n"
"SB v[172:175],a[20:23],a[44:47]\n"
"LD a[20:23],v2,4352\n"
"SB v[160:163],a[24:27],a[48:51]\n"
"SB v[168:171],a[24:27],a[52:55]\n"
"LD a[24:27],v2,6144\n"
"SB v[164:167],a[28:31],a[48:51]\n"
"SB v[172:175],a[28:31],a[52:55]\n"
"LD a[28:31],v2,6400\n"
"SH v[216:219],a[232:235],v[188:191]\n"
"SH v[208:211],a[232:235],v[180:183]\n"
"s_nop 0\n"
"GA v160\n"
"WL 6\n"
"SH v[220:223],a[236:239],v[188:191]\n"
"GL v160\n"
"GA v161\n"
"SH v[212:215],a[236:239],v[180:183]\n"
"GH v161\n"
"SH v[232:235],a[240:243],v[188:191]\n"
"CH v166,v166,v167\n"
"CH v174,v174,v175\n"
"SH v[224:227],a[240:243],v[180:183]\n"
"PF v167,v166,v166,s56\n"
"SH v[236:239],a[244:247],v[188:191]\n"
"GA v162\n"
"GA v163\n"
"SH v[228:231],a[244:247],v[180:183]\n"
"GL v162\n"
"SH v[248:251],a[248:251],v[188:191]\n"
"GH v163\n"
"PF v175,v174,v174,s56\n"
"SH v[240:243],a[248:251],v[180:183]\n"
"PM v167,v167,v166\n"
"SH v[252:255],a[252:255],v[188:191]\n"
"PM v175,v175,v174\n"
"GA v164\n"
"SH v[244:247],a[252:255],v[180:183]\n"
"GL v164\n"
"GA v165\n"
"s_waitcnt vmcnt(15)\n"
"s_nop 0\n"
"SH v[24:27],a[232:235],a[68:71]\n"
"GH v165\n"
"EX v167,0\n"
"SH v[16:19],a[232:235],a[64:67]\n"
"LD a[232:235],v2,18432\n"
"GA v168\n"
"SH v[28:31],a[236:239],a[68:71]\n"
"EX v175,0\n"
"SH v[20:23],a[236:239],a[64:67]\n"
"LD a[236:239],v2,18688\n"
"GL v168\n"
"SH v[72:75],a[240:243],a[68:71]\n"
"GA v169\n"
"GA v170\n"
"SH v[64:67],a[240:243],a[64:67]\n"
"GH v169\n"
"LD a[240:243],v2,20480\n"
"SH v[76:79],a[244:247],a[68:71]\n"
"EX v167,1\n"
"SH v[68:71],a[244:247],a[64:67]\n"
"LD a[244:247],v2,20736\n"
"GA v171\n"
"SH v[104:107],a[248:251],a[68:71]\n"
"EX v175,1\n"
"SH v[96:99],a[248:251],a[64:67]\n"
"LD a[248:251],v2,22528\n"
"GL v170\n"
"SH v[108:111],a[252:255],a[68:71]\n"
"GH v171\n"
"PF v167,v167,s57,s57\n"
"SH v[100:103],a[252:255],a[64:67]\n"
"LD a[252:255],v2,22784\n"
"s_waitcnt vmcnt(13) lgkmcnt(9)\n"
"SH v[24:27],a[232:235],a[76:79]\n"
"GA v172\n"
"GA v173\n"
"SH v[16:19],a[232:235],a[72:75]\n"
"LD a[232:235],v2,12288\n"
"WL 9\n"
"SH v[28:31],a[236:239],a[76:79]\n"
"GL v172\n"
"GH v173\n"
"SH v[20:23],a[236:239],a[72:75]\n"
"LD a[236:239],v2,12544\n"
"WL 9\n"
"SH v[72:75],a[240:243],a[76:79]\n"
"PF v175,v175,s57,s57\n"
"RC v167,0\n"
"SH v[64:67],a[240:243],a[72:75]\n"
"LD a[240:243],v2,14336\n"
"WL 9\n"
"SH v[76:79],a[244:247],a[76:79]\n"
"RC v175,0\n"
"SH v[68:71],a[244:247],a[72:75]\n"
"LD a[244:247],v2,14592\n"
"WL 9\n"
"SH v[104:107],a[248:251],a[76:79]\n"
"RC v167,1\n"
"SH v[96:99],a[248:251],a[72:75]\n"
"LD a[248:251],v2,16384\n"
"WL 7\n"
"SH v[108:111],a[252:255],a[76:79]\n"
"RC v175,1\n"
"SH v[100:103],a[252:255],a[72:75]\n"
"LD a[252:255],v2,16640\n"
"CB v16,v16,v17\n"
"SZ v[176:179],a[8:11],a[32:35]\n"
"CB v24,v24,v25\n"
"CB v17,v18,v19\n"
"SZ v[184:187],a[8:11],a[36:39]\n"
"CB v25,v26,v27\n"
"CB v18,v20,v21\n"
"SZ v[180:183],a[12:15],a[32:35]\n"
"CB v26,v28,v29\n"
"CB v19,v22,v23\n"
"SZ v[188:191],a[12:15],a[36:39]\n"
"buffer_store_dwordx4 v[16:19], v4, s[40:43], s95 offen offset:0 sc1\n"
"SB v[176:179],a[16:19],a[40:43]\n"
"CB v27,v30,v31\n"
"CB v64,v64,v65\n"
"SB v[184:187],a[16:19],a[44:47]\n"
"buffer_store_dwordx4 v[24:27], v4, s[40:43], s95 offen offset:3072 sc1\n"
"SB v[180:183],a[20:23],a[40:43]\n"
"CB v72,v72,v73\n"
"CB v65,v66,v67\n"
"SB v[188:191],a[20:23],a[44:47]\n"
"CB v73,v74,v75\n"
"CB v66,v68,v69\n"
"SB v[176:179],a[24:27],a[48:51]\n"
"CB v74,v76,v77\n"
"CB v67,v70,v71\n"
"PM v175,v174,v175\n"
"SB v[184:187],a[24:27],a[52:55]\n"
"buffer_store_dwordx4 v[64:67], v4, s[40:43], s95 offen offset:64 sc1\n"
"CB v75,v78,v79\n"
"PM v167,v166,v167\n"
"WL 0\n"
"v_or_b32 v174, v172, v173\n"
"CB v96,v96,v97\n"
"CB v104,v104,v105\n"
"v_or_b32_e64 v173, v170, v171\n"
"CB v97,v98,v99\n"
"CB v105,v106,v107\n"
"SB v[180:183],a[28:31],a[48:51]\n"
"buffer_store_dwordx4 v[72:75], v4, s[40:43], s95 offen offset:3136 sc1\n"
"CB v98,v100,v101\n"
"SB v[188:191],a[28:31],a[52:55]\n"
"v_or_b32_e64 v172, v168, v169\n"
"CB v106,v108,v109\n"
"CB v99,v102,v103\n"
"SH v[216:219],a[232:235],v[172:175]\n"
"buffer_store_dwordx4 v[96:99], v4, s[40:43], s95 offen offset:128 sc1\n"
"v_or_b32_e64 v166, v164, v165\n"
"CB v107,v110,v111\n"
"v_or_b32 v165, v162, v163\n"
"v_or_b32 v164, v160, v161\n"
"GA v176\n"
"GA v177\n"
"SH v[208:211],a[232:235],v[164:167]\n"
"buffer_store_dwordx4 v[104:107], v4, s[40:43], s95 offen offset:3200 sc1\n"
"SH v[220:223],a[236:239],v[172:175]\n"
"LD a[232:235],v2,18432\n"
"GL v176\n"
"SH v[212:215],a[236:239],v[164:167]\n"
"GH v177\n"
"SH v[232:235],a[240:243],v[172:175]\n"
"LD a[236:239],v2,18688\n"
"CH v182,v182,v183\n"
"CH v190,v190,v191\n"
"SH v[224:227],a[240:243],v[164:167]\n"
"PF v183,v182,v182,s56\n"
"GA v178\n"
"GA v179\n"
"SH v[236:239],a[244:247],v[172:175]\n"
"GL v178\n"
"GH v179\n"
"LD a[240:243],v2,20480\n"
"SH v[228:231],a[244:247],v[164:167]\n"
"PF v191,v190,v190,s56\n"
"PM v183,v183,v182\n"
"PM v191,v191,v190\n"
"SH v[248:251],a[248:251],v[172:175]\n"
"GA v180\n"
"GA v181\n"
"EX v183,0\n"
"SH v[240:243],a[248:251],v[164:167]\n"
"GL v180\n"
"GH v181\n"
"LD a[244:247],v2,20736\n"
"EX v191,0\n"
"SH v[252:255],a[252:255],v[172:175]\n"
"GA v184\n"
"GA v185\n"
"SH v[244:247],a[252:255],v[164:167]\n"
"GL v184\n"
"GH v185\n"
"LD a[248:251],v2,22528\n"
"LD a[252:255],v2,22784\n"
"EX v183,1\n"
"GA v186\n"
"GA v187\n"
"EX v191,1\n"
"PF v183,v183,s57,s57\n"
"GA v188\n"
"GA v189\n"
"PF v191,v191,s57,s57\n"
"RC v183,0\n"
"RC v191,0\n"
"RC v183,1\n"
"RC v191,1\n"
"PM v183,v182,v183\n"
"PM v191,v190,v191\n"
"WL 5\n"
"v_or_b32 v182, v180, v181\n"
"v_or_b32 v181, v178, v179\n"
"v_or_b32 v180, v176, v177\n"
"GL v186\n"
"GH v187\n"
"GL v188\n"
"GH v189\n"
"WL 0\n"
"v_or_b32 v190, v188, v189\n"
"v_or_b32 v189, v186, v187\n"
"v_or_b32 v188, v184, v185\n"
"s_nop 1\n"
"SH v[216:219],a[232:235],v[188:191]\n"
"SH v[208:211],a[232:235],v[180:183]\n"
"SH v[220:223],a[236:239],v[188:191]\n"
"SH v[212:215],a[236:239],v[180:183]\n"
"SH v[232:235],a[240:243],v[188:191]\n"
"SH v[224:227],a[240:243],v[180:183]\n"
"SH v[236:239],a[244:247],v[188:191]\n"
"s_nop 1\n"
"CB v216,v216,v217\n"
"SH v[228:231],a[244:247],v[180:183]\n"
"CB v217,v218,v219\n"
"CB v208,v208,v209\n"
"SH v[248:251],a[248:251],v[188:191]\n"
"CB v209,v210,v211\n"
"CB v210,v212,v213\n"
"SH v[240:243],a[248:251],v[180:183]\n"
"CB v218,v220,v221\n"
"CB v211,v214,v215\n"
"SH v[252:255],a[252:255],v[188:191]\n"
"buffer_store_dwordx4 v[208:211], v4, s[40:43], s94 offen offset:0 sc1\n"
"CB v219,v222,v223\n"
"CB v224,v224,v225\n"
"SH v[244:247],a[252:255],v[180:183]\n"
"buffer_store_dwordx4 v[216:219], v4, s[40:43], s94 offen offset:3072 sc1\n"
"CB v232,v232,v233\n"
"CB v225,v226,v227\n"
"CB v233,v234,v235\n"
"CB v226,v228,v229\n"
"CB v234,v236,v237\n"
"CB v227,v230,v231\n"
"CB v235,v238,v239\n"
"CB v240,v240,v241\n"
"CB v248,v248,v249\n"
"CB v241,v242,v243\n"
"CB v249,v250,v251\n"
"CB v242,v244,v245\n"
"CB v250,v252,v253\n"
"CB v243,v246,v247\n"
"CB v251,v254,v255\n"
"buffer_store_dwordx4 v[224:227], v4, s[40:43], s94 offen offset:64 sc1\n"
"buffer_store_dwordx4 v[232:235], v4, s[40:43], s94 offen offset:3136 sc1\n"
"buffer_store_dwordx4 v[240:243], v4, s[40:43], s94 offen offset:128 sc1\n"
"buffer_store_dwordx4 v[248:251], v4, s[40:43], s94 offen offset:3200 sc1\n"
"s_or_b32 s66, s59, s58\n"
"s_cmp_lg_u32 s66, 0\n"
"s_cbranch_scc1 Lne7_%=\n"
"v_mov_b32 v5, s60\n"
"s_mov_b64 exec, 1\n"
"buffer_store_dword v5, v0, s[48:51], 0 offen offset:0 sc1\n"
"s_mov_b64 exec, -1\n"
"Lne7_%=:\n"
"Lcnx_end:\n"
".purgem GA\n"
".purgem GL\n"
".purgem GH\n"
".purgem EX\n"
".purgem RC\n"
".purgem MB\n"
".purgem MZ\n"
".purgem SB\n"
".purgem SZ\n"
".purgem SH\n"
".purgem NR\n"
".purgem LD\n"
".purgem WL\n"
".purgem AW\n"
".purgem AR\n"
".purgem PF\n"
".purgem PM\n"
".purgem CB\n"
".purgem CH\n"
    : "+v"(t0) : "s"(__builtin_amdgcn_kernarg_segment_ptr()), "s"(blockIdx.x) : "v1", "v2", "v3", "v4", "v5", "v6", "v7", "v8", "v9", "v10", "v11", "v12", "v13", "v14", "v15", "v16", "v17", "v18", "v19", "v20", "v21", "v22", "v23", "v24", "v25", "v26", "v27", "v28", "v29", "v30", "v31", "v32", "v33", "v34", "v35", "v36", "v37", "v38", "v39", "v40", "v41", "v42", "v43", "v44", "v45", "v46", "v47", "v48", "v49", "v50", "v51", "v52", "v53", "v54", "v55", "v56", "v57", "v58", "v59", "v60", "v61", "v62", "v63", "v64", "v65", "v66", "v67", "v68", "v69", "v70", "v71", "v72", "v73", "v74", "v75", "v76", "v77", "v78", "v79", "v80", "v81", "v82", "v83", "v84", "v85", "v86", "v87", "v88", "v89", "v90", "v91", "v92", "v93", "v94", "v95", "v96", "v97", "v98", "v99", "v100", "v101", "v102", "v103", "v104", "v105", "v106", "v107", "v108", "v109", "v110", "v111", "v112", "v113", "v114", "v115", "v116", "v117", "v118", "v119", "v120", "v121", "v122", "v123", "v124", "v125", "v126", "v127", "v128", "v129", "v130", "v131", "v132", "v133", "v134", "v135", "v136", "v137", "v138", "v139", "v140", "v141", "v142", "v143", "v144", "v145", "v146", "v147", "v148", "v149", "v150", "v151", "v152", "v153", "v154", "v155", "v156", "v157", "v158", "v159", "v160", "v161", "v162", "v163", "v164", "v165", "v166", "v167", "v168", "v169", "v170", "v171", "v172", "v173", "v174", "v175", "v176", "v177", "v178", "v179", "v180", "v181", "v182", "v183", "v184", "v185", "v186", "v187", "v188", "v189", "v190", "v191", "v192", "v193", "v194", "v195", "v196", "v197", "v198", "v199", "v200", "v201", "v202", "v203", "v204", "v205", "v206", "v207", "v208", "v209", "v210", "v211", "v212", "v213", "v214", "v215", "v216", "v217", "v218", "v219", "v220", "v221", "v222", "v223", "v224", "v225", "v226", "v227", "v228", "v229", "v230", "v231", "v232", "v233", "v234", "v235", "v236", "v237", "v238", "v239", "v240", "v241", "v242", "v243", "v244", "v245", "v246", "v247", "v248", "v249", "v250", "v251", "v252", "v253", "v254", "v255", "a0", "a1", "a2", "a3", "a4", "a5", "a6", "a7", "a8", "a9", "a10", "a11", "a12", "a13", "a14", "a15", "a16", "a17", "a18", "a19", "a20", "a21", "a22", "a23", "a24", "a25", "a26", "a27", "a28", "a29", "a30", "a31", "a32", "a33", "a34", "a35", "a36", "a37", "a38", "a39", "a40", "a41", "a42", "a43", "a44", "a45", "a46", "a47", "a48", "a49", "a50", "a51", "a52", "a53", "a54", "a55", "a56", "a57", "a58", "a59", "a60", "a61", "a62", "a63", "a64", "a65", "a66", "a67", "a68", "a69", "a70", "a71", "a72", "a73", "a74", "a75", "a76", "a77", "a78", "a79", "a80", "a81", "a82", "a83", "a84", "a85", "a86", "a87", "a88", "a89", "a90", "a91", "a92", "a93", "a94", "a95", "a96", "a97", "a98", "a99", "a100", "a101", "a102", "a103", "a104", "a105", "a106", "a107", "a108", "a109", "a110", "a111", "a112", "a113", "a114", "a115", "a116", "a117", "a118", "a119", "a120", "a121", "a122", "a123", "a124", "a125", "a126", "a127", "a128", "a129", "a130", "a131", "a132", "a133", "a134", "a135", "a136", "a137", "a138", "a139", "a140", "a141", "a142", "a143", "a144", "a145", "a146", "a147", "a148", "a149", "a150", "a151", "a152", "a153", "a154", "a155", "a156", "a157", "a158", "a159", "a160", "a161", "a162", "a163", "a164", "a165", "a166", "a167", "a168", "a169", "a170", "a171", "a172", "a173", "a174", "a175", "a176", "a177", "a178", "a179", "a180", "a181", "a182", "a183", "a184", "a185", "a186", "a187", "a188", "a189", "a190", "a191", "a192", "a193", "a194", "a195", "a196", "a197", "a198", "a199", "a200", "a201", "a202", "a203", "a204", "a205", "a206", "a207", "a208", "a209", "a210", "a211", "a212", "a213", "a214", "a215", "a216", "a217", "a218", "a219", "a220", "a221", "a222", "a223", "a224", "a225", "a226", "a227", "a228", "a229", "a230", "a231", "a232", "a233", "a234", "a235", "a236", "a237", "a238", "a239", "a240", "a241", "a242", "a243", "a244", "a245", "a246", "a247", "a248", "a249", "a250", "a251", "a252", "a253", "a254", "a255", "s4", "s5", "s6", "s7", "s8", "s9", "s10", "s11", "s12", "s13", "s14", "s15", "s16", "s17", "s18", "s19", "s20", "s21", "s22", "s23", "s24", "s25", "s26", "s27", "s28", "s29", "s30", "s31", "s34", "s35", "s36", "s37", "s38", "s39", "s40", "s41", "s42", "s43", "s44", "s45", "s46", "s47", "s48", "s49", "s50", "s51", "s52", "s53", "s54", "s55", "s56", "s57", "s58", "s59", "s60", "s61", "s62", "s63", "s64", "s65", "s66", "s67", "s68", "s69", "s70", "s71", "s72", "s73", "s74", "s75", "s76", "s77", "s78", "s79", "s80", "s81", "s82", "s83", "s84", "s85", "s86", "s87", "s88", "s89", "s90", "s91", "s92", "s93", "s94", "s95", "vcc", "scc", "memory");
}

extern "C" void kernel_launch(void* const* d_in, const int* in_sizes, int n_in, void* d_out, int out_size, void* d_ws, size_t ws_size, hipStream_t stream) {
  (void)hipFuncSetAttribute((const void*)k_main, hipFuncAttributeMaxDynamicSharedMemorySize, 163840);
  k_main<<<256, 256, 163840, stream>>>(d_in[0], d_out, d_ws, d_in[3], d_in[4], d_in[2], d_in[1], d_in[5]);
}
```
